```python
import torch

def _softmax(x, axis=-1):
    return torch.softmax(x, dim=axis)
def _gelu(x):
    return torch.nn.functional.gelu(x, approximate="tanh")
def _silu(x):
    return torch.nn.functional.silu(x)
def _sigmoid(x):
    return torch.sigmoid(x)
def _layernorm(x, axis=-1, eps=1e-5):
    m = x.mean(dim=axis, keepdim=True)
    v = x.var(dim=axis, keepdim=True, unbiased=False)
    return (x - m) / torch.sqrt(v + eps)
def _scatter_add(tgt, idx, src, axis=0):
    return tgt.clone().index_add_(axis, idx, src)
def _segment_sum(src, seg, n):
    out = torch.zeros((n,) + src.shape[1:], dtype=src.dtype)
    return out.index_add_(0, seg, src)
def _pad(x, pw):
    flat = tuple(v for pair in reversed(pw) for v in pair)
    return torch.nn.functional.pad(x, flat)

kernel_name = 'concat_heads__gqa_block_cached__mha_block_cached__near_ridge'

def setup_inputs():

    _w = (lambda a: (lambda t: t.to(torch.bfloat16) if t.is_floating_point() else t)(torch.as_tensor(a)))
    def _A_setup():
        B, Sq, D = 8, 144, 448
        Hq, Hkv, Sk, Dh = 16, 8, 1024, 256
        assert Hq % Hkv == 0, (Hq, Hkv)
        return {
            "x": _w((lambda *s: torch.randn(*s, dtype=torch.bfloat16) * 0.8)(B, Sq, D)),
            "k_cache": _w((lambda *s: torch.randn(*s, dtype=torch.bfloat16) * 0.8)(B, Hkv, Sk, Dh)),
            "v_cache": _w((lambda *s: torch.randn(*s, dtype=torch.bfloat16) * 0.05)(B, Hkv, Sk, Dh)),
            "wq": _w((lambda *s: torch.randn(*s, dtype=torch.bfloat16) * 0.8)(D, Hq * Dh) * (D ** -0.5)),
            "wo": _w((lambda *s: torch.randn(*s, dtype=torch.bfloat16) * 0.05)(Hq * Dh, D) * ((Hq * Dh) ** -0.5)),
        }
    _A_in = _A_setup()

    def _B_setup():
        B, Sq, D = 8, 144, 448
        H, Sk, Dh = 16, 1024, 256
        return {
            "x": _w((lambda *s: torch.randn(*s, dtype=torch.bfloat16) * 0.8)(B, Sq, D)),
            "k_cache": _w((lambda *s: torch.randn(*s, dtype=torch.bfloat16) * 0.8)(B, H, Sk, Dh)),
            "v_cache": _w((lambda *s: torch.randn(*s, dtype=torch.bfloat16) * 0.05)(B, H, Sk, Dh)),
            "wq": _w((lambda *s: torch.randn(*s, dtype=torch.bfloat16) * 0.8)(D, H * Dh) * (D ** -0.5)),
            "wo": _w((lambda *s: torch.randn(*s, dtype=torch.bfloat16) * 0.05)(H * Dh, D) * ((H * Dh) ** -0.5)),
        }
    _B_in = _B_setup()

    return {
        "A_x": _A_in["x"],
        "A_k_cache": _A_in["k_cache"],
        "A_v_cache": _A_in["v_cache"],
        "A_wq": _A_in["wq"],
        "A_wo": _A_in["wo"],
        "B_x": _B_in["x"],
        "B_k_cache": _B_in["k_cache"],
        "B_v_cache": _B_in["v_cache"],
        "B_wq": _B_in["wq"],
        "B_wo": _B_in["wo"],
    }

def reference(A_x, A_k_cache, A_v_cache, A_wq, A_wo, B_x, B_k_cache, B_v_cache, B_wq, B_wo):
    def _A_ref(x, k_cache, v_cache, wq, wo):
        B, Sq, D = x.shape
        Hq, Dh = 16, 256
        Hkv = k_cache.shape[1]
        rep = Hq // Hkv
        q = (lambda x, *d: x.permute(*d))((x @ wq).reshape(B, Sq, Hq, Dh), 0, 2, 1, 3)
        k = (lambda x, n, axis: torch.repeat_interleave(x, n, dim=axis))(k_cache, rep, axis=1)
        v = (lambda x, n, axis: torch.repeat_interleave(x, n, dim=axis))(v_cache, rep, axis=1)
        scores = torch.einsum("bhqd,bhkd->bhqk", q, k) * (Dh ** -0.5)
        p = _softmax(scores, axis=-1)
        o = torch.einsum("bhqk,bhkd->bhqd", p, v)
        return (lambda x, *d: x.permute(*d))(o, 0, 2, 1, 3).reshape(B, Sq, Hq * Dh) @ wo

    def _B_ref(x, k_cache, v_cache, wq, wo):
        B, Sq, D = x.shape
        H, Dh = 16, 256
        q = (lambda x, *d: x.permute(*d))((x @ wq).reshape(B, Sq, H, Dh), 0, 2, 1, 3)
        scores = torch.einsum("bhqd,bhkd->bhqk", q, k_cache) * (Dh ** -0.5)
        p = _softmax(scores, axis=-1)
        o = torch.einsum("bhqk,bhkd->bhqd", p, v_cache)
        return (lambda x, *d: x.permute(*d))(o, 0, 2, 1, 3).reshape(B, Sq, H * Dh) @ wo

    a = _A_ref(A_x, A_k_cache, A_v_cache, A_wq, A_wo)
    b = _B_ref(B_x, B_k_cache, B_v_cache, B_wq, B_wo)
    return torch.concatenate([a, b], axis=-1)
```

```cpp
#include <hip/hip_runtime.h>
#include <hip/hip_bf16.h>
typedef unsigned short u16;
__device__ __forceinline__ float bf2f(u16 v){ return __uint_as_float(((unsigned)v)<<16); }
__device__ __forceinline__ u16 f2bf(float f){ unsigned u=__float_as_uint(f); u += 0x7fffu + ((u>>16)&1u); return (u16)(u>>16); }

__global__ void gemm_naive(const u16* A, const u16* B, u16* C, int M, int N, int K, int ldc){
  __shared__ float As[16][17], Bs[16][17];
  int tx=threadIdx.x&15, ty=threadIdx.x>>4;
  int row=blockIdx.y*16+ty, col=blockIdx.x*16+tx;
  float acc=0;
  for(int k0=0;k0<K;k0+=16){
    As[ty][tx]=bf2f(A[(size_t)row*K+k0+tx]);
    Bs[ty][tx]=bf2f(B[(size_t)(k0+ty)*N+col]);
    __syncthreads();
    for(int k=0;k<16;k++) acc+=As[ty][k]*Bs[k][tx];
    __syncthreads();
  }
  C[(size_t)row*ldc+col]=f2bf(acc);
}
__global__ void attn_naive(const u16* Q, const u16* Kc, const u16* Vc, u16* O, int Hkv){
  int q=blockIdx.x, h=blockIdx.y, b=blockIdx.z;
  int hk = h/(16/Hkv);
  __shared__ float qs[256], s[1024], red[256];
  int t=threadIdx.x;
  qs[t]=bf2f(Q[((size_t)(b*144+q))*4096+h*256+t]);
  __syncthreads();
  const u16* Kp=Kc+((size_t)(b*Hkv+hk))*1024*256;
  const u16* Vp=Vc+((size_t)(b*Hkv+hk))*1024*256;
  float mx=-1e30f;
  for(int j=t;j<1024;j+=256){
    float a=0;
    for(int d=0;d<256;d++) a+=qs[d]*bf2f(Kp[(size_t)j*256+d]);
    a*=0.0625f; s[j]=a; mx=fmaxf(mx,a);
  }
  red[t]=mx; __syncthreads();
  for(int o=128;o>0;o>>=1){ if(t<o) red[t]=fmaxf(red[t],red[t+o]); __syncthreads(); }
  mx=red[0]; __syncthreads();
  float sum=0;
  for(int j=t;j<1024;j+=256){ float e=__expf(s[j]-mx); s[j]=e; sum+=e; }
  red[t]=sum; __syncthreads();
  for(int o=128;o>0;o>>=1){ if(t<o) red[t]+=red[t+o]; __syncthreads(); }
  sum=red[0];
  float a=0;
  for(int j=0;j<1024;j++) a+=s[j]*bf2f(Vp[(size_t)j*256+t]);
  O[((size_t)(b*144+q))*4096+h*256+t]=f2bf(a/sum);
}
extern "C" void kernel_launch(void* const* d_in, const int* in_sizes, int n_in,
                              void* d_out, int out_size, void* d_ws, size_t ws_size,
                              hipStream_t stream) {
  u16* ws=(u16*)d_ws;
  for(int br=0;br<2;br++){
    const u16* x=(const u16*)d_in[br*5+0]; const u16* k=(const u16*)d_in[br*5+1]; const u16* v=(const u16*)d_in[br*5+2];
    const u16* wq=(const u16*)d_in[br*5+3]; const u16* wo=(const u16*)d_in[br*5+4];
    u16* Q=ws+(size_t)br*2*1152*4096; u16* O=Q+(size_t)1152*4096;
    gemm_naive<<<dim3(4096/16,1152/16),256,0,stream>>>(x,wq,Q,1152,4096,448,4096);
    attn_naive<<<dim3(144,16,8),256,0,stream>>>(Q,k,v,O,br==0?8:16);
    gemm_naive<<<dim3(448/16,1152/16),256,0,stream>>>(O,wo,(u16*)d_out+br*448,1152,448,4096,896);
  }
}
```

```cpp
#include <hip/hip_runtime.h>
#include <hip/hip_bf16.h>
typedef unsigned long long u64;
constexpr unsigned PART_BYTES = 129024;

__global__ void __launch_bounds__(256, 1) fused_attn_kernel(const char* xA, const char* kA, const char* vA, const char* wqA, const char* woA,
                                                            const char* xB, const char* kB, const char* vB, const char* wqB, const char* woB, char* part, char* out) {
  __shared__ __attribute__((aligned(16))) char lds[163840];

  const unsigned bid = blockIdx.x, xcd = bid & 7u, slot = bid >> 3;
  const bool isA = slot < 16u;
  const unsigned b = (slot & 15u) >> 1, h = 2u * xcd + (slot & 1u);
  const size_t kvoff = (size_t)(isA ? b * 8u + xcd : b * 16u + h) * 524288u;
  const unsigned rot = 0u;
  const char* xp = (isA ? xA : xB) + (size_t)b * 129024u;
  const char* kp = (isA ? kA : kB) + kvoff;
  const char* vp = (isA ? vA : vB) + kvoff;
  const char* wq = (isA ? wqA : wqB) + h * 512u;
  const char* wo = (isA ? woA : woB) + (size_t)h * 229376u;
  char* gp = part + (size_t)(slot >> 1) * 16u * PART_BYTES;
  char* pp = gp + h * PART_BYTES;
  char* fp = part + (size_t)256u * PART_BYTES + (slot >> 1) * 16384u;
  char* op = out + (size_t)b * 144u * 1792u + (isA ? 0u : 896u);
  asm volatile(
    "START_%=:\n"
    "v_and_b32 v1, 63, %[tid]\n"
    "v_lshrrev_b32 v2, 6, %[tid]\n"
    "s_nop 0\n"
    "v_readfirstlane_b32 s56, v2\n"
    "s_nop 3\n"
    "s_cmp_eq_u32 s56, 3\n"
    "s_cbranch_scc1 LD0_%=\n"
    "v_lshrrev_b32 v19, 4, v1\n"
    "v_and_b32 v20, 15, v1\n"
    "v_bfe_u32 v21, v1, 2, 2\n"
    "v_and_b32 v22, 3, v1\n"
    "s_mov_b64 s[52:53], %[xp]\n"
    "s_and_b32 s53, s53, 0xffff\n"
    "s_mov_b32 s54, 129024\n"
    "s_mov_b32 s55, 0x20000\n"
    "s_mul_i32 s64, s56, 43008\n"
    "v_lshrrev_b32 v15, 3, v1\n"
    "v_mul_u32_u24 v15, 896, v15\n"
    "v_and_b32 v16, 7, v1\n"
    "v_lshlrev_b32 v17, 1, v19\n"
    "v_xor_b32 v16, v16, v17\n"
    "v_lshl_add_u32 v15, v16, 4, v15\n"
    "v_add_u32 v10, s64, v15\n"
    "v_add_u32 v249, 6144, v10\n"
    "s_lshl_b32 s66, s56, 14\n"
    "s_add_u32 s69, s66, 114688\n"
    "s_mul_i32 s36, %[rot], 128\n"
    "s_add_u32 s67, s36, 0\n"
    "s_add_u32 m0, s69, 0\n"
    "s_nop 0\n"
    "buffer_load_dwordx4 v10, s[52:55], s67 offen offset:0  lds\n"
    "buffer_load_dwordx4 v249, s[52:55], s67 offen offset:1024  lds\n"
    "s_add_u32 s67, s36, 14336\n"
    "s_add_u32 m0, s69, 2048\n"
    "s_nop 0\n"
    "buffer_load_dwordx4 v10, s[52:55], s67 offen offset:0  lds\n"
    "buffer_load_dwordx4 v249, s[52:55], s67 offen offset:1024  lds\n"
    "s_add_u32 s67, s36, 28672\n"
    "s_add_u32 m0, s69, 4096\n"
    "s_nop 0\n"
    "buffer_load_dwordx4 v10, s[52:55], s67 offen offset:0  lds\n"
    "buffer_load_dwordx4 v249, s[52:55], s67 offen offset:1024  lds\n"
    "s_branch CSET_%=\n"
    "LD0_%=:\n"
    "s_mov_b64 s[44:45], %[wq]\n"
    "s_and_b32 s45, s45, 0xffff\n"
    "s_mov_b32 s46, 3662336\n"
    "s_mov_b32 s47, 0x20000\n"
    "v_lshrrev_b32 v4, 3, v1\n"
    "v_and_b32 v2, 7, v1\n"
    "v_bfe_u32 v3, v1, 4, 1\n"
    "v_lshlrev_b32 v3, 2, v3\n"
    "v_xor_b32 v6, v2, v3\n"
    "v_lshlrev_b32 v2, 13, v4\n"
    "v_lshl_add_u32 v2, v6, 4, v2\n"
    "v_xor_b32 v3, 0, v2\n"
    "v_add_u32 v48, 0, v2\n"
    "v_add_u32 v49, 64512, v3\n"
    "v_add_u32 v50, 129024, v2\n"
    "v_add_u32 v51, 193536, v3\n"
    "v_add_u32 v52, 262144, v2\n"
    "v_add_u32 v53, 326656, v3\n"
    "v_add_u32 v54, 391168, v2\n"
    "v_add_u32 v55, 455680, v3\n"
    "s_mul_i32 s57, %[rot], 524288\n"
    "s_add_u32 s65, s57, 0\n"
    "s_mov_b32 m0, 0\n"
    "s_nop 0\n"
    "buffer_load_dwordx4 v48, s[44:47], s65 offen offset:0 nt lds\n"
    "buffer_load_dwordx4 v49, s[44:47], s65 offen offset:1024 nt lds\n"
    "buffer_load_dwordx4 v50, s[44:47], s65 offen offset:2048 nt lds\n"
    "buffer_load_dwordx4 v51, s[44:47], s65 offen offset:3072 nt lds\n"
    "s_mov_b32 m0, 4096\n"
    "s_nop 0\n"
    "buffer_load_dwordx4 v52, s[44:47], s65 offen offset:0 nt lds\n"
    "buffer_load_dwordx4 v53, s[44:47], s65 offen offset:1024 nt lds\n"
    "buffer_load_dwordx4 v54, s[44:47], s65 offen offset:2048 nt lds\n"
    "buffer_load_dwordx4 v55, s[44:47], s65 offen offset:3072 nt lds\n"
    "s_add_u32 s65, s57, 128\n"
    "s_mov_b32 m0, 8192\n"
    "s_nop 0\n"
    "buffer_load_dwordx4 v48, s[44:47], s65 offen offset:0 nt lds\n"
    "buffer_load_dwordx4 v49, s[44:47], s65 offen offset:1024 nt lds\n"
    "buffer_load_dwordx4 v50, s[44:47], s65 offen offset:2048 nt lds\n"
    "buffer_load_dwordx4 v51, s[44:47], s65 offen offset:3072 nt lds\n"
    "s_mov_b32 m0, 12288\n"
    "s_nop 0\n"
    "buffer_load_dwordx4 v52, s[44:47], s65 offen offset:0 nt lds\n"
    "buffer_load_dwordx4 v53, s[44:47], s65 offen offset:1024 nt lds\n"
    "buffer_load_dwordx4 v54, s[44:47], s65 offen offset:2048 nt lds\n"
    "buffer_load_dwordx4 v55, s[44:47], s65 offen offset:3072 nt lds\n"
    "s_add_u32 s58, s57, 524288\n"
    "s_cmp_eq_u32 s58, 3670016\n"
    "s_cselect_b32 s58, 0, s58\n"
    "s_add_u32 s59, s58, 524288\n"
    "s_cmp_eq_u32 s59, 3670016\n"
    "s_cselect_b32 s59, 0, s59\n"
    "s_add_u32 s60, s59, 524288\n"
    "s_cmp_eq_u32 s60, 3670016\n"
    "s_cselect_b32 s60, 0, s60\n"
    "s_add_u32 s61, s60, 524288\n"
    "s_cmp_eq_u32 s61, 3670016\n"
    "s_cselect_b32 s61, 0, s61\n"
    "s_add_u32 s62, s61, 524288\n"
    "s_cmp_eq_u32 s62, 3670016\n"
    "s_cselect_b32 s62, 0, s62\n"
    "s_add_u32 s63, s62, 524288\n"
    "s_cmp_eq_u32 s63, 3670016\n"
    "s_cselect_b32 s63, 0, s63\n"
    "s_add_u32 s65, s57, 256\n"
    "s_mov_b32 m0, 16384\n"
    "s_nop 0\n"
    "buffer_load_dwordx4 v48, s[44:47], s65 offen offset:0 nt lds\n"
    "buffer_load_dwordx4 v49, s[44:47], s65 offen offset:1024 nt lds\n"
    "buffer_load_dwordx4 v50, s[44:47], s65 offen offset:2048 nt lds\n"
    "buffer_load_dwordx4 v51, s[44:47], s65 offen offset:3072 nt lds\n"
    "s_mov_b32 m0, 20480\n"
    "s_nop 0\n"
    "buffer_load_dwordx4 v52, s[44:47], s65 offen offset:0 nt lds\n"
    "buffer_load_dwordx4 v53, s[44:47], s65 offen offset:1024 nt lds\n"
    "buffer_load_dwordx4 v54, s[44:47], s65 offen offset:2048 nt lds\n"
    "buffer_load_dwordx4 v55, s[44:47], s65 offen offset:3072 nt lds\n"
    "s_add_u32 s65, s58, 0\n"
    "s_mov_b32 m0, 24576\n"
    "s_nop 0\n"
    "buffer_load_dwordx4 v48, s[44:47], s65 offen offset:0 nt lds\n"
    "buffer_load_dwordx4 v49, s[44:47], s65 offen offset:1024 nt lds\n"
    "buffer_load_dwordx4 v50, s[44:47], s65 offen offset:2048 nt lds\n"
    "buffer_load_dwordx4 v51, s[44:47], s65 offen offset:3072 nt lds\n"
    "s_mov_b32 m0, 28672\n"
    "s_nop 0\n"
    "buffer_load_dwordx4 v52, s[44:47], s65 offen offset:0 nt lds\n"
    "buffer_load_dwordx4 v53, s[44:47], s65 offen offset:1024 nt lds\n"
    "buffer_load_dwordx4 v54, s[44:47], s65 offen offset:2048 nt lds\n"
    "buffer_load_dwordx4 v55, s[44:47], s65 offen offset:3072 nt lds\n"
    "s_getpc_b64 s[70:71]\n"
    "PCREF_%=:\n"
    "s_mul_i32 s64, %[slot], 3072\n"
    "v_lshlrev_b32 v7, 6, v1\n"
    "v_add_u32 v7, s64, v7\n"
    "s_mov_b32 s64, END_%=-PCREF_%=\n"
    "v_min_u32 v7, s64, v7\n"
    "v_and_b32 v7, -4, v7\n"
    "global_load_dword v8, v7, s[70:71] \n"
    "s_branch LDM_%=\n"
    "CSET_%=:\n"
    "s_add_u32 s37, s36, 128\n"
    "s_cmp_eq_u32 s37, 896\n"
    "s_cselect_b32 s37, 0, s37\n"
    "s_add_u32 s38, s37, 128\n"
    "s_cmp_eq_u32 s38, 896\n"
    "s_cselect_b32 s38, 0, s38\n"
    "s_add_u32 s39, s38, 128\n"
    "s_cmp_eq_u32 s39, 896\n"
    "s_cselect_b32 s39, 0, s39\n"
    "s_add_u32 s40, s39, 128\n"
    "s_cmp_eq_u32 s40, 896\n"
    "s_cselect_b32 s40, 0, s40\n"
    "s_add_u32 s41, s40, 128\n"
    "s_cmp_eq_u32 s41, 896\n"
    "s_cselect_b32 s41, 0, s41\n"
    "s_add_u32 s42, s41, 128\n"
    "s_cmp_eq_u32 s42, 896\n"
    "s_cselect_b32 s42, 0, s42\n"
    "v_lshlrev_b32 v15, 7, v20\n"
    "v_add_u32 v15, s69, v15\n"
    "v_bfe_u32 v16, v20, 1, 2\n"
    "v_lshlrev_b32 v16, 1, v16\n"
    "v_or_b32 v17, 0, v19\n"
    "v_xor_b32 v17, v17, v16\n"
    "v_lshl_add_u32 v2, v17, 4, v15\n"
    "v_or_b32 v17, 4, v19\n"
    "v_xor_b32 v17, v17, v16\n"
    "v_lshl_add_u32 v3, v17, 4, v15\n"
    "v_lshl_add_u32 v15, v19, 2, v21\n"
    "v_lshlrev_b32 v15, 7, v15\n"
    "v_lshl_add_u32 v15, v22, 3, v15\n"
    "v_add_u32 v15, 147456, v15\n"
    "v_and_b32 v16, 1, v19\n"
    "v_lshlrev_b32 v16, 1, v16\n"
    "v_lshrrev_b32 v18, 1, v21\n"
    "v_or_b32 v16, v16, v18\n"
    "v_xor_b32 v17, 0, v16\n"
    "v_lshl_add_u32 v4, v17, 5, v15\n"
    "v_xor_b32 v17, 1, v16\n"
    "v_lshl_add_u32 v5, v17, 5, v15\n"
    "v_xor_b32 v17, 2, v16\n"
    "v_lshl_add_u32 v6, v17, 5, v15\n"
    "v_xor_b32 v17, 3, v16\n"
    "v_lshl_add_u32 v7, v17, 5, v15\n"
    "v_lshl_add_u32 v15, v19, 3, v21\n"
    "v_lshlrev_b32 v15, 7, v15\n"
    "v_lshrrev_b32 v16, 1, v21\n"
    "v_lshlrev_b32 v16, 2, v16\n"
    "v_or_b32 v17, 0, v22\n"
    "v_xor_b32 v17, v17, v16\n"
    "v_lshl_add_u32 v4, v17, 4, v15\n"
    "v_or_b32 v17, 4, v22\n"
    "v_xor_b32 v17, v17, v16\n"
    "v_lshl_add_u32 v5, v17, 4, v15\n"
    "s_add_u32 s67, s37, 0\n"
    "s_add_u32 m0, s69, 6144\n"
    "s_nop 0\n"
    "buffer_load_dwordx4 v10, s[52:55], s67 offen offset:0  lds\n"
    "buffer_load_dwordx4 v249, s[52:55], s67 offen offset:1024  lds\n"
    "s_add_u32 s67, s37, 14336\n"
    "s_add_u32 m0, s69, 8192\n"
    "s_nop 0\n"
    "buffer_load_dwordx4 v10, s[52:55], s67 offen offset:0  lds\n"
    "buffer_load_dwordx4 v249, s[52:55], s67 offen offset:1024  lds\n"
    "s_add_u32 s67, s37, 28672\n"
    "s_add_u32 m0, s69, 10240\n"
    "s_nop 0\n"
    "buffer_load_dwordx4 v10, s[52:55], s67 offen offset:0  lds\n"
    "buffer_load_dwordx4 v249, s[52:55], s67 offen offset:1024  lds\n"
    "s_add_u32 s67, s38, 0\n"
    "s_add_u32 m0, s69, 12288\n"
    "s_nop 0\n"
    "buffer_load_dwordx4 v10, s[52:55], s67 offen offset:0  lds\n"
    "buffer_load_dwordx4 v249, s[52:55], s67 offen offset:1024  lds\n"
    "s_add_u32 s67, s38, 14336\n"
    "s_add_u32 m0, s69, 14336\n"
    "s_nop 0\n"
    "buffer_load_dwordx4 v10, s[52:55], s67 offen offset:0  lds\n"
    "buffer_load_dwordx4 v249, s[52:55], s67 offen offset:1024  lds\n"
    "s_mov_b64 s[28:29], %[fp]\n"
    "s_and_b32 s29, s29, 0xffff\n"
    "s_mov_b32 s30, 8320\n"
    "s_mov_b32 s31, 0x20000\n"
    "s_mul_i32 s64, s56, 6144\n"
    "v_lshlrev_b32 v15, 4, v1\n"
    "v_add_u32 v11, s64, v15\n"
    "s_mov_b64 s[96:97], %[pp]\n"
    "s_and_b32 s97, s97, 0xffff\n"
    "s_mov_b32 s98, 129024\n"
    "s_mov_b32 s99, 0x20000\n"
    "s_lshl_b32 s64, s56, 4\n"
    "s_add_u32 s64, s64, %[head]\n"
    "s_lshl_b32 s64, s64, 3\n"
    "v_lshlrev_b32 v15, 20, v1\n"
    "v_add_u32 v23, s64, v15\n"
    "v_mov_b32 v9, 0\n"
    "v_mov_b32 v252, 0\n"
    "v_mov_b32 v253, 0\n"
    "s_mov_b32 s68, 0x3db8aa3b\n"
    "s_mov_b32 s59, 32768\n"
    "s_mov_b32 s60, 0xfffe0000\n"
    "s_mov_b32 s61, 0\n"
    "s_mov_b32 s58, 0\n"
    "s_waitcnt vmcnt(14)\n"
    "ds_read_b128 a[0:3], v2 offset:0\n"
    "s_waitcnt vmcnt(12)\n"
    "ds_read_b128 a[4:7], v2 offset:2048\n"
    "s_waitcnt vmcnt(10)\n"
    "ds_read_b128 a[8:11], v2 offset:4096\n"
    "ds_read_b128 a[12:15], v3 offset:0\n"
    "ds_read_b128 a[16:19], v3 offset:2048\n"
    "ds_read_b128 a[20:23], v3 offset:4096\n"
    "s_barrier\n"
    "ds_read_b64_tr_b16 v[216:217], v4 offset:0\n"
    "ds_read_b64_tr_b16 v[218:219], v4 offset:512\n"
    "ds_read_b64_tr_b16 v[220:221], v4 offset:8\n"
    "ds_read_b64_tr_b16 v[222:223], v4 offset:520\n"
    "ds_read_b64_tr_b16 v[224:225], v5 offset:0\n"
    "ds_read_b64_tr_b16 v[226:227], v5 offset:512\n"
    "ds_read_b64_tr_b16 v[228:229], v5 offset:8\n"
    "ds_read_b64_tr_b16 v[230:231], v5 offset:520\n"
    "s_waitcnt lgkmcnt(6)\n"
    "v_mfma_f32_16x16x32_bf16 v[120:123], v[216:219], a[0:3], 0\n"
    "s_add_u32 s67, s38, 28672\n"
    "ds_read_b64_tr_b16 v[232:233], v4 offset:4096\n"
    "v_mfma_f32_16x16x32_bf16 v[124:127], v[216:219], a[4:7], 0\n"
    "ds_read_b64_tr_b16 v[234:235], v4 offset:4608\n"
    "v_mfma_f32_16x16x32_bf16 v[128:131], v[216:219], a[8:11], 0\n"
    "s_waitcnt lgkmcnt(6)\n"
    "v_mfma_f32_16x16x32_bf16 v[140:143], v[220:223], a[8:11], 0\n"
    "ds_read_b64_tr_b16 v[236:237], v4 offset:4104\n"
    "v_mfma_f32_16x16x32_bf16 v[136:139], v[220:223], a[4:7], 0\n"
    "ds_read_b64_tr_b16 v[238:239], v4 offset:4616\n"
    "v_mfma_f32_16x16x32_bf16 v[132:135], v[220:223], a[0:3], 0\n"
    "s_waitcnt lgkmcnt(6)\n"
    "v_mfma_f32_16x16x32_bf16 v[144:147], v[224:227], a[0:3], 0\n"
    "ds_read_b64_tr_b16 v[240:241], v5 offset:4096\n"
    "v_mfma_f32_16x16x32_bf16 v[148:151], v[224:227], a[4:7], 0\n"
    "ds_read_b64_tr_b16 v[242:243], v5 offset:4608\n"
    "v_mfma_f32_16x16x32_bf16 v[152:155], v[224:227], a[8:11], 0\n"
    "s_waitcnt lgkmcnt(6)\n"
    "v_mfma_f32_16x16x32_bf16 v[164:167], v[228:231], a[8:11], 0\n"
    "ds_read_b64_tr_b16 v[244:245], v5 offset:4104\n"
    "v_mfma_f32_16x16x32_bf16 v[160:163], v[228:231], a[4:7], 0\n"
    "ds_read_b64_tr_b16 v[246:247], v5 offset:4616\n"
    "v_mfma_f32_16x16x32_bf16 v[156:159], v[228:231], a[0:3], 0\n"
    "s_waitcnt lgkmcnt(6)\n"
    "v_mfma_f32_16x16x32_bf16 v[120:123], v[232:235], a[12:15], v[120:123]\n"
    "ds_read_b64_tr_b16 v[216:217], v4 offset:8192\n"
    "v_mfma_f32_16x16x32_bf16 v[124:127], v[232:235], a[16:19], v[124:127]\n"
    "ds_read_b64_tr_b16 v[218:219], v4 offset:8704\n"
    "v_mfma_f32_16x16x32_bf16 v[128:131], v[232:235], a[20:23], v[128:131]\n"
    "s_waitcnt lgkmcnt(6)\n"
    "v_mfma_f32_16x16x32_bf16 v[140:143], v[236:239], a[20:23], v[140:143]\n"
    "ds_read_b64_tr_b16 v[220:221], v4 offset:8200\n"
    "v_mfma_f32_16x16x32_bf16 v[136:139], v[236:239], a[16:19], v[136:139]\n"
    "ds_read_b64_tr_b16 v[222:223], v4 offset:8712\n"
    "v_mfma_f32_16x16x32_bf16 v[132:135], v[236:239], a[12:15], v[132:135]\n"
    "s_waitcnt lgkmcnt(6)\n"
    "v_mfma_f32_16x16x32_bf16 v[144:147], v[240:243], a[12:15], v[144:147]\n"
    "ds_read_b64_tr_b16 v[224:225], v5 offset:8192\n"
    "v_mfma_f32_16x16x32_bf16 v[148:151], v[240:243], a[16:19], v[148:151]\n"
    "ds_read_b64_tr_b16 v[226:227], v5 offset:8704\n"
    "v_mfma_f32_16x16x32_bf16 v[152:155], v[240:243], a[20:23], v[152:155]\n"
    "s_waitcnt lgkmcnt(6)\n"
    "v_mfma_f32_16x16x32_bf16 v[164:167], v[244:247], a[20:23], v[164:167]\n"
    "ds_read_b64_tr_b16 v[228:229], v5 offset:8200\n"
    "v_mfma_f32_16x16x32_bf16 v[160:163], v[244:247], a[16:19], v[160:163]\n"
    "ds_read_b64_tr_b16 v[230:231], v5 offset:8712\n"
    "v_mfma_f32_16x16x32_bf16 v[156:159], v[244:247], a[12:15], v[156:159]\n"
    "s_waitcnt lgkmcnt(6)\n"
    "v_mfma_f32_16x16x32_bf16 v[168:171], v[216:219], a[0:3], 0\n"
    "ds_read_b64_tr_b16 v[232:233], v4 offset:12288\n"
    "v_mfma_f32_16x16x32_bf16 v[172:175], v[216:219], a[4:7], 0\n"
    "ds_read_b64_tr_b16 v[234:235], v4 offset:12800\n"
    "v_mfma_f32_16x16x32_bf16 v[176:179], v[216:219], a[8:11], 0\n"
    "s_waitcnt lgkmcnt(6)\n"
    "v_mfma_f32_16x16x32_bf16 v[188:191], v[220:223], a[8:11], 0\n"
    "ds_read_b64_tr_b16 v[236:237], v4 offset:12296\n"
    "v_mfma_f32_16x16x32_bf16 v[184:187], v[220:223], a[4:7], 0\n"
    "ds_read_b64_tr_b16 v[238:239], v4 offset:12808\n"
    "v_add_u32 v4, 0x4000, v4\n"
    "v_mfma_f32_16x16x32_bf16 v[180:183], v[220:223], a[0:3], 0\n"
    "s_waitcnt vmcnt(8) lgkmcnt(6)\n"
    "ds_read_b128 a[24:27], v2 offset:6144\n"
    "v_mfma_f32_16x16x32_bf16 v[192:195], v[224:227], a[0:3], 0\n"
    "ds_read_b64_tr_b16 v[240:241], v5 offset:12288\n"
    "v_mfma_f32_16x16x32_bf16 v[196:199], v[224:227], a[4:7], 0\n"
    "ds_read_b64_tr_b16 v[242:243], v5 offset:12800\n"
    "v_mfma_f32_16x16x32_bf16 v[200:203], v[224:227], a[8:11], 0\n"
    "s_waitcnt vmcnt(6) lgkmcnt(7)\n"
    "ds_read_b128 a[28:31], v2 offset:8192\n"
    "v_mfma_f32_16x16x32_bf16 v[212:215], v[228:231], a[8:11], 0\n"
    "ds_read_b64_tr_b16 v[244:245], v5 offset:12296\n"
    "v_mfma_f32_16x16x32_bf16 v[208:211], v[228:231], a[4:7], 0\n"
    "ds_read_b64_tr_b16 v[246:247], v5 offset:12808\n"
    "v_add_u32 v5, 0x4000, v5\n"
    "v_mfma_f32_16x16x32_bf16 v[204:207], v[228:231], a[0:3], 0\n"
    "s_waitcnt vmcnt(4) lgkmcnt(8)\n"
    "ds_read_b128 a[32:35], v2 offset:10240\n"
    "v_mfma_f32_16x16x32_bf16 v[168:171], v[232:235], a[12:15], v[168:171]\n"
    "s_barrier\n"
    "ds_read_b64_tr_b16 v[216:217], v4 offset:0\n"
    "v_mfma_f32_16x16x32_bf16 v[172:175], v[232:235], a[16:19], v[172:175]\n"
    "ds_read_b64_tr_b16 v[218:219], v4 offset:512\n"
    "v_mfma_f32_16x16x32_bf16 v[176:179], v[232:235], a[20:23], v[176:179]\n"
    "s_waitcnt lgkmcnt(9)\n"
    "ds_read_b128 a[36:39], v3 offset:6144\n"
    "v_mfma_f32_16x16x32_bf16 v[188:191], v[236:239], a[20:23], v[188:191]\n"
    "ds_read_b64_tr_b16 v[220:221], v4 offset:8\n"
    "v_mfma_f32_16x16x32_bf16 v[184:187], v[236:239], a[16:19], v[184:187]\n"
    "ds_read_b64_tr_b16 v[222:223], v4 offset:520\n"
    "v_mfma_f32_16x16x32_bf16 v[180:183], v[236:239], a[12:15], v[180:183]\n"
    "s_waitcnt lgkmcnt(9)\n"
    "ds_read_b128 a[40:43], v3 offset:8192\n"
    "v_mfma_f32_16x16x32_bf16 v[192:195], v[240:243], a[12:15], v[192:195]\n"
    "ds_read_b64_tr_b16 v[224:225], v5 offset:0\n"
    "v_mfma_f32_16x16x32_bf16 v[196:199], v[240:243], a[16:19], v[196:199]\n"
    "ds_read_b64_tr_b16 v[226:227], v5 offset:512\n"
    "v_mfma_f32_16x16x32_bf16 v[200:203], v[240:243], a[20:23], v[200:203]\n"
    "s_waitcnt lgkmcnt(9)\n"
    "ds_read_b128 a[44:47], v3 offset:10240\n"
    "v_mfma_f32_16x16x32_bf16 v[212:215], v[244:247], a[20:23], v[212:215]\n"
    "s_add_u32 m0, s69, 0\n"
    "ds_read_b64_tr_b16 v[228:229], v5 offset:8\n"
    "v_mfma_f32_16x16x32_bf16 v[208:211], v[244:247], a[16:19], v[208:211]\n"
    "ds_read_b64_tr_b16 v[230:231], v5 offset:520\n"
    "v_mfma_f32_16x16x32_bf16 v[204:207], v[244:247], a[12:15], v[204:207]\n"
    "s_waitcnt lgkmcnt(9)\n"
    "buffer_load_dwordx4 v10, s[52:55], s67 offen offset:0  lds\n"
    "v_mfma_f32_16x16x32_bf16 v[72:75], v[216:219], a[0:3], 0\n"
    "ds_read_b64_tr_b16 v[232:233], v4 offset:4096\n"
    "v_mfma_f32_16x16x32_bf16 v[76:79], v[216:219], a[4:7], 0\n"
    "ds_read_b64_tr_b16 v[234:235], v4 offset:4608\n"
    "v_mfma_f32_16x16x32_bf16 v[80:83], v[216:219], a[8:11], 0\n"
    "s_waitcnt lgkmcnt(8)\n"
    "buffer_load_dwordx4 v249, s[52:55], s67 offen offset:1024  lds\n"
    "v_mfma_f32_16x16x32_bf16 v[104:107], v[220:223], a[8:11], 0\n"
    "s_add_u32 s67, s39, 0\n"
    "ds_read_b64_tr_b16 v[236:237], v4 offset:4104\n"
    "v_mfma_f32_16x16x32_bf16 v[100:103], v[220:223], a[4:7], 0\n"
    "s_add_u32 m0, s69, 2048\n"
    "ds_read_b64_tr_b16 v[238:239], v4 offset:4616\n"
    "v_mfma_f32_16x16x32_bf16 v[96:99], v[220:223], a[0:3], 0\n"
    "s_waitcnt lgkmcnt(7)\n"
    "buffer_load_dwordx4 v10, s[52:55], s67 offen offset:0  lds\n"
    "v_mfma_f32_16x16x32_bf16 v[84:87], v[224:227], a[0:3], 0\n"
    "ds_read_b64_tr_b16 v[240:241], v5 offset:4096\n"
    "v_mfma_f32_16x16x32_bf16 v[88:91], v[224:227], a[4:7], 0\n"
    "ds_read_b64_tr_b16 v[242:243], v5 offset:4608\n"
    "v_mfma_f32_16x16x32_bf16 v[92:95], v[224:227], a[8:11], 0\n"
    "s_waitcnt lgkmcnt(6)\n"
    "buffer_load_dwordx4 v249, s[52:55], s67 offen offset:1024  lds\n"
    "v_mfma_f32_16x16x32_bf16 v[116:119], v[228:231], a[8:11], 0\n"
    "s_add_u32 s67, s39, 14336\n"
    "ds_read_b64_tr_b16 v[244:245], v5 offset:4104\n"
    "v_mfma_f32_16x16x32_bf16 v[112:115], v[228:231], a[4:7], 0\n"
    "s_add_u32 m0, s69, 4096\n"
    "ds_read_b64_tr_b16 v[246:247], v5 offset:4616\n"
    "v_mfma_f32_16x16x32_bf16 v[108:111], v[228:231], a[0:3], 0\n"
    "s_waitcnt lgkmcnt(6)\n"
    "buffer_load_dwordx4 v10, s[52:55], s67 offen offset:0  lds\n"
    "v_mfma_f32_16x16x32_bf16 v[72:75], v[232:235], a[12:15], v[72:75]\n"
    "ds_read_b64_tr_b16 v[216:217], v4 offset:8192\n"
    "v_mfma_f32_16x16x32_bf16 v[76:79], v[232:235], a[16:19], v[76:79]\n"
    "ds_read_b64_tr_b16 v[218:219], v4 offset:8704\n"
    "v_mfma_f32_16x16x32_bf16 v[80:83], v[232:235], a[20:23], v[80:83]\n"
    "s_waitcnt lgkmcnt(6)\n"
    "buffer_load_dwordx4 v249, s[52:55], s67 offen offset:1024  lds\n"
    "v_mfma_f32_16x16x32_bf16 v[104:107], v[236:239], a[20:23], v[104:107]\n"
    "s_add_u32 s67, s39, 28672\n"
    "ds_read_b64_tr_b16 v[220:221], v4 offset:8200\n"
    "v_mfma_f32_16x16x32_bf16 v[100:103], v[236:239], a[16:19], v[100:103]\n"
    "s_add_u32 m0, s69, 6144\n"
    "ds_read_b64_tr_b16 v[222:223], v4 offset:8712\n"
    "v_mfma_f32_16x16x32_bf16 v[96:99], v[236:239], a[12:15], v[96:99]\n"
    "s_waitcnt lgkmcnt(6)\n"
    "buffer_load_dwordx4 v10, s[52:55], s67 offen offset:0  lds\n"
    "v_mfma_f32_16x16x32_bf16 v[84:87], v[240:243], a[12:15], v[84:87]\n"
    "ds_read_b64_tr_b16 v[224:225], v5 offset:8192\n"
    "v_mfma_f32_16x16x32_bf16 v[88:91], v[240:243], a[16:19], v[88:91]\n"
    "ds_read_b64_tr_b16 v[226:227], v5 offset:8704\n"
    "v_mfma_f32_16x16x32_bf16 v[92:95], v[240:243], a[20:23], v[92:95]\n"
    "s_waitcnt lgkmcnt(6)\n"
    "buffer_load_dwordx4 v249, s[52:55], s67 offen offset:1024  lds\n"
    "v_mfma_f32_16x16x32_bf16 v[116:119], v[244:247], a[20:23], v[116:119]\n"
    "s_add_u32 s67, s40, 0\n"
    "ds_read_b64_tr_b16 v[228:229], v5 offset:8200\n"
    "v_mfma_f32_16x16x32_bf16 v[112:115], v[244:247], a[16:19], v[112:115]\n"
    "s_add_u32 m0, s69, 8192\n"
    "ds_read_b64_tr_b16 v[230:231], v5 offset:8712\n"
    "v_mfma_f32_16x16x32_bf16 v[108:111], v[244:247], a[12:15], v[108:111]\n"
    "s_waitcnt lgkmcnt(6)\n"
    "buffer_load_dwordx4 v10, s[52:55], s67 offen offset:0  lds\n"
    "v_mfma_f32_16x16x32_bf16 v[120:123], v[216:219], a[24:27], v[120:123]\n"
    "ds_read_b64_tr_b16 v[232:233], v4 offset:12288\n"
    "v_mfma_f32_16x16x32_bf16 v[124:127], v[216:219], a[28:31], v[124:127]\n"
    "ds_read_b64_tr_b16 v[234:235], v4 offset:12800\n"
    "v_mfma_f32_16x16x32_bf16 v[128:131], v[216:219], a[32:35], v[128:131]\n"
    "s_waitcnt lgkmcnt(6)\n"
    "buffer_load_dwordx4 v249, s[52:55], s67 offen offset:1024  lds\n"
    "v_mfma_f32_16x16x32_bf16 v[140:143], v[220:223], a[32:35], v[140:143]\n"
    "s_add_u32 s67, s40, 14336\n"
    "ds_read_b64_tr_b16 v[236:237], v4 offset:12296\n"
    "v_mfma_f32_16x16x32_bf16 v[136:139], v[220:223], a[28:31], v[136:139]\n"
    "s_add_u32 m0, s69, 10240\n"
    "ds_read_b64_tr_b16 v[238:239], v4 offset:12808\n"
    "v_add_u32 v4, 0x4000, v4\n"
    "v_mfma_f32_16x16x32_bf16 v[132:135], v[220:223], a[24:27], v[132:135]\n"
    "s_waitcnt lgkmcnt(6)\n"
    "buffer_load_dwordx4 v10, s[52:55], s67 offen offset:0  lds\n"
    "v_mfma_f32_16x16x32_bf16 v[144:147], v[224:227], a[24:27], v[144:147]\n"
    "ds_read_b64_tr_b16 v[240:241], v5 offset:12288\n"
    "v_mfma_f32_16x16x32_bf16 v[148:151], v[224:227], a[28:31], v[148:151]\n"
    "ds_read_b64_tr_b16 v[242:243], v5 offset:12800\n"
    "v_mfma_f32_16x16x32_bf16 v[152:155], v[224:227], a[32:35], v[152:155]\n"
    "s_waitcnt lgkmcnt(6)\n"
    "buffer_load_dwordx4 v249, s[52:55], s67 offen offset:1024  lds\n"
    "v_mfma_f32_16x16x32_bf16 v[164:167], v[228:231], a[32:35], v[164:167]\n"
    "s_add_u32 s67, s40, 28672\n"
    "ds_read_b64_tr_b16 v[244:245], v5 offset:12296\n"
    "v_mfma_f32_16x16x32_bf16 v[160:163], v[228:231], a[28:31], v[160:163]\n"
    "ds_read_b64_tr_b16 v[246:247], v5 offset:12808\n"
    "v_add_u32 v5, 0x4000, v5\n"
    "v_mfma_f32_16x16x32_bf16 v[156:159], v[228:231], a[24:27], v[156:159]\n"
    "s_waitcnt lgkmcnt(6)\n"
    "v_mfma_f32_16x16x32_bf16 v[120:123], v[232:235], a[36:39], v[120:123]\n"
    "s_barrier\n"
    "ds_read_b64_tr_b16 v[216:217], v4 offset:0\n"
    "v_mfma_f32_16x16x32_bf16 v[124:127], v[232:235], a[40:43], v[124:127]\n"
    "ds_read_b64_tr_b16 v[218:219], v4 offset:512\n"
    "v_mfma_f32_16x16x32_bf16 v[128:131], v[232:235], a[44:47], v[128:131]\n"
    "s_waitcnt lgkmcnt(6)\n"
    "v_mfma_f32_16x16x32_bf16 v[140:143], v[236:239], a[44:47], v[140:143]\n"
    "ds_read_b64_tr_b16 v[220:221], v4 offset:8\n"
    "v_mfma_f32_16x16x32_bf16 v[136:139], v[236:239], a[40:43], v[136:139]\n"
    "ds_read_b64_tr_b16 v[222:223], v4 offset:520\n"
    "v_mfma_f32_16x16x32_bf16 v[132:135], v[236:239], a[36:39], v[132:135]\n"
    "s_waitcnt lgkmcnt(6)\n"
    "v_mfma_f32_16x16x32_bf16 v[144:147], v[240:243], a[36:39], v[144:147]\n"
    "ds_read_b64_tr_b16 v[224:225], v5 offset:0\n"
    "v_mfma_f32_16x16x32_bf16 v[148:151], v[240:243], a[40:43], v[148:151]\n"
    "ds_read_b64_tr_b16 v[226:227], v5 offset:512\n"
    "v_mfma_f32_16x16x32_bf16 v[152:155], v[240:243], a[44:47], v[152:155]\n"
    "s_waitcnt lgkmcnt(6)\n"
    "v_mfma_f32_16x16x32_bf16 v[164:167], v[244:247], a[44:47], v[164:167]\n"
    "ds_read_b64_tr_b16 v[228:229], v5 offset:8\n"
    "v_mfma_f32_16x16x32_bf16 v[160:163], v[244:247], a[40:43], v[160:163]\n"
    "ds_read_b64_tr_b16 v[230:231], v5 offset:520\n"
    "v_mfma_f32_16x16x32_bf16 v[156:159], v[244:247], a[36:39], v[156:159]\n"
    "s_waitcnt lgkmcnt(6)\n"
    "v_mfma_f32_16x16x32_bf16 v[168:171], v[216:219], a[24:27], v[168:171]\n"
    "ds_read_b64_tr_b16 v[232:233], v4 offset:4096\n"
    "v_mfma_f32_16x16x32_bf16 v[172:175], v[216:219], a[28:31], v[172:175]\n"
    "ds_read_b64_tr_b16 v[234:235], v4 offset:4608\n"
    "v_mfma_f32_16x16x32_bf16 v[176:179], v[216:219], a[32:35], v[176:179]\n"
    "s_waitcnt lgkmcnt(6)\n"
    "v_mfma_f32_16x16x32_bf16 v[188:191], v[220:223], a[32:35], v[188:191]\n"
    "ds_read_b64_tr_b16 v[236:237], v4 offset:4104\n"
    "v_mfma_f32_16x16x32_bf16 v[184:187], v[220:223], a[28:31], v[184:187]\n"
    "ds_read_b64_tr_b16 v[238:239], v4 offset:4616\n"
    "v_mfma_f32_16x16x32_bf16 v[180:183], v[220:223], a[24:27], v[180:183]\n"
    "s_waitcnt vmcnt(14) lgkmcnt(6)\n"
    "ds_read_b128 a[48:51], v2 offset:12288\n"
    "v_mfma_f32_16x16x32_bf16 v[192:195], v[224:227], a[24:27], v[192:195]\n"
    "ds_read_b64_tr_b16 v[240:241], v5 offset:4096\n"
    "v_mfma_f32_16x16x32_bf16 v[196:199], v[224:227], a[28:31], v[196:199]\n"
    "ds_read_b64_tr_b16 v[242:243], v5 offset:4608\n"
    "v_mfma_f32_16x16x32_bf16 v[200:203], v[224:227], a[32:35], v[200:203]\n"
    "s_waitcnt vmcnt(12) lgkmcnt(7)\n"
    "ds_read_b128 a[52:55], v2 offset:14336\n"
    "v_mfma_f32_16x16x32_bf16 v[212:215], v[228:231], a[32:35], v[212:215]\n"
    "ds_read_b64_tr_b16 v[244:245], v5 offset:4104\n"
    "v_mfma_f32_16x16x32_bf16 v[208:211], v[228:231], a[28:31], v[208:211]\n"
    "ds_read_b64_tr_b16 v[246:247], v5 offset:4616\n"
    "v_mfma_f32_16x16x32_bf16 v[204:207], v[228:231], a[24:27], v[204:207]\n"
    "s_waitcnt vmcnt(10) lgkmcnt(8)\n"
    "ds_read_b128 a[56:59], v2 offset:0\n"
    "v_mfma_f32_16x16x32_bf16 v[168:171], v[232:235], a[36:39], v[168:171]\n"
    "ds_read_b64_tr_b16 v[216:217], v4 offset:8192\n"
    "v_mfma_f32_16x16x32_bf16 v[172:175], v[232:235], a[40:43], v[172:175]\n"
    "ds_read_b64_tr_b16 v[218:219], v4 offset:8704\n"
    "v_mfma_f32_16x16x32_bf16 v[176:179], v[232:235], a[44:47], v[176:179]\n"
    "s_waitcnt lgkmcnt(9)\n"
    "ds_read_b128 a[60:63], v3 offset:12288\n"
    "v_mfma_f32_16x16x32_bf16 v[188:191], v[236:239], a[44:47], v[188:191]\n"
    "ds_read_b64_tr_b16 v[220:221], v4 offset:8200\n"
    "v_mfma_f32_16x16x32_bf16 v[184:187], v[236:239], a[40:43], v[184:187]\n"
    "ds_read_b64_tr_b16 v[222:223], v4 offset:8712\n"
    "v_mfma_f32_16x16x32_bf16 v[180:183], v[236:239], a[36:39], v[180:183]\n"
    "s_waitcnt lgkmcnt(9)\n"
    "ds_read_b128 a[64:67], v3 offset:14336\n"
    "v_mfma_f32_16x16x32_bf16 v[192:195], v[240:243], a[36:39], v[192:195]\n"
    "ds_read_b64_tr_b16 v[224:225], v5 offset:8192\n"
    "v_mfma_f32_16x16x32_bf16 v[196:199], v[240:243], a[40:43], v[196:199]\n"
    "ds_read_b64_tr_b16 v[226:227], v5 offset:8704\n"
    "v_mfma_f32_16x16x32_bf16 v[200:203], v[240:243], a[44:47], v[200:203]\n"
    "s_waitcnt lgkmcnt(9)\n"
    "ds_read_b128 a[68:71], v3 offset:0\n"
    "v_mfma_f32_16x16x32_bf16 v[212:215], v[244:247], a[44:47], v[212:215]\n"
    "ds_read_b64_tr_b16 v[228:229], v5 offset:8200\n"
    "v_mfma_f32_16x16x32_bf16 v[208:211], v[244:247], a[40:43], v[208:211]\n"
    "ds_read_b64_tr_b16 v[230:231], v5 offset:8712\n"
    "v_mfma_f32_16x16x32_bf16 v[204:207], v[244:247], a[36:39], v[204:207]\n"
    "s_waitcnt lgkmcnt(9)\n"
    "v_mfma_f32_16x16x32_bf16 v[72:75], v[216:219], a[24:27], v[72:75]\n"
    "ds_read_b64_tr_b16 v[232:233], v4 offset:12288\n"
    "v_mfma_f32_16x16x32_bf16 v[76:79], v[216:219], a[28:31], v[76:79]\n"
    "ds_read_b64_tr_b16 v[234:235], v4 offset:12800\n"
    "v_mfma_f32_16x16x32_bf16 v[80:83], v[216:219], a[32:35], v[80:83]\n"
    "s_waitcnt lgkmcnt(8)\n"
    "v_mfma_f32_16x16x32_bf16 v[104:107], v[220:223], a[32:35], v[104:107]\n"
    "s_add_u32 m0, s69, 12288\n"
    "ds_read_b64_tr_b16 v[236:237], v4 offset:12296\n"
    "v_mfma_f32_16x16x32_bf16 v[100:103], v[220:223], a[28:31], v[100:103]\n"
    "ds_read_b64_tr_b16 v[238:239], v4 offset:12808\n"
    "v_add_u32 v4, 0x4000, v4\n"
    "v_mfma_f32_16x16x32_bf16 v[96:99], v[220:223], a[24:27], v[96:99]\n"
    "s_waitcnt lgkmcnt(7)\n"
    "buffer_load_dwordx4 v10, s[52:55], s67 offen offset:0  lds\n"
    "v_mfma_f32_16x16x32_bf16 v[84:87], v[224:227], a[24:27], v[84:87]\n"
    "ds_read_b64_tr_b16 v[240:241], v5 offset:12288\n"
    "v_mfma_f32_16x16x32_bf16 v[88:91], v[224:227], a[28:31], v[88:91]\n"
    "ds_read_b64_tr_b16 v[242:243], v5 offset:12800\n"
    "v_mfma_f32_16x16x32_bf16 v[92:95], v[224:227], a[32:35], v[92:95]\n"
    "s_waitcnt lgkmcnt(6)\n"
    "buffer_load_dwordx4 v249, s[52:55], s67 offen offset:1024  lds\n"
    "v_mfma_f32_16x16x32_bf16 v[116:119], v[228:231], a[32:35], v[116:119]\n"
    "s_add_u32 s67, s41, 0\n"
    "ds_read_b64_tr_b16 v[244:245], v5 offset:12296\n"
    "v_mfma_f32_16x16x32_bf16 v[112:115], v[228:231], a[28:31], v[112:115]\n"
    "s_add_u32 m0, s69, 14336\n"
    "ds_read_b64_tr_b16 v[246:247], v5 offset:12808\n"
    "v_add_u32 v5, 0x4000, v5\n"
    "v_mfma_f32_16x16x32_bf16 v[108:111], v[228:231], a[24:27], v[108:111]\n"
    "s_waitcnt lgkmcnt(6)\n"
    "buffer_load_dwordx4 v10, s[52:55], s67 offen offset:0  lds\n"
    "v_mfma_f32_16x16x32_bf16 v[72:75], v[232:235], a[36:39], v[72:75]\n"
    "s_barrier\n"
    "ds_read_b64_tr_b16 v[216:217], v4 offset:0\n"
    "v_mfma_f32_16x16x32_bf16 v[76:79], v[232:235], a[40:43], v[76:79]\n"
    "ds_read_b64_tr_b16 v[218:219], v4 offset:512\n"
    "v_mfma_f32_16x16x32_bf16 v[80:83], v[232:235], a[44:47], v[80:83]\n"
    "s_waitcnt lgkmcnt(6)\n"
    "buffer_load_dwordx4 v249, s[52:55], s67 offen offset:1024  lds\n"
    "v_mfma_f32_16x16x32_bf16 v[104:107], v[236:239], a[44:47], v[104:107]\n"
    "s_add_u32 s67, s41, 14336\n"
    "ds_read_b64_tr_b16 v[220:221], v4 offset:8\n"
    "v_mfma_f32_16x16x32_bf16 v[100:103], v[236:239], a[40:43], v[100:103]\n"
    "s_add_u32 m0, s69, 0\n"
    "ds_read_b64_tr_b16 v[222:223], v4 offset:520\n"
    "v_mfma_f32_16x16x32_bf16 v[96:99], v[236:239], a[36:39], v[96:99]\n"
    "s_waitcnt lgkmcnt(6)\n"
    "buffer_load_dwordx4 v10, s[52:55], s67 offen offset:0  lds\n"
    "v_mfma_f32_16x16x32_bf16 v[84:87], v[240:243], a[36:39], v[84:87]\n"
    "ds_read_b64_tr_b16 v[224:225], v5 offset:0\n"
    "v_mfma_f32_16x16x32_bf16 v[88:91], v[240:243], a[40:43], v[88:91]\n"
    "ds_read_b64_tr_b16 v[226:227], v5 offset:512\n"
    "v_mfma_f32_16x16x32_bf16 v[92:95], v[240:243], a[44:47], v[92:95]\n"
    "s_waitcnt lgkmcnt(6)\n"
    "buffer_load_dwordx4 v249, s[52:55], s67 offen offset:1024  lds\n"
    "v_mfma_f32_16x16x32_bf16 v[116:119], v[244:247], a[44:47], v[116:119]\n"
    "s_add_u32 s67, s41, 28672\n"
    "ds_read_b64_tr_b16 v[228:229], v5 offset:8\n"
    "v_mfma_f32_16x16x32_bf16 v[112:115], v[244:247], a[40:43], v[112:115]\n"
    "ds_read_b64_tr_b16 v[230:231], v5 offset:520\n"
    "v_mfma_f32_16x16x32_bf16 v[108:111], v[244:247], a[36:39], v[108:111]\n"
    "s_waitcnt lgkmcnt(6)\n"
    "v_mfma_f32_16x16x32_bf16 v[120:123], v[216:219], a[48:51], v[120:123]\n"
    "ds_read_b64_tr_b16 v[232:233], v4 offset:4096\n"
    "v_mfma_f32_16x16x32_bf16 v[124:127], v[216:219], a[52:55], v[124:127]\n"
    "ds_read_b64_tr_b16 v[234:235], v4 offset:4608\n"
    "v_mfma_f32_16x16x32_bf16 v[128:131], v[216:219], a[56:59], v[128:131]\n"
    "s_waitcnt lgkmcnt(6)\n"
    "v_mfma_f32_16x16x32_bf16 v[140:143], v[220:223], a[56:59], v[140:143]\n"
    "ds_read_b64_tr_b16 v[236:237], v4 offset:4104\n"
    "v_mfma_f32_16x16x32_bf16 v[136:139], v[220:223], a[52:55], v[136:139]\n"
    "ds_read_b64_tr_b16 v[238:239], v4 offset:4616\n"
    "v_mfma_f32_16x16x32_bf16 v[132:135], v[220:223], a[48:51], v[132:135]\n"
    "s_waitcnt lgkmcnt(6)\n"
    "v_mfma_f32_16x16x32_bf16 v[144:147], v[224:227], a[48:51], v[144:147]\n"
    "ds_read_b64_tr_b16 v[240:241], v5 offset:4096\n"
    "v_mfma_f32_16x16x32_bf16 v[148:151], v[224:227], a[52:55], v[148:151]\n"
    "ds_read_b64_tr_b16 v[242:243], v5 offset:4608\n"
    "v_mfma_f32_16x16x32_bf16 v[152:155], v[224:227], a[56:59], v[152:155]\n"
    "s_waitcnt lgkmcnt(6)\n"
    "v_mfma_f32_16x16x32_bf16 v[164:167], v[228:231], a[56:59], v[164:167]\n"
    "ds_read_b64_tr_b16 v[244:245], v5 offset:4104\n"
    "v_mfma_f32_16x16x32_bf16 v[160:163], v[228:231], a[52:55], v[160:163]\n"
    "ds_read_b64_tr_b16 v[246:247], v5 offset:4616\n"
    "v_mfma_f32_16x16x32_bf16 v[156:159], v[228:231], a[48:51], v[156:159]\n"
    "s_waitcnt lgkmcnt(6)\n"
    "v_mfma_f32_16x16x32_bf16 v[120:123], v[232:235], a[60:63], v[120:123]\n"
    "ds_read_b64_tr_b16 v[216:217], v4 offset:8192\n"
    "v_mfma_f32_16x16x32_bf16 v[124:127], v[232:235], a[64:67], v[124:127]\n"
    "ds_read_b64_tr_b16 v[218:219], v4 offset:8704\n"
    "v_mfma_f32_16x16x32_bf16 v[128:131], v[232:235], a[68:71], v[128:131]\n"
    "s_waitcnt lgkmcnt(6)\n"
    "v_mfma_f32_16x16x32_bf16 v[140:143], v[236:239], a[68:71], v[140:143]\n"
    "ds_read_b64_tr_b16 v[220:221], v4 offset:8200\n"
    "v_mfma_f32_16x16x32_bf16 v[136:139], v[236:239], a[64:67], v[136:139]\n"
    "ds_read_b64_tr_b16 v[222:223], v4 offset:8712\n"
    "v_mfma_f32_16x16x32_bf16 v[132:135], v[236:239], a[60:63], v[132:135]\n"
    "s_waitcnt lgkmcnt(6)\n"
    "v_mfma_f32_16x16x32_bf16 v[144:147], v[240:243], a[60:63], v[144:147]\n"
    "ds_read_b64_tr_b16 v[224:225], v5 offset:8192\n"
    "v_mfma_f32_16x16x32_bf16 v[148:151], v[240:243], a[64:67], v[148:151]\n"
    "ds_read_b64_tr_b16 v[226:227], v5 offset:8704\n"
    "v_mfma_f32_16x16x32_bf16 v[152:155], v[240:243], a[68:71], v[152:155]\n"
    "s_waitcnt lgkmcnt(6)\n"
    "v_mfma_f32_16x16x32_bf16 v[164:167], v[244:247], a[68:71], v[164:167]\n"
    "ds_read_b64_tr_b16 v[228:229], v5 offset:8200\n"
    "v_mfma_f32_16x16x32_bf16 v[160:163], v[244:247], a[64:67], v[160:163]\n"
    "ds_read_b64_tr_b16 v[230:231], v5 offset:8712\n"
    "v_mfma_f32_16x16x32_bf16 v[156:159], v[244:247], a[60:63], v[156:159]\n"
    "s_waitcnt lgkmcnt(6)\n"
    "v_mfma_f32_16x16x32_bf16 v[168:171], v[216:219], a[48:51], v[168:171]\n"
    "ds_read_b64_tr_b16 v[232:233], v4 offset:12288\n"
    "v_mfma_f32_16x16x32_bf16 v[172:175], v[216:219], a[52:55], v[172:175]\n"
    "ds_read_b64_tr_b16 v[234:235], v4 offset:12800\n"
    "v_mfma_f32_16x16x32_bf16 v[176:179], v[216:219], a[56:59], v[176:179]\n"
    "s_waitcnt lgkmcnt(6)\n"
    "v_mfma_f32_16x16x32_bf16 v[188:191], v[220:223], a[56:59], v[188:191]\n"
    "ds_read_b64_tr_b16 v[236:237], v4 offset:12296\n"
    "v_mfma_f32_16x16x32_bf16 v[184:187], v[220:223], a[52:55], v[184:187]\n"
    "ds_read_b64_tr_b16 v[238:239], v4 offset:12808\n"
    "v_add_u32 v4, 0x4000, v4\n"
    "v_mfma_f32_16x16x32_bf16 v[180:183], v[220:223], a[48:51], v[180:183]\n"
    "s_waitcnt vmcnt(14) lgkmcnt(6)\n"
    "ds_read_b128 a[72:75], v2 offset:2048\n"
    "v_mfma_f32_16x16x32_bf16 v[192:195], v[224:227], a[48:51], v[192:195]\n"
    "ds_read_b64_tr_b16 v[240:241], v5 offset:12288\n"
    "v_mfma_f32_16x16x32_bf16 v[196:199], v[224:227], a[52:55], v[196:199]\n"
    "ds_read_b64_tr_b16 v[242:243], v5 offset:12800\n"
    "v_mfma_f32_16x16x32_bf16 v[200:203], v[224:227], a[56:59], v[200:203]\n"
    "s_waitcnt vmcnt(12) lgkmcnt(7)\n"
    "ds_read_b128 a[76:79], v2 offset:4096\n"
    "v_mfma_f32_16x16x32_bf16 v[212:215], v[228:231], a[56:59], v[212:215]\n"
    "ds_read_b64_tr_b16 v[244:245], v5 offset:12296\n"
    "v_mfma_f32_16x16x32_bf16 v[208:211], v[228:231], a[52:55], v[208:211]\n"
    "ds_read_b64_tr_b16 v[246:247], v5 offset:12808\n"
    "v_add_u32 v5, 0x4000, v5\n"
    "v_mfma_f32_16x16x32_bf16 v[204:207], v[228:231], a[48:51], v[204:207]\n"
    "s_waitcnt vmcnt(10) lgkmcnt(8)\n"
    "ds_read_b128 a[80:83], v2 offset:6144\n"
    "v_mfma_f32_16x16x32_bf16 v[168:171], v[232:235], a[60:63], v[168:171]\n"
    "s_barrier\n"
    "ds_read_b64_tr_b16 v[216:217], v4 offset:0\n"
    "v_mfma_f32_16x16x32_bf16 v[172:175], v[232:235], a[64:67], v[172:175]\n"
    "ds_read_b64_tr_b16 v[218:219], v4 offset:512\n"
    "v_mfma_f32_16x16x32_bf16 v[176:179], v[232:235], a[68:71], v[176:179]\n"
    "s_waitcnt lgkmcnt(9)\n"
    "ds_read_b128 a[84:87], v3 offset:2048\n"
    "v_mfma_f32_16x16x32_bf16 v[188:191], v[236:239], a[68:71], v[188:191]\n"
    "ds_read_b64_tr_b16 v[220:221], v4 offset:8\n"
    "v_mfma_f32_16x16x32_bf16 v[184:187], v[236:239], a[64:67], v[184:187]\n"
    "ds_read_b64_tr_b16 v[222:223], v4 offset:520\n"
    "v_mfma_f32_16x16x32_bf16 v[180:183], v[236:239], a[60:63], v[180:183]\n"
    "s_waitcnt lgkmcnt(9)\n"
    "ds_read_b128 a[88:91], v3 offset:4096\n"
    "v_mfma_f32_16x16x32_bf16 v[192:195], v[240:243], a[60:63], v[192:195]\n"
    "ds_read_b64_tr_b16 v[224:225], v5 offset:0\n"
    "v_mfma_f32_16x16x32_bf16 v[196:199], v[240:243], a[64:67], v[196:199]\n"
    "ds_read_b64_tr_b16 v[226:227], v5 offset:512\n"
    "v_mfma_f32_16x16x32_bf16 v[200:203], v[240:243], a[68:71], v[200:203]\n"
    "s_waitcnt lgkmcnt(9)\n"
    "ds_read_b128 a[92:95], v3 offset:6144\n"
    "v_mfma_f32_16x16x32_bf16 v[212:215], v[244:247], a[68:71], v[212:215]\n"
    "ds_read_b64_tr_b16 v[228:229], v5 offset:8\n"
    "v_mfma_f32_16x16x32_bf16 v[208:211], v[244:247], a[64:67], v[208:211]\n"
    "ds_read_b64_tr_b16 v[230:231], v5 offset:520\n"
    "v_mfma_f32_16x16x32_bf16 v[204:207], v[244:247], a[60:63], v[204:207]\n"
    "s_waitcnt lgkmcnt(9)\n"
    "v_mfma_f32_16x16x32_bf16 v[72:75], v[216:219], a[48:51], v[72:75]\n"
    "ds_read_b64_tr_b16 v[232:233], v4 offset:4096\n"
    "v_mfma_f32_16x16x32_bf16 v[76:79], v[216:219], a[52:55], v[76:79]\n"
    "ds_read_b64_tr_b16 v[234:235], v4 offset:4608\n"
    "v_mfma_f32_16x16x32_bf16 v[80:83], v[216:219], a[56:59], v[80:83]\n"
    "s_waitcnt lgkmcnt(8)\n"
    "v_mfma_f32_16x16x32_bf16 v[104:107], v[220:223], a[56:59], v[104:107]\n"
    "s_add_u32 m0, s69, 2048\n"
    "ds_read_b64_tr_b16 v[236:237], v4 offset:4104\n"
    "v_mfma_f32_16x16x32_bf16 v[100:103], v[220:223], a[52:55], v[100:103]\n"
    "ds_read_b64_tr_b16 v[238:239], v4 offset:4616\n"
    "v_mfma_f32_16x16x32_bf16 v[96:99], v[220:223], a[48:51], v[96:99]\n"
    "s_waitcnt lgkmcnt(7)\n"
    "buffer_load_dwordx4 v10, s[52:55], s67 offen offset:0  lds\n"
    "v_mfma_f32_16x16x32_bf16 v[84:87], v[224:227], a[48:51], v[84:87]\n"
    "ds_read_b64_tr_b16 v[240:241], v5 offset:4096\n"
    "v_mfma_f32_16x16x32_bf16 v[88:91], v[224:227], a[52:55], v[88:91]\n"
    "ds_read_b64_tr_b16 v[242:243], v5 offset:4608\n"
    "v_mfma_f32_16x16x32_bf16 v[92:95], v[224:227], a[56:59], v[92:95]\n"
    "s_waitcnt lgkmcnt(6)\n"
    "buffer_load_dwordx4 v249, s[52:55], s67 offen offset:1024  lds\n"
    "v_mfma_f32_16x16x32_bf16 v[116:119], v[228:231], a[56:59], v[116:119]\n"
    "s_add_u32 s67, s42, 0\n"
    "ds_read_b64_tr_b16 v[244:245], v5 offset:4104\n"
    "v_mfma_f32_16x16x32_bf16 v[112:115], v[228:231], a[52:55], v[112:115]\n"
    "s_add_u32 m0, s69, 4096\n"
    "ds_read_b64_tr_b16 v[246:247], v5 offset:4616\n"
    "v_mfma_f32_16x16x32_bf16 v[108:111], v[228:231], a[48:51], v[108:111]\n"
    "s_waitcnt lgkmcnt(6)\n"
    "buffer_load_dwordx4 v10, s[52:55], s67 offen offset:0  lds\n"
    "v_mfma_f32_16x16x32_bf16 v[72:75], v[232:235], a[60:63], v[72:75]\n"
    "ds_read_b64_tr_b16 v[216:217], v4 offset:8192\n"
    "v_mfma_f32_16x16x32_bf16 v[76:79], v[232:235], a[64:67], v[76:79]\n"
    "ds_read_b64_tr_b16 v[218:219], v4 offset:8704\n"
    "v_mfma_f32_16x16x32_bf16 v[80:83], v[232:235], a[68:71], v[80:83]\n"
    "s_waitcnt lgkmcnt(6)\n"
    "buffer_load_dwordx4 v249, s[52:55], s67 offen offset:1024  lds\n"
    "v_mfma_f32_16x16x32_bf16 v[104:107], v[236:239], a[68:71], v[104:107]\n"
    "s_add_u32 s67, s42, 14336\n"
    "ds_read_b64_tr_b16 v[220:221], v4 offset:8200\n"
    "v_mfma_f32_16x16x32_bf16 v[100:103], v[236:239], a[64:67], v[100:103]\n"
    "s_add_u32 m0, s69, 6144\n"
    "ds_read_b64_tr_b16 v[222:223], v4 offset:8712\n"
    "v_mfma_f32_16x16x32_bf16 v[96:99], v[236:239], a[60:63], v[96:99]\n"
    "s_waitcnt lgkmcnt(6)\n"
    "buffer_load_dwordx4 v10, s[52:55], s67 offen offset:0  lds\n"
    "v_mfma_f32_16x16x32_bf16 v[84:87], v[240:243], a[60:63], v[84:87]\n"
    "ds_read_b64_tr_b16 v[224:225], v5 offset:8192\n"
    "v_mfma_f32_16x16x32_bf16 v[88:91], v[240:243], a[64:67], v[88:91]\n"
    "ds_read_b64_tr_b16 v[226:227], v5 offset:8704\n"
    "v_mfma_f32_16x16x32_bf16 v[92:95], v[240:243], a[68:71], v[92:95]\n"
    "s_waitcnt lgkmcnt(6)\n"
    "buffer_load_dwordx4 v249, s[52:55], s67 offen offset:1024  lds\n"
    "v_mfma_f32_16x16x32_bf16 v[116:119], v[244:247], a[68:71], v[116:119]\n"
    "s_add_u32 s67, s42, 28672\n"
    "ds_read_b64_tr_b16 v[228:229], v5 offset:8200\n"
    "v_mfma_f32_16x16x32_bf16 v[112:115], v[244:247], a[64:67], v[112:115]\n"
    "ds_read_b64_tr_b16 v[230:231], v5 offset:8712\n"
    "v_mfma_f32_16x16x32_bf16 v[108:111], v[244:247], a[60:63], v[108:111]\n"
    "s_waitcnt lgkmcnt(6)\n"
    "v_mfma_f32_16x16x32_bf16 v[120:123], v[216:219], a[72:75], v[120:123]\n"
    "ds_read_b64_tr_b16 v[232:233], v4 offset:12288\n"
    "v_mfma_f32_16x16x32_bf16 v[124:127], v[216:219], a[76:79], v[124:127]\n"
    "ds_read_b64_tr_b16 v[234:235], v4 offset:12800\n"
    "v_mfma_f32_16x16x32_bf16 v[128:131], v[216:219], a[80:83], v[128:131]\n"
    "s_waitcnt lgkmcnt(6)\n"
    "v_mfma_f32_16x16x32_bf16 v[140:143], v[220:223], a[80:83], v[140:143]\n"
    "ds_read_b64_tr_b16 v[236:237], v4 offset:12296\n"
    "v_mfma_f32_16x16x32_bf16 v[136:139], v[220:223], a[76:79], v[136:139]\n"
    "ds_read_b64_tr_b16 v[238:239], v4 offset:12808\n"
    "v_add_u32 v4, 0x4000, v4\n"
    "v_mfma_f32_16x16x32_bf16 v[132:135], v[220:223], a[72:75], v[132:135]\n"
    "s_waitcnt lgkmcnt(6)\n"
    "v_mfma_f32_16x16x32_bf16 v[144:147], v[224:227], a[72:75], v[144:147]\n"
    "ds_read_b64_tr_b16 v[240:241], v5 offset:12288\n"
    "v_mfma_f32_16x16x32_bf16 v[148:151], v[224:227], a[76:79], v[148:151]\n"
    "ds_read_b64_tr_b16 v[242:243], v5 offset:12800\n"
    "v_mfma_f32_16x16x32_bf16 v[152:155], v[224:227], a[80:83], v[152:155]\n"
    "s_waitcnt lgkmcnt(6)\n"
    "v_mfma_f32_16x16x32_bf16 v[164:167], v[228:231], a[80:83], v[164:167]\n"
    "ds_read_b64_tr_b16 v[244:245], v5 offset:12296\n"
    "v_mfma_f32_16x16x32_bf16 v[160:163], v[228:231], a[76:79], v[160:163]\n"
    "ds_read_b64_tr_b16 v[246:247], v5 offset:12808\n"
    "v_add_u32 v5, 0x4000, v5\n"
    "v_mfma_f32_16x16x32_bf16 v[156:159], v[228:231], a[72:75], v[156:159]\n"
    "s_waitcnt lgkmcnt(6)\n"
    "v_mfma_f32_16x16x32_bf16 v[120:123], v[232:235], a[84:87], v[120:123]\n"
    "s_barrier\n"
    "ds_read_b64_tr_b16 v[216:217], v4 offset:0\n"
    "v_mfma_f32_16x16x32_bf16 v[124:127], v[232:235], a[88:91], v[124:127]\n"
    "ds_read_b64_tr_b16 v[218:219], v4 offset:512\n"
    "v_mfma_f32_16x16x32_bf16 v[128:131], v[232:235], a[92:95], v[128:131]\n"
    "s_waitcnt lgkmcnt(6)\n"
    "v_mfma_f32_16x16x32_bf16 v[140:143], v[236:239], a[92:95], v[140:143]\n"
    "ds_read_b64_tr_b16 v[220:221], v4 offset:8\n"
    "v_mfma_f32_16x16x32_bf16 v[136:139], v[236:239], a[88:91], v[136:139]\n"
    "ds_read_b64_tr_b16 v[222:223], v4 offset:520\n"
    "v_mfma_f32_16x16x32_bf16 v[132:135], v[236:239], a[84:87], v[132:135]\n"
    "s_waitcnt lgkmcnt(6)\n"
    "v_mfma_f32_16x16x32_bf16 v[144:147], v[240:243], a[84:87], v[144:147]\n"
    "ds_read_b64_tr_b16 v[224:225], v5 offset:0\n"
    "v_mfma_f32_16x16x32_bf16 v[148:151], v[240:243], a[88:91], v[148:151]\n"
    "ds_read_b64_tr_b16 v[226:227], v5 offset:512\n"
    "v_mfma_f32_16x16x32_bf16 v[152:155], v[240:243], a[92:95], v[152:155]\n"
    "s_waitcnt lgkmcnt(6)\n"
    "v_mfma_f32_16x16x32_bf16 v[164:167], v[244:247], a[92:95], v[164:167]\n"
    "ds_read_b64_tr_b16 v[228:229], v5 offset:8\n"
    "v_mfma_f32_16x16x32_bf16 v[160:163], v[244:247], a[88:91], v[160:163]\n"
    "ds_read_b64_tr_b16 v[230:231], v5 offset:520\n"
    "v_mfma_f32_16x16x32_bf16 v[156:159], v[244:247], a[84:87], v[156:159]\n"
    "s_waitcnt lgkmcnt(6)\n"
    "v_mfma_f32_16x16x32_bf16 v[168:171], v[216:219], a[72:75], v[168:171]\n"
    "ds_read_b64_tr_b16 v[232:233], v4 offset:4096\n"
    "v_mfma_f32_16x16x32_bf16 v[172:175], v[216:219], a[76:79], v[172:175]\n"
    "ds_read_b64_tr_b16 v[234:235], v4 offset:4608\n"
    "v_mfma_f32_16x16x32_bf16 v[176:179], v[216:219], a[80:83], v[176:179]\n"
    "s_waitcnt lgkmcnt(6)\n"
    "v_mfma_f32_16x16x32_bf16 v[188:191], v[220:223], a[80:83], v[188:191]\n"
    "ds_read_b64_tr_b16 v[236:237], v4 offset:4104\n"
    "v_mfma_f32_16x16x32_bf16 v[184:187], v[220:223], a[76:79], v[184:187]\n"
    "ds_read_b64_tr_b16 v[238:239], v4 offset:4616\n"
    "v_mfma_f32_16x16x32_bf16 v[180:183], v[220:223], a[72:75], v[180:183]\n"
    "s_waitcnt vmcnt(14) lgkmcnt(6)\n"
    "ds_read_b128 a[96:99], v2 offset:8192\n"
    "v_mfma_f32_16x16x32_bf16 v[192:195], v[224:227], a[72:75], v[192:195]\n"
    "ds_read_b64_tr_b16 v[240:241], v5 offset:4096\n"
    "v_mfma_f32_16x16x32_bf16 v[196:199], v[224:227], a[76:79], v[196:199]\n"
    "ds_read_b64_tr_b16 v[242:243], v5 offset:4608\n"
    "v_mfma_f32_16x16x32_bf16 v[200:203], v[224:227], a[80:83], v[200:203]\n"
    "s_waitcnt vmcnt(12) lgkmcnt(7)\n"
    "ds_read_b128 a[100:103], v2 offset:10240\n"
    "v_mfma_f32_16x16x32_bf16 v[212:215], v[228:231], a[80:83], v[212:215]\n"
    "ds_read_b64_tr_b16 v[244:245], v5 offset:4104\n"
    "v_mfma_f32_16x16x32_bf16 v[208:211], v[228:231], a[76:79], v[208:211]\n"
    "ds_read_b64_tr_b16 v[246:247], v5 offset:4616\n"
    "v_mfma_f32_16x16x32_bf16 v[204:207], v[228:231], a[72:75], v[204:207]\n"
    "s_waitcnt vmcnt(10) lgkmcnt(8)\n"
    "ds_read_b128 a[104:107], v2 offset:12288\n"
    "v_mfma_f32_16x16x32_bf16 v[168:171], v[232:235], a[84:87], v[168:171]\n"
    "ds_read_b64_tr_b16 v[216:217], v4 offset:8192\n"
    "v_mfma_f32_16x16x32_bf16 v[172:175], v[232:235], a[88:91], v[172:175]\n"
    "ds_read_b64_tr_b16 v[218:219], v4 offset:8704\n"
    "v_mfma_f32_16x16x32_bf16 v[176:179], v[232:235], a[92:95], v[176:179]\n"
    "s_waitcnt lgkmcnt(9)\n"
    "ds_read_b128 a[108:111], v3 offset:8192\n"
    "v_mfma_f32_16x16x32_bf16 v[188:191], v[236:239], a[92:95], v[188:191]\n"
    "ds_read_b64_tr_b16 v[220:221], v4 offset:8200\n"
    "v_mfma_f32_16x16x32_bf16 v[184:187], v[236:239], a[88:91], v[184:187]\n"
    "ds_read_b64_tr_b16 v[222:223], v4 offset:8712\n"
    "v_mfma_f32_16x16x32_bf16 v[180:183], v[236:239], a[84:87], v[180:183]\n"
    "s_waitcnt lgkmcnt(9)\n"
    "ds_read_b128 a[112:115], v3 offset:10240\n"
    "v_mfma_f32_16x16x32_bf16 v[192:195], v[240:243], a[84:87], v[192:195]\n"
    "ds_read_b64_tr_b16 v[224:225], v5 offset:8192\n"
    "v_mfma_f32_16x16x32_bf16 v[196:199], v[240:243], a[88:91], v[196:199]\n"
    "ds_read_b64_tr_b16 v[226:227], v5 offset:8704\n"
    "v_mfma_f32_16x16x32_bf16 v[200:203], v[240:243], a[92:95], v[200:203]\n"
    "s_waitcnt lgkmcnt(9)\n"
    "ds_read_b128 a[116:119], v3 offset:12288\n"
    "v_mfma_f32_16x16x32_bf16 v[212:215], v[244:247], a[92:95], v[212:215]\n"
    "ds_read_b64_tr_b16 v[228:229], v5 offset:8200\n"
    "v_mfma_f32_16x16x32_bf16 v[208:211], v[244:247], a[88:91], v[208:211]\n"
    "ds_read_b64_tr_b16 v[230:231], v5 offset:8712\n"
    "v_mfma_f32_16x16x32_bf16 v[204:207], v[244:247], a[84:87], v[204:207]\n"
    "s_waitcnt lgkmcnt(9)\n"
    "v_mfma_f32_16x16x32_bf16 v[72:75], v[216:219], a[72:75], v[72:75]\n"
    "ds_read_b64_tr_b16 v[232:233], v4 offset:12288\n"
    "v_mfma_f32_16x16x32_bf16 v[76:79], v[216:219], a[76:79], v[76:79]\n"
    "ds_read_b64_tr_b16 v[234:235], v4 offset:12800\n"
    "v_mfma_f32_16x16x32_bf16 v[80:83], v[216:219], a[80:83], v[80:83]\n"
    "s_waitcnt lgkmcnt(8)\n"
    "v_mfma_f32_16x16x32_bf16 v[104:107], v[220:223], a[80:83], v[104:107]\n"
    "s_add_u32 m0, s69, 8192\n"
    "ds_read_b64_tr_b16 v[236:237], v4 offset:12296\n"
    "v_mfma_f32_16x16x32_bf16 v[100:103], v[220:223], a[76:79], v[100:103]\n"
    "ds_read_b64_tr_b16 v[238:239], v4 offset:12808\n"
    "v_add_u32 v4, 0x4000, v4\n"
    "v_mfma_f32_16x16x32_bf16 v[96:99], v[220:223], a[72:75], v[96:99]\n"
    "s_waitcnt lgkmcnt(7)\n"
    "buffer_load_dwordx4 v10, s[52:55], s67 offen offset:0  lds\n"
    "v_mfma_f32_16x16x32_bf16 v[84:87], v[224:227], a[72:75], v[84:87]\n"
    "ds_read_b64_tr_b16 v[240:241], v5 offset:12288\n"
    "v_mfma_f32_16x16x32_bf16 v[88:91], v[224:227], a[76:79], v[88:91]\n"
    "ds_read_b64_tr_b16 v[242:243], v5 offset:12800\n"
    "v_mfma_f32_16x16x32_bf16 v[92:95], v[224:227], a[80:83], v[92:95]\n"
    "s_waitcnt lgkmcnt(6)\n"
    "buffer_load_dwordx4 v249, s[52:55], s67 offen offset:1024  lds\n"
    "v_mfma_f32_16x16x32_bf16 v[116:119], v[228:231], a[80:83], v[116:119]\n"
    "ds_read_b64_tr_b16 v[244:245], v5 offset:12296\n"
    "v_mfma_f32_16x16x32_bf16 v[112:115], v[228:231], a[76:79], v[112:115]\n"
    "ds_read_b64_tr_b16 v[246:247], v5 offset:12808\n"
    "v_add_u32 v5, 0x4000, v5\n"
    "v_mfma_f32_16x16x32_bf16 v[108:111], v[228:231], a[72:75], v[108:111]\n"
    "s_waitcnt lgkmcnt(6)\n"
    "v_mfma_f32_16x16x32_bf16 v[72:75], v[232:235], a[84:87], v[72:75]\n"
    "s_barrier\n"
    "ds_read_b64_tr_b16 v[216:217], v4 offset:0\n"
    "v_mfma_f32_16x16x32_bf16 v[76:79], v[232:235], a[88:91], v[76:79]\n"
    "ds_read_b64_tr_b16 v[218:219], v4 offset:512\n"
    "v_mfma_f32_16x16x32_bf16 v[80:83], v[232:235], a[92:95], v[80:83]\n"
    "s_waitcnt lgkmcnt(6)\n"
    "v_mfma_f32_16x16x32_bf16 v[104:107], v[236:239], a[92:95], v[104:107]\n"
    "ds_read_b64_tr_b16 v[220:221], v4 offset:8\n"
    "v_mfma_f32_16x16x32_bf16 v[100:103], v[236:239], a[88:91], v[100:103]\n"
    "ds_read_b64_tr_b16 v[222:223], v4 offset:520\n"
    "v_mfma_f32_16x16x32_bf16 v[96:99], v[236:239], a[84:87], v[96:99]\n"
    "s_waitcnt lgkmcnt(6)\n"
    "v_mfma_f32_16x16x32_bf16 v[84:87], v[240:243], a[84:87], v[84:87]\n"
    "ds_read_b64_tr_b16 v[224:225], v5 offset:0\n"
    "v_mfma_f32_16x16x32_bf16 v[88:91], v[240:243], a[88:91], v[88:91]\n"
    "ds_read_b64_tr_b16 v[226:227], v5 offset:512\n"
    "v_mfma_f32_16x16x32_bf16 v[92:95], v[240:243], a[92:95], v[92:95]\n"
    "s_waitcnt lgkmcnt(6)\n"
    "v_mfma_f32_16x16x32_bf16 v[116:119], v[244:247], a[92:95], v[116:119]\n"
    "ds_read_b64_tr_b16 v[228:229], v5 offset:8\n"
    "v_mfma_f32_16x16x32_bf16 v[112:115], v[244:247], a[88:91], v[112:115]\n"
    "ds_read_b64_tr_b16 v[230:231], v5 offset:520\n"
    "v_mfma_f32_16x16x32_bf16 v[108:111], v[244:247], a[84:87], v[108:111]\n"
    "s_waitcnt lgkmcnt(6)\n"
    "v_mfma_f32_16x16x32_bf16 v[120:123], v[216:219], a[96:99], v[120:123]\n"
    "ds_read_b64_tr_b16 v[232:233], v4 offset:4096\n"
    "v_mfma_f32_16x16x32_bf16 v[124:127], v[216:219], a[100:103], v[124:127]\n"
    "ds_read_b64_tr_b16 v[234:235], v4 offset:4608\n"
    "v_mfma_f32_16x16x32_bf16 v[128:131], v[216:219], a[104:107], v[128:131]\n"
    "s_waitcnt lgkmcnt(6)\n"
    "v_mfma_f32_16x16x32_bf16 v[140:143], v[220:223], a[104:107], v[140:143]\n"
    "ds_read_b64_tr_b16 v[236:237], v4 offset:4104\n"
    "v_mfma_f32_16x16x32_bf16 v[136:139], v[220:223], a[100:103], v[136:139]\n"
    "ds_read_b64_tr_b16 v[238:239], v4 offset:4616\n"
    "v_mfma_f32_16x16x32_bf16 v[132:135], v[220:223], a[96:99], v[132:135]\n"
    "s_waitcnt lgkmcnt(6)\n"
    "v_mfma_f32_16x16x32_bf16 v[144:147], v[224:227], a[96:99], v[144:147]\n"
    "ds_read_b64_tr_b16 v[240:241], v5 offset:4096\n"
    "v_mfma_f32_16x16x32_bf16 v[148:151], v[224:227], a[100:103], v[148:151]\n"
    "ds_read_b64_tr_b16 v[242:243], v5 offset:4608\n"
    "v_mfma_f32_16x16x32_bf16 v[152:155], v[224:227], a[104:107], v[152:155]\n"
    "s_waitcnt lgkmcnt(6)\n"
    "v_mfma_f32_16x16x32_bf16 v[164:167], v[228:231], a[104:107], v[164:167]\n"
    "ds_read_b64_tr_b16 v[244:245], v5 offset:4104\n"
    "v_mfma_f32_16x16x32_bf16 v[160:163], v[228:231], a[100:103], v[160:163]\n"
    "ds_read_b64_tr_b16 v[246:247], v5 offset:4616\n"
    "v_mfma_f32_16x16x32_bf16 v[156:159], v[228:231], a[96:99], v[156:159]\n"
    "s_waitcnt lgkmcnt(6)\n"
    "v_mfma_f32_16x16x32_bf16 v[120:123], v[232:235], a[108:111], v[120:123]\n"
    "ds_read_b64_tr_b16 v[216:217], v4 offset:8192\n"
    "v_mfma_f32_16x16x32_bf16 v[124:127], v[232:235], a[112:115], v[124:127]\n"
    "ds_read_b64_tr_b16 v[218:219], v4 offset:8704\n"
    "v_mfma_f32_16x16x32_bf16 v[128:131], v[232:235], a[116:119], v[128:131]\n"
    "s_waitcnt lgkmcnt(6)\n"
    "v_mfma_f32_16x16x32_bf16 v[140:143], v[236:239], a[116:119], v[140:143]\n"
    "ds_read_b64_tr_b16 v[220:221], v4 offset:8200\n"
    "v_mfma_f32_16x16x32_bf16 v[136:139], v[236:239], a[112:115], v[136:139]\n"
    "ds_read_b64_tr_b16 v[222:223], v4 offset:8712\n"
    "v_mfma_f32_16x16x32_bf16 v[132:135], v[236:239], a[108:111], v[132:135]\n"
    "s_waitcnt lgkmcnt(6)\n"
    "v_mfma_f32_16x16x32_bf16 v[144:147], v[240:243], a[108:111], v[144:147]\n"
    "ds_read_b64_tr_b16 v[224:225], v5 offset:8192\n"
    "v_mfma_f32_16x16x32_bf16 v[148:151], v[240:243], a[112:115], v[148:151]\n"
    "ds_read_b64_tr_b16 v[226:227], v5 offset:8704\n"
    "v_mfma_f32_16x16x32_bf16 v[152:155], v[240:243], a[116:119], v[152:155]\n"
    "s_waitcnt lgkmcnt(6)\n"
    "v_mfma_f32_16x16x32_bf16 v[164:167], v[244:247], a[116:119], v[164:167]\n"
    "ds_read_b64_tr_b16 v[228:229], v5 offset:8200\n"
    "v_mfma_f32_16x16x32_bf16 v[160:163], v[244:247], a[112:115], v[160:163]\n"
    "ds_read_b64_tr_b16 v[230:231], v5 offset:8712\n"
    "v_mfma_f32_16x16x32_bf16 v[156:159], v[244:247], a[108:111], v[156:159]\n"
    "s_waitcnt lgkmcnt(6)\n"
    "v_mfma_f32_16x16x32_bf16 v[168:171], v[216:219], a[96:99], v[168:171]\n"
    "ds_read_b64_tr_b16 v[232:233], v4 offset:12288\n"
    "v_mfma_f32_16x16x32_bf16 v[172:175], v[216:219], a[100:103], v[172:175]\n"
    "ds_read_b64_tr_b16 v[234:235], v4 offset:12800\n"
    "v_mfma_f32_16x16x32_bf16 v[176:179], v[216:219], a[104:107], v[176:179]\n"
    "s_waitcnt lgkmcnt(6)\n"
    "v_mfma_f32_16x16x32_bf16 v[188:191], v[220:223], a[104:107], v[188:191]\n"
    "ds_read_b64_tr_b16 v[236:237], v4 offset:12296\n"
    "v_mfma_f32_16x16x32_bf16 v[184:187], v[220:223], a[100:103], v[184:187]\n"
    "ds_read_b64_tr_b16 v[238:239], v4 offset:12808\n"
    "v_add_u32 v4, 0xfffe8000, v4\n"
    "v_mfma_f32_16x16x32_bf16 v[180:183], v[220:223], a[96:99], v[180:183]\n"
    "s_waitcnt vmcnt(10) lgkmcnt(6)\n"
    "ds_read_b128 a[120:123], v2 offset:14336\n"
    "v_mfma_f32_16x16x32_bf16 v[192:195], v[224:227], a[96:99], v[192:195]\n"
    "ds_read_b64_tr_b16 v[240:241], v5 offset:12288\n"
    "v_mfma_f32_16x16x32_bf16 v[196:199], v[224:227], a[100:103], v[196:199]\n"
    "ds_read_b64_tr_b16 v[242:243], v5 offset:12800\n"
    "v_mfma_f32_16x16x32_bf16 v[200:203], v[224:227], a[104:107], v[200:203]\n"
    "s_waitcnt vmcnt(8) lgkmcnt(7)\n"
    "ds_read_b128 a[124:127], v2 offset:0\n"
    "v_mfma_f32_16x16x32_bf16 v[212:215], v[228:231], a[104:107], v[212:215]\n"
    "ds_read_b64_tr_b16 v[244:245], v5 offset:12296\n"
    "v_mfma_f32_16x16x32_bf16 v[208:211], v[228:231], a[100:103], v[208:211]\n"
    "ds_read_b64_tr_b16 v[246:247], v5 offset:12808\n"
    "v_add_u32 v5, 0xfffe8000, v5\n"
    "v_mfma_f32_16x16x32_bf16 v[204:207], v[228:231], a[96:99], v[204:207]\n"
    "s_waitcnt vmcnt(6) lgkmcnt(8)\n"
    "ds_read_b128 a[128:131], v2 offset:2048\n"
    "v_mfma_f32_16x16x32_bf16 v[168:171], v[232:235], a[108:111], v[168:171]\n"
    "s_barrier\n"
    "ds_read_b64_tr_b16 v[216:217], v4 offset:0\n"
    "v_mfma_f32_16x16x32_bf16 v[172:175], v[232:235], a[112:115], v[172:175]\n"
    "ds_read_b64_tr_b16 v[218:219], v4 offset:512\n"
    "v_mfma_f32_16x16x32_bf16 v[176:179], v[232:235], a[116:119], v[176:179]\n"
    "s_waitcnt lgkmcnt(9)\n"
    "ds_read_b128 a[132:135], v3 offset:14336\n"
    "v_mfma_f32_16x16x32_bf16 v[188:191], v[236:239], a[116:119], v[188:191]\n"
    "ds_read_b64_tr_b16 v[220:221], v4 offset:8\n"
    "v_mfma_f32_16x16x32_bf16 v[184:187], v[236:239], a[112:115], v[184:187]\n"
    "ds_read_b64_tr_b16 v[222:223], v4 offset:520\n"
    "v_mfma_f32_16x16x32_bf16 v[180:183], v[236:239], a[108:111], v[180:183]\n"
    "s_waitcnt lgkmcnt(9)\n"
    "ds_read_b128 a[136:139], v3 offset:0\n"
    "v_mfma_f32_16x16x32_bf16 v[192:195], v[240:243], a[108:111], v[192:195]\n"
    "ds_read_b64_tr_b16 v[224:225], v5 offset:0\n"
    "v_mfma_f32_16x16x32_bf16 v[196:199], v[240:243], a[112:115], v[196:199]\n"
    "ds_read_b64_tr_b16 v[226:227], v5 offset:512\n"
    "v_mfma_f32_16x16x32_bf16 v[200:203], v[240:243], a[116:119], v[200:203]\n"
    "s_waitcnt lgkmcnt(9)\n"
    "ds_read_b128 a[140:143], v3 offset:2048\n"
    "v_mfma_f32_16x16x32_bf16 v[212:215], v[244:247], a[116:119], v[212:215]\n"
    "ds_read_b64_tr_b16 v[228:229], v5 offset:8\n"
    "v_mfma_f32_16x16x32_bf16 v[208:211], v[244:247], a[112:115], v[208:211]\n"
    "ds_read_b64_tr_b16 v[230:231], v5 offset:520\n"
    "v_mfma_f32_16x16x32_bf16 v[204:207], v[244:247], a[108:111], v[204:207]\n"
    "s_waitcnt lgkmcnt(9)\n"
    "v_mfma_f32_16x16x32_bf16 v[72:75], v[216:219], a[96:99], v[72:75]\n"
    "ds_read_b64_tr_b16 v[232:233], v4 offset:4096\n"
    "v_mfma_f32_16x16x32_bf16 v[76:79], v[216:219], a[100:103], v[76:79]\n"
    "ds_read_b64_tr_b16 v[234:235], v4 offset:4608\n"
    "v_mfma_f32_16x16x32_bf16 v[80:83], v[216:219], a[104:107], v[80:83]\n"
    "s_waitcnt lgkmcnt(8)\n"
    "v_mfma_f32_16x16x32_bf16 v[104:107], v[220:223], a[104:107], v[104:107]\n"
    "ds_read_b64_tr_b16 v[236:237], v4 offset:4104\n"
    "v_mfma_f32_16x16x32_bf16 v[100:103], v[220:223], a[100:103], v[100:103]\n"
    "ds_read_b64_tr_b16 v[238:239], v4 offset:4616\n"
    "v_mfma_f32_16x16x32_bf16 v[96:99], v[220:223], a[96:99], v[96:99]\n"
    "s_waitcnt lgkmcnt(7)\n"
    "v_mfma_f32_16x16x32_bf16 v[84:87], v[224:227], a[96:99], v[84:87]\n"
    "ds_read_b64_tr_b16 v[240:241], v5 offset:4096\n"
    "v_mfma_f32_16x16x32_bf16 v[88:91], v[224:227], a[100:103], v[88:91]\n"
    "ds_read_b64_tr_b16 v[242:243], v5 offset:4608\n"
    "v_mfma_f32_16x16x32_bf16 v[92:95], v[224:227], a[104:107], v[92:95]\n"
    "s_waitcnt lgkmcnt(6)\n"
    "v_mfma_f32_16x16x32_bf16 v[116:119], v[228:231], a[104:107], v[116:119]\n"
    "ds_read_b64_tr_b16 v[244:245], v5 offset:4104\n"
    "v_mfma_f32_16x16x32_bf16 v[112:115], v[228:231], a[100:103], v[112:115]\n"
    "ds_read_b64_tr_b16 v[246:247], v5 offset:4616\n"
    "v_mfma_f32_16x16x32_bf16 v[108:111], v[228:231], a[96:99], v[108:111]\n"
    "s_waitcnt lgkmcnt(6)\n"
    "v_mfma_f32_16x16x32_bf16 v[72:75], v[232:235], a[108:111], v[72:75]\n"
    "ds_read_b64_tr_b16 v[216:217], v4 offset:8192\n"
    "v_mfma_f32_16x16x32_bf16 v[76:79], v[232:235], a[112:115], v[76:79]\n"
    "ds_read_b64_tr_b16 v[218:219], v4 offset:8704\n"
    "v_mfma_f32_16x16x32_bf16 v[80:83], v[232:235], a[116:119], v[80:83]\n"
    "s_waitcnt lgkmcnt(6)\n"
    "v_mfma_f32_16x16x32_bf16 v[104:107], v[236:239], a[116:119], v[104:107]\n"
    "ds_read_b64_tr_b16 v[220:221], v4 offset:8200\n"
    "v_mfma_f32_16x16x32_bf16 v[100:103], v[236:239], a[112:115], v[100:103]\n"
    "ds_read_b64_tr_b16 v[222:223], v4 offset:8712\n"
    "v_mfma_f32_16x16x32_bf16 v[96:99], v[236:239], a[108:111], v[96:99]\n"
    "s_waitcnt lgkmcnt(6)\n"
    "v_mfma_f32_16x16x32_bf16 v[84:87], v[240:243], a[108:111], v[84:87]\n"
    "ds_read_b64_tr_b16 v[224:225], v5 offset:8192\n"
    "v_mfma_f32_16x16x32_bf16 v[88:91], v[240:243], a[112:115], v[88:91]\n"
    "ds_read_b64_tr_b16 v[226:227], v5 offset:8704\n"
    "v_mfma_f32_16x16x32_bf16 v[92:95], v[240:243], a[116:119], v[92:95]\n"
    "s_waitcnt lgkmcnt(6)\n"
    "v_mfma_f32_16x16x32_bf16 v[116:119], v[244:247], a[116:119], v[116:119]\n"
    "ds_read_b64_tr_b16 v[228:229], v5 offset:8200\n"
    "v_mfma_f32_16x16x32_bf16 v[112:115], v[244:247], a[112:115], v[112:115]\n"
    "ds_read_b64_tr_b16 v[230:231], v5 offset:8712\n"
    "v_mfma_f32_16x16x32_bf16 v[108:111], v[244:247], a[108:111], v[108:111]\n"
    "s_waitcnt lgkmcnt(6)\n"
    "v_mfma_f32_16x16x32_bf16 v[120:123], v[216:219], a[120:123], v[120:123]\n"
    "ds_read_b64_tr_b16 v[232:233], v4 offset:12288\n"
    "v_mfma_f32_16x16x32_bf16 v[124:127], v[216:219], a[124:127], v[124:127]\n"
    "ds_read_b64_tr_b16 v[234:235], v4 offset:12800\n"
    "v_mfma_f32_16x16x32_bf16 v[128:131], v[216:219], a[128:131], v[128:131]\n"
    "s_waitcnt lgkmcnt(6)\n"
    "v_mfma_f32_16x16x32_bf16 v[140:143], v[220:223], a[128:131], v[140:143]\n"
    "ds_read_b64_tr_b16 v[236:237], v4 offset:12296\n"
    "v_mfma_f32_16x16x32_bf16 v[136:139], v[220:223], a[124:127], v[136:139]\n"
    "ds_read_b64_tr_b16 v[238:239], v4 offset:12808\n"
    "v_add_u32 v4, 0x4000, v4\n"
    "v_mfma_f32_16x16x32_bf16 v[132:135], v[220:223], a[120:123], v[132:135]\n"
    "s_waitcnt lgkmcnt(6)\n"
    "v_mfma_f32_16x16x32_bf16 v[144:147], v[224:227], a[120:123], v[144:147]\n"
    "ds_read_b64_tr_b16 v[240:241], v5 offset:12288\n"
    "v_mfma_f32_16x16x32_bf16 v[148:151], v[224:227], a[124:127], v[148:151]\n"
    "ds_read_b64_tr_b16 v[242:243], v5 offset:12800\n"
    "v_mfma_f32_16x16x32_bf16 v[152:155], v[224:227], a[128:131], v[152:155]\n"
    "s_waitcnt lgkmcnt(6)\n"
    "v_mfma_f32_16x16x32_bf16 v[164:167], v[228:231], a[128:131], v[164:167]\n"
    "ds_read_b64_tr_b16 v[244:245], v5 offset:12296\n"
    "v_mfma_f32_16x16x32_bf16 v[160:163], v[228:231], a[124:127], v[160:163]\n"
    "ds_read_b64_tr_b16 v[246:247], v5 offset:12808\n"
    "v_add_u32 v5, 0x4000, v5\n"
    "v_mfma_f32_16x16x32_bf16 v[156:159], v[228:231], a[120:123], v[156:159]\n"
    "s_waitcnt lgkmcnt(6)\n"
    "v_mfma_f32_16x16x32_bf16 v[120:123], v[232:235], a[132:135], v[120:123]\n"
    "s_barrier\n"
    "ds_read_b64_tr_b16 v[216:217], v4 offset:0\n"
    "v_mfma_f32_16x16x32_bf16 v[124:127], v[232:235], a[136:139], v[124:127]\n"
    "ds_read_b64_tr_b16 v[218:219], v4 offset:512\n"
    "v_mfma_f32_16x16x32_bf16 v[128:131], v[232:235], a[140:143], v[128:131]\n"
    "s_waitcnt lgkmcnt(6)\n"
    "v_mfma_f32_16x16x32_bf16 v[140:143], v[236:239], a[140:143], v[140:143]\n"
    "ds_read_b64_tr_b16 v[220:221], v4 offset:8\n"
    "v_mfma_f32_16x16x32_bf16 v[136:139], v[236:239], a[136:139], v[136:139]\n"
    "ds_read_b64_tr_b16 v[222:223], v4 offset:520\n"
    "v_mfma_f32_16x16x32_bf16 v[132:135], v[236:239], a[132:135], v[132:135]\n"
    "s_waitcnt lgkmcnt(6)\n"
    "v_mfma_f32_16x16x32_bf16 v[144:147], v[240:243], a[132:135], v[144:147]\n"
    "ds_read_b64_tr_b16 v[224:225], v5 offset:0\n"
    "v_mfma_f32_16x16x32_bf16 v[148:151], v[240:243], a[136:139], v[148:151]\n"
    "ds_read_b64_tr_b16 v[226:227], v5 offset:512\n"
    "v_mfma_f32_16x16x32_bf16 v[152:155], v[240:243], a[140:143], v[152:155]\n"
    "s_waitcnt lgkmcnt(6)\n"
    "v_mfma_f32_16x16x32_bf16 v[164:167], v[244:247], a[140:143], v[164:167]\n"
    "ds_read_b64_tr_b16 v[228:229], v5 offset:8\n"
    "v_mfma_f32_16x16x32_bf16 v[160:163], v[244:247], a[136:139], v[160:163]\n"
    "ds_read_b64_tr_b16 v[230:231], v5 offset:520\n"
    "v_mfma_f32_16x16x32_bf16 v[156:159], v[244:247], a[132:135], v[156:159]\n"
    "s_waitcnt lgkmcnt(6)\n"
    "v_mfma_f32_16x16x32_bf16 v[168:171], v[216:219], a[120:123], v[168:171]\n"
    "ds_read_b64_tr_b16 v[232:233], v4 offset:4096\n"
    "v_mfma_f32_16x16x32_bf16 v[172:175], v[216:219], a[124:127], v[172:175]\n"
    "ds_read_b64_tr_b16 v[234:235], v4 offset:4608\n"
    "v_mfma_f32_16x16x32_bf16 v[176:179], v[216:219], a[128:131], v[176:179]\n"
    "s_waitcnt lgkmcnt(6)\n"
    "v_mfma_f32_16x16x32_bf16 v[188:191], v[220:223], a[128:131], v[188:191]\n"
    "ds_read_b64_tr_b16 v[236:237], v4 offset:4104\n"
    "v_mfma_f32_16x16x32_bf16 v[184:187], v[220:223], a[124:127], v[184:187]\n"
    "ds_read_b64_tr_b16 v[238:239], v4 offset:4616\n"
    "v_mfma_f32_16x16x32_bf16 v[180:183], v[220:223], a[120:123], v[180:183]\n"
    "s_waitcnt vmcnt(4) lgkmcnt(6)\n"
    "ds_read_b128 a[144:147], v2 offset:4096\n"
    "v_mfma_f32_16x16x32_bf16 v[192:195], v[224:227], a[120:123], v[192:195]\n"
    "ds_read_b64_tr_b16 v[240:241], v5 offset:4096\n"
    "v_mfma_f32_16x16x32_bf16 v[196:199], v[224:227], a[124:127], v[196:199]\n"
    "ds_read_b64_tr_b16 v[242:243], v5 offset:4608\n"
    "v_mfma_f32_16x16x32_bf16 v[200:203], v[224:227], a[128:131], v[200:203]\n"
    "s_waitcnt vmcnt(2) lgkmcnt(7)\n"
    "ds_read_b128 a[148:151], v2 offset:6144\n"
    "v_mfma_f32_16x16x32_bf16 v[212:215], v[228:231], a[128:131], v[212:215]\n"
    "ds_read_b64_tr_b16 v[244:245], v5 offset:4104\n"
    "v_mfma_f32_16x16x32_bf16 v[208:211], v[228:231], a[124:127], v[208:211]\n"
    "ds_read_b64_tr_b16 v[246:247], v5 offset:4616\n"
    "v_mfma_f32_16x16x32_bf16 v[204:207], v[228:231], a[120:123], v[204:207]\n"
    "s_waitcnt vmcnt(0) lgkmcnt(8)\n"
    "ds_read_b128 a[152:155], v2 offset:8192\n"
    "v_mfma_f32_16x16x32_bf16 v[168:171], v[232:235], a[132:135], v[168:171]\n"
    "ds_read_b64_tr_b16 v[216:217], v4 offset:8192\n"
    "v_mfma_f32_16x16x32_bf16 v[172:175], v[232:235], a[136:139], v[172:175]\n"
    "ds_read_b64_tr_b16 v[218:219], v4 offset:8704\n"
    "v_mfma_f32_16x16x32_bf16 v[176:179], v[232:235], a[140:143], v[176:179]\n"
    "s_waitcnt lgkmcnt(9)\n"
    "ds_read_b128 a[156:159], v3 offset:4096\n"
    "v_mfma_f32_16x16x32_bf16 v[188:191], v[236:239], a[140:143], v[188:191]\n"
    "ds_read_b64_tr_b16 v[220:221], v4 offset:8200\n"
    "v_mfma_f32_16x16x32_bf16 v[184:187], v[236:239], a[136:139], v[184:187]\n"
    "ds_read_b64_tr_b16 v[222:223], v4 offset:8712\n"
    "v_mfma_f32_16x16x32_bf16 v[180:183], v[236:239], a[132:135], v[180:183]\n"
    "s_waitcnt lgkmcnt(9)\n"
    "ds_read_b128 a[160:163], v3 offset:6144\n"
    "v_mfma_f32_16x16x32_bf16 v[192:195], v[240:243], a[132:135], v[192:195]\n"
    "ds_read_b64_tr_b16 v[224:225], v5 offset:8192\n"
    "v_mfma_f32_16x16x32_bf16 v[196:199], v[240:243], a[136:139], v[196:199]\n"
    "ds_read_b64_tr_b16 v[226:227], v5 offset:8704\n"
    "v_mfma_f32_16x16x32_bf16 v[200:203], v[240:243], a[140:143], v[200:203]\n"
    "s_waitcnt lgkmcnt(9)\n"
    "ds_read_b128 a[164:167], v3 offset:8192\n"
    "v_mfma_f32_16x16x32_bf16 v[212:215], v[244:247], a[140:143], v[212:215]\n"
    "ds_read_b64_tr_b16 v[228:229], v5 offset:8200\n"
    "v_subrev_u32 v2, s66, v2\n"
    "v_mfma_f32_16x16x32_bf16 v[208:211], v[244:247], a[136:139], v[208:211]\n"
    "ds_read_b64_tr_b16 v[230:231], v5 offset:8712\n"
    "v_subrev_u32 v3, s66, v3\n"
    "v_mfma_f32_16x16x32_bf16 v[204:207], v[244:247], a[132:135], v[204:207]\n"
    "s_waitcnt lgkmcnt(9)\n"
    "v_mfma_f32_16x16x32_bf16 v[72:75], v[216:219], a[120:123], v[72:75]\n"
    "ds_read_b64_tr_b16 v[232:233], v4 offset:12288\n"
    "v_mfma_f32_16x16x32_bf16 v[76:79], v[216:219], a[124:127], v[76:79]\n"
    "ds_read_b64_tr_b16 v[234:235], v4 offset:12800\n"
    "v_mfma_f32_16x16x32_bf16 v[80:83], v[216:219], a[128:131], v[80:83]\n"
    "s_waitcnt lgkmcnt(8)\n"
    "v_mfma_f32_16x16x32_bf16 v[104:107], v[220:223], a[128:131], v[104:107]\n"
    "ds_read_b64_tr_b16 v[236:237], v4 offset:12296\n"
    "v_mfma_f32_16x16x32_bf16 v[100:103], v[220:223], a[124:127], v[100:103]\n"
    "ds_read_b64_tr_b16 v[238:239], v4 offset:12808\n"
    "v_add_u32 v4, 0x4000, v4\n"
    "v_mfma_f32_16x16x32_bf16 v[96:99], v[220:223], a[120:123], v[96:99]\n"
    "s_waitcnt lgkmcnt(7)\n"
    "v_mfma_f32_16x16x32_bf16 v[84:87], v[224:227], a[120:123], v[84:87]\n"
    "ds_read_b64_tr_b16 v[240:241], v5 offset:12288\n"
    "v_mfma_f32_16x16x32_bf16 v[88:91], v[224:227], a[124:127], v[88:91]\n"
    "ds_read_b64_tr_b16 v[242:243], v5 offset:12800\n"
    "v_mfma_f32_16x16x32_bf16 v[92:95], v[224:227], a[128:131], v[92:95]\n"
    "s_waitcnt lgkmcnt(6)\n"
    "v_mfma_f32_16x16x32_bf16 v[116:119], v[228:231], a[128:131], v[116:119]\n"
    "ds_read_b64_tr_b16 v[244:245], v5 offset:12296\n"
    "v_mfma_f32_16x16x32_bf16 v[112:115], v[228:231], a[124:127], v[112:115]\n"
    "ds_read_b64_tr_b16 v[246:247], v5 offset:12808\n"
    "v_add_u32 v5, 0x4000, v5\n"
    "v_mfma_f32_16x16x32_bf16 v[108:111], v[228:231], a[120:123], v[108:111]\n"
    "s_waitcnt lgkmcnt(6)\n"
    "v_mfma_f32_16x16x32_bf16 v[72:75], v[232:235], a[132:135], v[72:75]\n"
    "s_barrier\n"
    "ds_read_b64_tr_b16 v[216:217], v4 offset:0\n"
    "v_mfma_f32_16x16x32_bf16 v[76:79], v[232:235], a[136:139], v[76:79]\n"
    "ds_read_b64_tr_b16 v[218:219], v4 offset:512\n"
    "v_mfma_f32_16x16x32_bf16 v[80:83], v[232:235], a[140:143], v[80:83]\n"
    "s_waitcnt lgkmcnt(6)\n"
    "v_mfma_f32_16x16x32_bf16 v[104:107], v[236:239], a[140:143], v[104:107]\n"
    "ds_read_b64_tr_b16 v[220:221], v4 offset:8\n"
    "v_mfma_f32_16x16x32_bf16 v[100:103], v[236:239], a[136:139], v[100:103]\n"
    "ds_read_b64_tr_b16 v[222:223], v4 offset:520\n"
    "v_mfma_f32_16x16x32_bf16 v[96:99], v[236:239], a[132:135], v[96:99]\n"
    "s_waitcnt lgkmcnt(6)\n"
    "v_mfma_f32_16x16x32_bf16 v[84:87], v[240:243], a[132:135], v[84:87]\n"
    "ds_read_b64_tr_b16 v[224:225], v5 offset:0\n"
    "v_mfma_f32_16x16x32_bf16 v[88:91], v[240:243], a[136:139], v[88:91]\n"
    "ds_read_b64_tr_b16 v[226:227], v5 offset:512\n"
    "v_mfma_f32_16x16x32_bf16 v[92:95], v[240:243], a[140:143], v[92:95]\n"
    "s_waitcnt lgkmcnt(6)\n"
    "v_mfma_f32_16x16x32_bf16 v[116:119], v[244:247], a[140:143], v[116:119]\n"
    "ds_read_b64_tr_b16 v[228:229], v5 offset:8\n"
    "v_mfma_f32_16x16x32_bf16 v[112:115], v[244:247], a[136:139], v[112:115]\n"
    "ds_read_b64_tr_b16 v[230:231], v5 offset:520\n"
    "v_mfma_f32_16x16x32_bf16 v[108:111], v[244:247], a[132:135], v[108:111]\n"
    "s_waitcnt lgkmcnt(6)\n"
    "v_mfma_f32_16x16x32_bf16 v[120:123], v[216:219], a[144:147], v[120:123]\n"
    "ds_read_b64_tr_b16 v[232:233], v4 offset:4096\n"
    "v_mfma_f32_16x16x32_bf16 v[124:127], v[216:219], a[148:151], v[124:127]\n"
    "ds_read_b64_tr_b16 v[234:235], v4 offset:4608\n"
    "v_mfma_f32_16x16x32_bf16 v[128:131], v[216:219], a[152:155], v[128:131]\n"
    "s_waitcnt lgkmcnt(6)\n"
    "v_mfma_f32_16x16x32_bf16 v[140:143], v[220:223], a[152:155], v[140:143]\n"
    "ds_read_b64_tr_b16 v[236:237], v4 offset:4104\n"
    "v_mfma_f32_16x16x32_bf16 v[136:139], v[220:223], a[148:151], v[136:139]\n"
    "ds_read_b64_tr_b16 v[238:239], v4 offset:4616\n"
    "v_mfma_f32_16x16x32_bf16 v[132:135], v[220:223], a[144:147], v[132:135]\n"
    "s_waitcnt lgkmcnt(6)\n"
    "v_mfma_f32_16x16x32_bf16 v[144:147], v[224:227], a[144:147], v[144:147]\n"
    "ds_read_b64_tr_b16 v[240:241], v5 offset:4096\n"
    "v_mfma_f32_16x16x32_bf16 v[148:151], v[224:227], a[148:151], v[148:151]\n"
    "ds_read_b64_tr_b16 v[242:243], v5 offset:4608\n"
    "v_mfma_f32_16x16x32_bf16 v[152:155], v[224:227], a[152:155], v[152:155]\n"
    "s_waitcnt lgkmcnt(6)\n"
    "v_mfma_f32_16x16x32_bf16 v[164:167], v[228:231], a[152:155], v[164:167]\n"
    "ds_read_b64_tr_b16 v[244:245], v5 offset:4104\n"
    "v_mfma_f32_16x16x32_bf16 v[160:163], v[228:231], a[148:151], v[160:163]\n"
    "ds_read_b64_tr_b16 v[246:247], v5 offset:4616\n"
    "v_mfma_f32_16x16x32_bf16 v[156:159], v[228:231], a[144:147], v[156:159]\n"
    "s_waitcnt lgkmcnt(6)\n"
    "v_mfma_f32_16x16x32_bf16 v[120:123], v[232:235], a[156:159], v[120:123]\n"
    "ds_read_b64_tr_b16 v[216:217], v4 offset:8192\n"
    "v_mfma_f32_16x16x32_bf16 v[124:127], v[232:235], a[160:163], v[124:127]\n"
    "ds_read_b64_tr_b16 v[218:219], v4 offset:8704\n"
    "v_mfma_f32_16x16x32_bf16 v[128:131], v[232:235], a[164:167], v[128:131]\n"
    "s_waitcnt lgkmcnt(6)\n"
    "v_mfma_f32_16x16x32_bf16 v[140:143], v[236:239], a[164:167], v[140:143]\n"
    "ds_read_b64_tr_b16 v[220:221], v4 offset:8200\n"
    "v_mfma_f32_16x16x32_bf16 v[136:139], v[236:239], a[160:163], v[136:139]\n"
    "ds_read_b64_tr_b16 v[222:223], v4 offset:8712\n"
    "v_mul_f32 v120, s68, v120\n"
    "v_mfma_f32_16x16x32_bf16 v[132:135], v[236:239], a[156:159], v[132:135]\n"
    "s_waitcnt lgkmcnt(6)\n"
    "v_mul_f32 v121, s68, v121\n"
    "v_cvt_pk_bf16_f32 v24, v120, v121\n"
    "v_mfma_f32_16x16x32_bf16 v[144:147], v[240:243], a[156:159], v[144:147]\n"
    "ds_read_b64_tr_b16 v[224:225], v5 offset:8192\n"
    "v_mul_f32 v122, s68, v122\n"
    "v_mfma_f32_16x16x32_bf16 v[148:151], v[240:243], a[160:163], v[148:151]\n"
    "ds_read_b64_tr_b16 v[226:227], v5 offset:8704\n"
    "v_mul_f32 v123, s68, v123\n"
    "v_mfma_f32_16x16x32_bf16 v[152:155], v[240:243], a[164:167], v[152:155]\n"
    "s_waitcnt lgkmcnt(6)\n"
    "v_cvt_pk_bf16_f32 v25, v122, v123\n"
    "v_mul_f32 v124, s68, v124\n"
    "v_mfma_f32_16x16x32_bf16 v[164:167], v[244:247], a[164:167], v[164:167]\n"
    "ds_read_b64_tr_b16 v[228:229], v5 offset:8200\n"
    "v_mul_f32 v132, s68, v132\n"
    "v_mfma_f32_16x16x32_bf16 v[160:163], v[244:247], a[160:163], v[160:163]\n"
    "ds_read_b64_tr_b16 v[230:231], v5 offset:8712\n"
    "v_mul_f32 v133, s68, v133\n"
    "v_mfma_f32_16x16x32_bf16 v[156:159], v[244:247], a[156:159], v[156:159]\n"
    "s_waitcnt lgkmcnt(6)\n"
    "v_cvt_pk_bf16_f32 v26, v132, v133\n"
    "v_mul_f32 v134, s68, v134\n"
    "v_mfma_f32_16x16x32_bf16 v[168:171], v[216:219], a[144:147], v[168:171]\n"
    "ds_read_b64_tr_b16 v[232:233], v4 offset:12288\n"
    "v_mul_f32 v135, s68, v135\n"
    "v_mfma_f32_16x16x32_bf16 v[172:175], v[216:219], a[148:151], v[172:175]\n"
    "ds_read_b64_tr_b16 v[234:235], v4 offset:12800\n"
    "v_cvt_pk_bf16_f32 v27, v134, v135\n"
    "v_mfma_f32_16x16x32_bf16 v[176:179], v[216:219], a[152:155], v[176:179]\n"
    "s_waitcnt lgkmcnt(6)\n"
    "v_mul_f32 v125, s68, v125\n"
    "v_cvt_pk_bf16_f32 v28, v124, v125\n"
    "v_mfma_f32_16x16x32_bf16 v[188:191], v[220:223], a[152:155], v[188:191]\n"
    "ds_read_b64_tr_b16 v[236:237], v4 offset:12296\n"
    "v_mul_f32 v126, s68, v126\n"
    "v_mfma_f32_16x16x32_bf16 v[184:187], v[220:223], a[148:151], v[184:187]\n"
    "ds_read_b64_tr_b16 v[238:239], v4 offset:12808\n"
    "v_mul_f32 v127, s68, v127\n"
    "v_mfma_f32_16x16x32_bf16 v[180:183], v[220:223], a[144:147], v[180:183]\n"
    "s_waitcnt lgkmcnt(6)\n"
    "v_cvt_pk_bf16_f32 v29, v126, v127\n"
    "v_mul_f32 v136, s68, v136\n"
    "v_mfma_f32_16x16x32_bf16 v[192:195], v[224:227], a[144:147], v[192:195]\n"
    "ds_read_b64_tr_b16 v[240:241], v5 offset:12288\n"
    "v_mul_f32 v137, s68, v137\n"
    "v_mfma_f32_16x16x32_bf16 v[196:199], v[224:227], a[148:151], v[196:199]\n"
    "ds_read_b64_tr_b16 v[242:243], v5 offset:12800\n"
    "v_cvt_pk_bf16_f32 v30, v136, v137\n"
    "v_mfma_f32_16x16x32_bf16 v[200:203], v[224:227], a[152:155], v[200:203]\n"
    "s_waitcnt lgkmcnt(6)\n"
    "v_mul_f32 v138, s68, v138\n"
    "v_mul_f32 v139, s68, v139\n"
    "v_mfma_f32_16x16x32_bf16 v[212:215], v[228:231], a[152:155], v[212:215]\n"
    "ds_read_b64_tr_b16 v[244:245], v5 offset:12296\n"
    "v_cvt_pk_bf16_f32 v31, v138, v139\n"
    "v_mfma_f32_16x16x32_bf16 v[208:211], v[228:231], a[148:151], v[208:211]\n"
    "ds_read_b64_tr_b16 v[246:247], v5 offset:12808\n"
    "v_mul_f32 v128, s68, v128\n"
    "v_mfma_f32_16x16x32_bf16 v[204:207], v[228:231], a[144:147], v[204:207]\n"
    "s_waitcnt lgkmcnt(6)\n"
    "v_mul_f32 v129, s68, v129\n"
    "v_cvt_pk_bf16_f32 v32, v128, v129\n"
    "v_add_u32 v4, 0x4000, v4\n"
    "v_mfma_f32_16x16x32_bf16 v[168:171], v[232:235], a[156:159], v[168:171]\n"
    "s_barrier\n"
    "ds_read_b64_tr_b16 v[216:217], v4 offset:0\n"
    "v_mul_f32 v130, s68, v130\n"
    "v_mfma_f32_16x16x32_bf16 v[172:175], v[232:235], a[160:163], v[172:175]\n"
    "ds_read_b64_tr_b16 v[218:219], v4 offset:512\n"
    "v_mul_f32 v131, s68, v131\n"
    "v_mfma_f32_16x16x32_bf16 v[176:179], v[232:235], a[164:167], v[176:179]\n"
    "s_waitcnt lgkmcnt(6)\n"
    "v_cvt_pk_bf16_f32 v33, v130, v131\n"
    "v_mul_f32 v140, s68, v140\n"
    "v_mfma_f32_16x16x32_bf16 v[188:191], v[236:239], a[164:167], v[188:191]\n"
    "ds_read_b64_tr_b16 v[220:221], v4 offset:8\n"
    "v_mul_f32 v141, s68, v141\n"
    "v_mfma_f32_16x16x32_bf16 v[184:187], v[236:239], a[160:163], v[184:187]\n"
    "ds_read_b64_tr_b16 v[222:223], v4 offset:520\n"
    "v_cvt_pk_bf16_f32 v34, v140, v141\n"
    "v_mfma_f32_16x16x32_bf16 v[180:183], v[236:239], a[156:159], v[180:183]\n"
    "s_waitcnt lgkmcnt(6)\n"
    "v_mul_f32 v142, s68, v142\n"
    "v_mul_f32 v143, s68, v143\n"
    "v_add_u32 v5, 0x4000, v5\n"
    "v_mfma_f32_16x16x32_bf16 v[192:195], v[240:243], a[156:159], v[192:195]\n"
    "ds_read_b64_tr_b16 v[224:225], v5 offset:0\n"
    "v_cvt_pk_bf16_f32 v35, v142, v143\n"
    "v_mfma_f32_16x16x32_bf16 v[196:199], v[240:243], a[160:163], v[196:199]\n"
    "ds_read_b64_tr_b16 v[226:227], v5 offset:512\n"
    "v_mul_f32 v144, s68, v144\n"
    "v_mfma_f32_16x16x32_bf16 v[200:203], v[240:243], a[164:167], v[200:203]\n"
    "s_waitcnt lgkmcnt(6)\n"
    "v_mul_f32 v145, s68, v145\n"
    "v_cvt_pk_bf16_f32 v36, v144, v145\n"
    "v_mfma_f32_16x16x32_bf16 v[212:215], v[244:247], a[164:167], v[212:215]\n"
    "ds_read_b64_tr_b16 v[228:229], v5 offset:8\n"
    "v_mul_f32 v146, s68, v146\n"
    "v_mfma_f32_16x16x32_bf16 v[208:211], v[244:247], a[160:163], v[208:211]\n"
    "ds_read_b64_tr_b16 v[230:231], v5 offset:520\n"
    "v_mul_f32 v147, s68, v147\n"
    "v_mfma_f32_16x16x32_bf16 v[204:207], v[244:247], a[156:159], v[204:207]\n"
    "s_waitcnt lgkmcnt(6)\n"
    "v_cvt_pk_bf16_f32 v37, v146, v147\n"
    "v_mul_f32 v156, s68, v156\n"
    "v_mfma_f32_16x16x32_bf16 v[72:75], v[216:219], a[144:147], v[72:75]\n"
    "ds_read_b64_tr_b16 v[232:233], v4 offset:4096\n"
    "v_mul_f32 v157, s68, v157\n"
    "v_mfma_f32_16x16x32_bf16 v[76:79], v[216:219], a[148:151], v[76:79]\n"
    "ds_read_b64_tr_b16 v[234:235], v4 offset:4608\n"
    "v_cvt_pk_bf16_f32 v38, v156, v157\n"
    "v_mfma_f32_16x16x32_bf16 v[80:83], v[216:219], a[152:155], v[80:83]\n"
    "s_waitcnt lgkmcnt(6)\n"
    "v_mul_f32 v158, s68, v158\n"
    "v_mul_f32 v159, s68, v159\n"
    "v_mfma_f32_16x16x32_bf16 v[104:107], v[220:223], a[152:155], v[104:107]\n"
    "ds_read_b64_tr_b16 v[236:237], v4 offset:4104\n"
    "v_cvt_pk_bf16_f32 v39, v158, v159\n"
    "v_mfma_f32_16x16x32_bf16 v[100:103], v[220:223], a[148:151], v[100:103]\n"
    "ds_read_b64_tr_b16 v[238:239], v4 offset:4616\n"
    "v_mul_f32 v148, s68, v148\n"
    "v_mfma_f32_16x16x32_bf16 v[96:99], v[220:223], a[144:147], v[96:99]\n"
    "s_waitcnt lgkmcnt(6)\n"
    "v_mul_f32 v149, s68, v149\n"
    "v_cvt_pk_bf16_f32 v40, v148, v149\n"
    "v_mfma_f32_16x16x32_bf16 v[84:87], v[224:227], a[144:147], v[84:87]\n"
    "ds_read_b64_tr_b16 v[240:241], v5 offset:4096\n"
    "v_mul_f32 v150, s68, v150\n"
    "v_mfma_f32_16x16x32_bf16 v[88:91], v[224:227], a[148:151], v[88:91]\n"
    "ds_read_b64_tr_b16 v[242:243], v5 offset:4608\n"
    "v_mul_f32 v151, s68, v151\n"
    "v_mfma_f32_16x16x32_bf16 v[92:95], v[224:227], a[152:155], v[92:95]\n"
    "s_waitcnt lgkmcnt(6)\n"
    "v_cvt_pk_bf16_f32 v41, v150, v151\n"
    "v_mul_f32 v160, s68, v160\n"
    "v_mfma_f32_16x16x32_bf16 v[116:119], v[228:231], a[152:155], v[116:119]\n"
    "ds_read_b64_tr_b16 v[244:245], v5 offset:4104\n"
    "v_mul_f32 v161, s68, v161\n"
    "v_mfma_f32_16x16x32_bf16 v[112:115], v[228:231], a[148:151], v[112:115]\n"
    "ds_read_b64_tr_b16 v[246:247], v5 offset:4616\n"
    "v_cvt_pk_bf16_f32 v42, v160, v161\n"
    "v_mfma_f32_16x16x32_bf16 v[108:111], v[228:231], a[144:147], v[108:111]\n"
    "s_waitcnt lgkmcnt(6)\n"
    "v_mul_f32 v162, s68, v162\n"
    "v_mul_f32 v163, s68, v163\n"
    "v_mfma_f32_16x16x32_bf16 v[72:75], v[232:235], a[156:159], v[72:75]\n"
    "ds_read_b64_tr_b16 v[216:217], v4 offset:8192\n"
    "v_cvt_pk_bf16_f32 v43, v162, v163\n"
    "v_mfma_f32_16x16x32_bf16 v[76:79], v[232:235], a[160:163], v[76:79]\n"
    "ds_read_b64_tr_b16 v[218:219], v4 offset:8704\n"
    "v_mul_f32 v152, s68, v152\n"
    "v_mfma_f32_16x16x32_bf16 v[80:83], v[232:235], a[164:167], v[80:83]\n"
    "s_waitcnt lgkmcnt(6)\n"
    "v_mul_f32 v153, s68, v153\n"
    "v_cvt_pk_bf16_f32 v44, v152, v153\n"
    "v_mfma_f32_16x16x32_bf16 v[104:107], v[236:239], a[164:167], v[104:107]\n"
    "ds_read_b64_tr_b16 v[220:221], v4 offset:8200\n"
    "v_mul_f32 v154, s68, v154\n"
    "v_mfma_f32_16x16x32_bf16 v[100:103], v[236:239], a[160:163], v[100:103]\n"
    "ds_read_b64_tr_b16 v[222:223], v4 offset:8712\n"
    "v_mul_f32 v155, s68, v155\n"
    "v_mfma_f32_16x16x32_bf16 v[96:99], v[236:239], a[156:159], v[96:99]\n"
    "s_waitcnt lgkmcnt(6)\n"
    "v_cvt_pk_bf16_f32 v45, v154, v155\n"
    "v_mul_f32 v164, s68, v164\n"
    "v_mfma_f32_16x16x32_bf16 v[84:87], v[240:243], a[156:159], v[84:87]\n"
    "ds_read_b64_tr_b16 v[224:225], v5 offset:8192\n"
    "v_mul_f32 v165, s68, v165\n"
    "v_mfma_f32_16x16x32_bf16 v[88:91], v[240:243], a[160:163], v[88:91]\n"
    "ds_read_b64_tr_b16 v[226:227], v5 offset:8704\n"
    "v_cvt_pk_bf16_f32 v46, v164, v165\n"
    "v_mfma_f32_16x16x32_bf16 v[92:95], v[240:243], a[164:167], v[92:95]\n"
    "s_waitcnt lgkmcnt(6)\n"
    "v_mul_f32 v166, s68, v166\n"
    "v_mul_f32 v167, s68, v167\n"
    "v_mfma_f32_16x16x32_bf16 v[116:119], v[244:247], a[164:167], v[116:119]\n"
    "ds_read_b64_tr_b16 v[228:229], v5 offset:8200\n"
    "v_cvt_pk_bf16_f32 v47, v166, v167\n"
    "v_mfma_f32_16x16x32_bf16 v[112:115], v[244:247], a[160:163], v[112:115]\n"
    "ds_read_b64_tr_b16 v[230:231], v5 offset:8712\n"
    "v_mul_f32 v168, s68, v168\n"
    "v_mfma_f32_16x16x32_bf16 v[108:111], v[244:247], a[156:159], v[108:111]\n"
    "s_waitcnt lgkmcnt(6)\n"
    "v_mul_f32 v169, s68, v169\n"
    "v_cvt_pk_bf16_f32 v48, v168, v169\n"
    "v_mfma_f32_16x16x32_bf16 a[208:211], v[216:219], a[0:3], 0\n"
    "ds_read_b64_tr_b16 v[232:233], v4 offset:12288\n"
    "v_mul_f32 v170, s68, v170\n"
    "v_mfma_f32_16x16x32_bf16 a[212:215], v[216:219], a[4:7], 0\n"
    "ds_read_b64_tr_b16 v[234:235], v4 offset:12800\n"
    "v_mul_f32 v171, s68, v171\n"
    "v_mfma_f32_16x16x32_bf16 a[216:219], v[216:219], a[8:11], 0\n"
    "s_waitcnt lgkmcnt(6)\n"
    "v_cvt_pk_bf16_f32 v49, v170, v171\n"
    "v_mul_f32 v180, s68, v180\n"
    "v_mfma_f32_16x16x32_bf16 a[228:231], v[220:223], a[8:11], 0\n"
    "ds_read_b64_tr_b16 v[236:237], v4 offset:12296\n"
    "v_mul_f32 v181, s68, v181\n"
    "v_mfma_f32_16x16x32_bf16 a[224:227], v[220:223], a[4:7], 0\n"
    "ds_read_b64_tr_b16 v[238:239], v4 offset:12808\n"
    "v_cvt_pk_bf16_f32 v50, v180, v181\n"
    "v_mfma_f32_16x16x32_bf16 a[220:223], v[220:223], a[0:3], 0\n"
    "s_waitcnt lgkmcnt(6)\n"
    "v_mul_f32 v182, s68, v182\n"
    "v_mul_f32 v183, s68, v183\n"
    "v_mfma_f32_16x16x32_bf16 a[232:235], v[224:227], a[0:3], 0\n"
    "ds_read_b64_tr_b16 v[240:241], v5 offset:12288\n"
    "v_cvt_pk_bf16_f32 v51, v182, v183\n"
    "v_mfma_f32_16x16x32_bf16 a[236:239], v[224:227], a[4:7], 0\n"
    "ds_read_b64_tr_b16 v[242:243], v5 offset:12800\n"
    "v_mul_f32 v172, s68, v172\n"
    "v_mfma_f32_16x16x32_bf16 a[240:243], v[224:227], a[8:11], 0\n"
    "s_waitcnt lgkmcnt(6)\n"
    "v_mul_f32 v173, s68, v173\n"
    "v_cvt_pk_bf16_f32 v52, v172, v173\n"
    "v_mfma_f32_16x16x32_bf16 a[252:255], v[228:231], a[8:11], 0\n"
    "ds_read_b64_tr_b16 v[244:245], v5 offset:12296\n"
    "v_mul_f32 v174, s68, v174\n"
    "v_mfma_f32_16x16x32_bf16 a[248:251], v[228:231], a[4:7], 0\n"
    "ds_read_b64_tr_b16 v[246:247], v5 offset:12808\n"
    "v_mul_f32 v175, s68, v175\n"
    "v_mfma_f32_16x16x32_bf16 a[244:247], v[228:231], a[0:3], 0\n"
    "s_waitcnt lgkmcnt(6)\n"
    "v_cvt_pk_bf16_f32 v53, v174, v175\n"
    "v_mul_f32 v184, s68, v184\n"
    "v_add_u32 v4, 0x4000, v4\n"
    "v_mfma_f32_16x16x32_bf16 a[208:211], v[232:235], a[12:15], a[208:211]\n"
    "s_barrier\n"
    "ds_read_b64_tr_b16 v[216:217], v4 offset:0\n"
    "v_mul_f32 v185, s68, v185\n"
    "v_mfma_f32_16x16x32_bf16 a[212:215], v[232:235], a[16:19], a[212:215]\n"
    "ds_read_b64_tr_b16 v[218:219], v4 offset:512\n"
    "v_cvt_pk_bf16_f32 v54, v184, v185\n"
    "v_mfma_f32_16x16x32_bf16 a[216:219], v[232:235], a[20:23], a[216:219]\n"
    "s_waitcnt lgkmcnt(6)\n"
    "v_mul_f32 v186, s68, v186\n"
    "v_mul_f32 v187, s68, v187\n"
    "v_mfma_f32_16x16x32_bf16 a[228:231], v[236:239], a[20:23], a[228:231]\n"
    "ds_read_b64_tr_b16 v[220:221], v4 offset:8\n"
    "v_cvt_pk_bf16_f32 v55, v186, v187\n"
    "v_mfma_f32_16x16x32_bf16 a[224:227], v[236:239], a[16:19], a[224:227]\n"
    "ds_read_b64_tr_b16 v[222:223], v4 offset:520\n"
    "v_mul_f32 v176, s68, v176\n"
    "v_mfma_f32_16x16x32_bf16 a[220:223], v[236:239], a[12:15], a[220:223]\n"
    "s_waitcnt lgkmcnt(6)\n"
    "v_mul_f32 v177, s68, v177\n"
    "v_cvt_pk_bf16_f32 v56, v176, v177\n"
    "v_add_u32 v5, 0x4000, v5\n"
    "v_mfma_f32_16x16x32_bf16 a[232:235], v[240:243], a[12:15], a[232:235]\n"
    "ds_read_b64_tr_b16 v[224:225], v5 offset:0\n"
    "v_mul_f32 v178, s68, v178\n"
    "v_mfma_f32_16x16x32_bf16 a[236:239], v[240:243], a[16:19], a[236:239]\n"
    "ds_read_b64_tr_b16 v[226:227], v5 offset:512\n"
    "v_mul_f32 v179, s68, v179\n"
    "v_mfma_f32_16x16x32_bf16 a[240:243], v[240:243], a[20:23], a[240:243]\n"
    "s_waitcnt lgkmcnt(6)\n"
    "v_cvt_pk_bf16_f32 v57, v178, v179\n"
    "v_mul_f32 v188, s68, v188\n"
    "v_mfma_f32_16x16x32_bf16 a[252:255], v[244:247], a[20:23], a[252:255]\n"
    "ds_read_b64_tr_b16 v[228:229], v5 offset:8\n"
    "v_mul_f32 v189, s68, v189\n"
    "v_mfma_f32_16x16x32_bf16 a[248:251], v[244:247], a[16:19], a[248:251]\n"
    "ds_read_b64_tr_b16 v[230:231], v5 offset:520\n"
    "v_cvt_pk_bf16_f32 v58, v188, v189\n"
    "v_mfma_f32_16x16x32_bf16 a[244:247], v[244:247], a[12:15], a[244:247]\n"
    "s_waitcnt lgkmcnt(6)\n"
    "v_mul_f32 v190, s68, v190\n"
    "v_mul_f32 v191, s68, v191\n"
    "v_mfma_f32_16x16x32_bf16 a[208:211], v[216:219], a[24:27], a[208:211]\n"
    "ds_read_b64_tr_b16 v[232:233], v4 offset:4096\n"
    "v_cvt_pk_bf16_f32 v59, v190, v191\n"
    "v_mfma_f32_16x16x32_bf16 a[212:215], v[216:219], a[28:31], a[212:215]\n"
    "ds_read_b64_tr_b16 v[234:235], v4 offset:4608\n"
    "v_mul_f32 v192, s68, v192\n"
    "v_mfma_f32_16x16x32_bf16 a[216:219], v[216:219], a[32:35], a[216:219]\n"
    "s_waitcnt lgkmcnt(6)\n"
    "v_mul_f32 v193, s68, v193\n"
    "v_cvt_pk_bf16_f32 v60, v192, v193\n"
    "v_mfma_f32_16x16x32_bf16 a[228:231], v[220:223], a[32:35], a[228:231]\n"
    "ds_read_b64_tr_b16 v[236:237], v4 offset:4104\n"
    "v_mul_f32 v194, s68, v194\n"
    "v_mfma_f32_16x16x32_bf16 a[224:227], v[220:223], a[28:31], a[224:227]\n"
    "ds_read_b64_tr_b16 v[238:239], v4 offset:4616\n"
    "v_mul_f32 v195, s68, v195\n"
    "v_mfma_f32_16x16x32_bf16 a[220:223], v[220:223], a[24:27], a[220:223]\n"
    "s_waitcnt lgkmcnt(6)\n"
    "v_cvt_pk_bf16_f32 v61, v194, v195\n"
    "v_mul_f32 v204, s68, v204\n"
    "v_mfma_f32_16x16x32_bf16 a[232:235], v[224:227], a[24:27], a[232:235]\n"
    "ds_read_b64_tr_b16 v[240:241], v5 offset:4096\n"
    "v_mul_f32 v205, s68, v205\n"
    "v_mfma_f32_16x16x32_bf16 a[236:239], v[224:227], a[28:31], a[236:239]\n"
    "ds_read_b64_tr_b16 v[242:243], v5 offset:4608\n"
    "v_cvt_pk_bf16_f32 v62, v204, v205\n"
    "v_mfma_f32_16x16x32_bf16 a[240:243], v[224:227], a[32:35], a[240:243]\n"
    "s_waitcnt lgkmcnt(6)\n"
    "v_mul_f32 v206, s68, v206\n"
    "v_mul_f32 v207, s68, v207\n"
    "v_mfma_f32_16x16x32_bf16 a[252:255], v[228:231], a[32:35], a[252:255]\n"
    "ds_read_b64_tr_b16 v[244:245], v5 offset:4104\n"
    "v_cvt_pk_bf16_f32 v63, v206, v207\n"
    "v_mfma_f32_16x16x32_bf16 a[248:251], v[228:231], a[28:31], a[248:251]\n"
    "ds_read_b64_tr_b16 v[246:247], v5 offset:4616\n"
    "v_mul_f32 v196, s68, v196\n"
    "v_mfma_f32_16x16x32_bf16 a[244:247], v[228:231], a[24:27], a[244:247]\n"
    "s_waitcnt lgkmcnt(6)\n"
    "v_mul_f32 v197, s68, v197\n"
    "v_cvt_pk_bf16_f32 v64, v196, v197\n"
    "v_mfma_f32_16x16x32_bf16 a[208:211], v[232:235], a[36:39], a[208:211]\n"
    "ds_read_b64_tr_b16 v[216:217], v4 offset:8192\n"
    "v_mul_f32 v198, s68, v198\n"
    "v_mfma_f32_16x16x32_bf16 a[212:215], v[232:235], a[40:43], a[212:215]\n"
    "ds_read_b64_tr_b16 v[218:219], v4 offset:8704\n"
    "v_mul_f32 v199, s68, v199\n"
    "v_mfma_f32_16x16x32_bf16 a[216:219], v[232:235], a[44:47], a[216:219]\n"
    "s_waitcnt lgkmcnt(6)\n"
    "v_cvt_pk_bf16_f32 v65, v198, v199\n"
    "v_mul_f32 v208, s68, v208\n"
    "v_mfma_f32_16x16x32_bf16 a[228:231], v[236:239], a[44:47], a[228:231]\n"
    "ds_read_b64_tr_b16 v[220:221], v4 offset:8200\n"
    "v_mul_f32 v209, s68, v209\n"
    "v_mfma_f32_16x16x32_bf16 a[224:227], v[236:239], a[40:43], a[224:227]\n"
    "ds_read_b64_tr_b16 v[222:223], v4 offset:8712\n"
    "v_cvt_pk_bf16_f32 v66, v208, v209\n"
    "v_mfma_f32_16x16x32_bf16 a[220:223], v[236:239], a[36:39], a[220:223]\n"
    "s_waitcnt lgkmcnt(6)\n"
    "v_mul_f32 v210, s68, v210\n"
    "v_mul_f32 v211, s68, v211\n"
    "v_mfma_f32_16x16x32_bf16 a[232:235], v[240:243], a[36:39], a[232:235]\n"
    "ds_read_b64_tr_b16 v[224:225], v5 offset:8192\n"
    "v_cvt_pk_bf16_f32 v67, v210, v211\n"
    "v_mfma_f32_16x16x32_bf16 a[236:239], v[240:243], a[40:43], a[236:239]\n"
    "ds_read_b64_tr_b16 v[226:227], v5 offset:8704\n"
    "v_mul_f32 v200, s68, v200\n"
    "v_mfma_f32_16x16x32_bf16 a[240:243], v[240:243], a[44:47], a[240:243]\n"
    "s_waitcnt lgkmcnt(6)\n"
    "v_mul_f32 v201, s68, v201\n"
    "v_cvt_pk_bf16_f32 v68, v200, v201\n"
    "v_mfma_f32_16x16x32_bf16 a[252:255], v[244:247], a[44:47], a[252:255]\n"
    "ds_read_b64_tr_b16 v[228:229], v5 offset:8200\n"
    "v_mul_f32 v202, s68, v202\n"
    "v_mfma_f32_16x16x32_bf16 a[248:251], v[244:247], a[40:43], a[248:251]\n"
    "ds_read_b64_tr_b16 v[230:231], v5 offset:8712\n"
    "v_mul_f32 v203, s68, v203\n"
    "v_mfma_f32_16x16x32_bf16 a[244:247], v[244:247], a[36:39], a[244:247]\n"
    "s_waitcnt lgkmcnt(6)\n"
    "v_cvt_pk_bf16_f32 v69, v202, v203\n"
    "v_mul_f32 v212, s68, v212\n"
    "v_mfma_f32_16x16x32_bf16 a[208:211], v[216:219], a[48:51], a[208:211]\n"
    "ds_read_b64_tr_b16 v[232:233], v4 offset:12288\n"
    "v_mul_f32 v213, s68, v213\n"
    "v_mfma_f32_16x16x32_bf16 a[212:215], v[216:219], a[52:55], a[212:215]\n"
    "ds_read_b64_tr_b16 v[234:235], v4 offset:12800\n"
    "v_cvt_pk_bf16_f32 v70, v212, v213\n"
    "v_mfma_f32_16x16x32_bf16 a[216:219], v[216:219], a[56:59], a[216:219]\n"
    "s_waitcnt lgkmcnt(6)\n"
    "v_mul_f32 v214, s68, v214\n"
    "v_mul_f32 v215, s68, v215\n"
    "v_mfma_f32_16x16x32_bf16 a[228:231], v[220:223], a[56:59], a[228:231]\n"
    "ds_read_b64_tr_b16 v[236:237], v4 offset:12296\n"
    "v_cvt_pk_bf16_f32 v71, v214, v215\n"
    "v_mfma_f32_16x16x32_bf16 a[224:227], v[220:223], a[52:55], a[224:227]\n"
    "ds_read_b64_tr_b16 v[238:239], v4 offset:12808\n"
    "v_mul_f32 v72, s68, v72\n"
    "v_mfma_f32_16x16x32_bf16 a[220:223], v[220:223], a[48:51], a[220:223]\n"
    "s_waitcnt lgkmcnt(6)\n"
    "v_mul_f32 v73, s68, v73\n"
    "v_cvt_pk_bf16_f32 v72, v72, v73\n"
    "v_mfma_f32_16x16x32_bf16 a[232:235], v[224:227], a[48:51], a[232:235]\n"
    "ds_read_b64_tr_b16 v[240:241], v5 offset:12288\n"
    "v_mul_f32 v74, s68, v74\n"
    "v_mfma_f32_16x16x32_bf16 a[236:239], v[224:227], a[52:55], a[236:239]\n"
    "ds_read_b64_tr_b16 v[242:243], v5 offset:12800\n"
    "v_mul_f32 v75, s68, v75\n"
    "v_mfma_f32_16x16x32_bf16 a[240:243], v[224:227], a[56:59], a[240:243]\n"
    "s_waitcnt lgkmcnt(6)\n"
    "v_cvt_pk_bf16_f32 v73, v74, v75\n"
    "v_mul_f32 v96, s68, v96\n"
    "v_mfma_f32_16x16x32_bf16 a[252:255], v[228:231], a[56:59], a[252:255]\n"
    "ds_read_b64_tr_b16 v[244:245], v5 offset:12296\n"
    "v_mul_f32 v97, s68, v97\n"
    "v_mfma_f32_16x16x32_bf16 a[248:251], v[228:231], a[52:55], a[248:251]\n"
    "ds_read_b64_tr_b16 v[246:247], v5 offset:12808\n"
    "v_cvt_pk_bf16_f32 v74, v96, v97\n"
    "v_mfma_f32_16x16x32_bf16 a[244:247], v[228:231], a[48:51], a[244:247]\n"
    "s_waitcnt lgkmcnt(6)\n"
    "v_mul_f32 v98, s68, v98\n"
    "v_mul_f32 v99, s68, v99\n"
    "v_add_u32 v4, 0x4000, v4\n"
    "v_mfma_f32_16x16x32_bf16 a[208:211], v[232:235], a[60:63], a[208:211]\n"
    "s_barrier\n"
    "ds_read_b64_tr_b16 v[216:217], v4 offset:0\n"
    "v_cvt_pk_bf16_f32 v75, v98, v99\n"
    "v_mfma_f32_16x16x32_bf16 a[212:215], v[232:235], a[64:67], a[212:215]\n"
    "ds_read_b64_tr_b16 v[218:219], v4 offset:512\n"
    "v_mul_f32 v76, s68, v76\n"
    "v_mfma_f32_16x16x32_bf16 a[216:219], v[232:235], a[68:71], a[216:219]\n"
    "s_waitcnt lgkmcnt(6)\n"
    "v_mul_f32 v77, s68, v77\n"
    "v_cvt_pk_bf16_f32 v76, v76, v77\n"
    "v_mfma_f32_16x16x32_bf16 a[228:231], v[236:239], a[68:71], a[228:231]\n"
    "ds_read_b64_tr_b16 v[220:221], v4 offset:8\n"
    "v_mul_f32 v78, s68, v78\n"
    "v_mfma_f32_16x16x32_bf16 a[224:227], v[236:239], a[64:67], a[224:227]\n"
    "ds_read_b64_tr_b16 v[222:223], v4 offset:520\n"
    "v_mul_f32 v79, s68, v79\n"
    "v_mfma_f32_16x16x32_bf16 a[220:223], v[236:239], a[60:63], a[220:223]\n"
    "s_waitcnt lgkmcnt(6)\n"
    "v_cvt_pk_bf16_f32 v77, v78, v79\n"
    "v_mul_f32 v100, s68, v100\n"
    "v_add_u32 v5, 0x4000, v5\n"
    "v_mfma_f32_16x16x32_bf16 a[232:235], v[240:243], a[60:63], a[232:235]\n"
    "ds_read_b64_tr_b16 v[224:225], v5 offset:0\n"
    "v_mul_f32 v101, s68, v101\n"
    "v_mfma_f32_16x16x32_bf16 a[236:239], v[240:243], a[64:67], a[236:239]\n"
    "ds_read_b64_tr_b16 v[226:227], v5 offset:512\n"
    "v_cvt_pk_bf16_f32 v78, v100, v101\n"
    "v_mfma_f32_16x16x32_bf16 a[240:243], v[240:243], a[68:71], a[240:243]\n"
    "s_waitcnt lgkmcnt(6)\n"
    "v_mul_f32 v102, s68, v102\n"
    "v_mul_f32 v103, s68, v103\n"
    "v_mfma_f32_16x16x32_bf16 a[252:255], v[244:247], a[68:71], a[252:255]\n"
    "ds_read_b64_tr_b16 v[228:229], v5 offset:8\n"
    "v_cvt_pk_bf16_f32 v79, v102, v103\n"
    "v_mfma_f32_16x16x32_bf16 a[248:251], v[244:247], a[64:67], a[248:251]\n"
    "ds_read_b64_tr_b16 v[230:231], v5 offset:520\n"
    "v_mul_f32 v80, s68, v80\n"
    "v_mfma_f32_16x16x32_bf16 a[244:247], v[244:247], a[60:63], a[244:247]\n"
    "s_waitcnt lgkmcnt(6)\n"
    "v_mul_f32 v81, s68, v81\n"
    "v_cvt_pk_bf16_f32 v80, v80, v81\n"
    "v_mfma_f32_16x16x32_bf16 a[208:211], v[216:219], a[72:75], a[208:211]\n"
    "ds_read_b64_tr_b16 v[232:233], v4 offset:4096\n"
    "v_mul_f32 v82, s68, v82\n"
    "v_mfma_f32_16x16x32_bf16 a[212:215], v[216:219], a[76:79], a[212:215]\n"
    "ds_read_b64_tr_b16 v[234:235], v4 offset:4608\n"
    "v_mul_f32 v83, s68, v83\n"
    "v_mfma_f32_16x16x32_bf16 a[216:219], v[216:219], a[80:83], a[216:219]\n"
    "s_waitcnt lgkmcnt(6)\n"
    "v_cvt_pk_bf16_f32 v81, v82, v83\n"
    "v_mul_f32 v104, s68, v104\n"
    "v_mfma_f32_16x16x32_bf16 a[228:231], v[220:223], a[80:83], a[228:231]\n"
    "ds_read_b64_tr_b16 v[236:237], v4 offset:4104\n"
    "v_mul_f32 v105, s68, v105\n"
    "v_mfma_f32_16x16x32_bf16 a[224:227], v[220:223], a[76:79], a[224:227]\n"
    "ds_read_b64_tr_b16 v[238:239], v4 offset:4616\n"
    "v_cvt_pk_bf16_f32 v82, v104, v105\n"
    "v_mfma_f32_16x16x32_bf16 a[220:223], v[220:223], a[72:75], a[220:223]\n"
    "s_waitcnt lgkmcnt(6)\n"
    "v_mul_f32 v106, s68, v106\n"
    "v_mul_f32 v107, s68, v107\n"
    "v_mfma_f32_16x16x32_bf16 a[232:235], v[224:227], a[72:75], a[232:235]\n"
    "ds_read_b64_tr_b16 v[240:241], v5 offset:4096\n"
    "v_cvt_pk_bf16_f32 v83, v106, v107\n"
    "v_mfma_f32_16x16x32_bf16 a[236:239], v[224:227], a[76:79], a[236:239]\n"
    "ds_read_b64_tr_b16 v[242:243], v5 offset:4608\n"
    "v_mul_f32 v84, s68, v84\n"
    "v_mfma_f32_16x16x32_bf16 a[240:243], v[224:227], a[80:83], a[240:243]\n"
    "s_waitcnt lgkmcnt(6)\n"
    "v_mul_f32 v85, s68, v85\n"
    "v_cvt_pk_bf16_f32 v84, v84, v85\n"
    "v_mfma_f32_16x16x32_bf16 a[252:255], v[228:231], a[80:83], a[252:255]\n"
    "ds_read_b64_tr_b16 v[244:245], v5 offset:4104\n"
    "v_mul_f32 v86, s68, v86\n"
    "v_mfma_f32_16x16x32_bf16 a[248:251], v[228:231], a[76:79], a[248:251]\n"
    "ds_read_b64_tr_b16 v[246:247], v5 offset:4616\n"
    "v_mul_f32 v87, s68, v87\n"
    "v_mfma_f32_16x16x32_bf16 a[244:247], v[228:231], a[72:75], a[244:247]\n"
    "s_waitcnt lgkmcnt(6)\n"
    "v_cvt_pk_bf16_f32 v85, v86, v87\n"
    "v_mul_f32 v108, s68, v108\n"
    "v_mfma_f32_16x16x32_bf16 a[208:211], v[232:235], a[84:87], a[208:211]\n"
    "ds_read_b64_tr_b16 v[216:217], v4 offset:8192\n"
    "v_mul_f32 v109, s68, v109\n"
    "v_mfma_f32_16x16x32_bf16 a[212:215], v[232:235], a[88:91], a[212:215]\n"
    "ds_read_b64_tr_b16 v[218:219], v4 offset:8704\n"
    "v_cvt_pk_bf16_f32 v86, v108, v109\n"
    "v_mfma_f32_16x16x32_bf16 a[216:219], v[232:235], a[92:95], a[216:219]\n"
    "s_waitcnt lgkmcnt(6)\n"
    "v_mul_f32 v110, s68, v110\n"
    "v_mul_f32 v111, s68, v111\n"
    "v_mfma_f32_16x16x32_bf16 a[228:231], v[236:239], a[92:95], a[228:231]\n"
    "ds_read_b64_tr_b16 v[220:221], v4 offset:8200\n"
    "v_cvt_pk_bf16_f32 v87, v110, v111\n"
    "v_mfma_f32_16x16x32_bf16 a[224:227], v[236:239], a[88:91], a[224:227]\n"
    "ds_read_b64_tr_b16 v[222:223], v4 offset:8712\n"
    "v_mul_f32 v88, s68, v88\n"
    "v_mfma_f32_16x16x32_bf16 a[220:223], v[236:239], a[84:87], a[220:223]\n"
    "s_waitcnt lgkmcnt(6)\n"
    "v_mul_f32 v89, s68, v89\n"
    "v_cvt_pk_bf16_f32 v88, v88, v89\n"
    "v_mfma_f32_16x16x32_bf16 a[232:235], v[240:243], a[84:87], a[232:235]\n"
    "ds_read_b64_tr_b16 v[224:225], v5 offset:8192\n"
    "v_mul_f32 v90, s68, v90\n"
    "v_mfma_f32_16x16x32_bf16 a[236:239], v[240:243], a[88:91], a[236:239]\n"
    "ds_read_b64_tr_b16 v[226:227], v5 offset:8704\n"
    "v_mul_f32 v91, s68, v91\n"
    "v_mfma_f32_16x16x32_bf16 a[240:243], v[240:243], a[92:95], a[240:243]\n"
    "s_waitcnt lgkmcnt(6)\n"
    "v_cvt_pk_bf16_f32 v89, v90, v91\n"
    "v_mul_f32 v112, s68, v112\n"
    "v_mfma_f32_16x16x32_bf16 a[252:255], v[244:247], a[92:95], a[252:255]\n"
    "ds_read_b64_tr_b16 v[228:229], v5 offset:8200\n"
    "v_mul_f32 v113, s68, v113\n"
    "v_mfma_f32_16x16x32_bf16 a[248:251], v[244:247], a[88:91], a[248:251]\n"
    "ds_read_b64_tr_b16 v[230:231], v5 offset:8712\n"
    "v_cvt_pk_bf16_f32 v90, v112, v113\n"
    "v_mfma_f32_16x16x32_bf16 a[244:247], v[244:247], a[84:87], a[244:247]\n"
    "s_waitcnt lgkmcnt(6)\n"
    "v_mul_f32 v114, s68, v114\n"
    "v_mul_f32 v115, s68, v115\n"
    "v_mfma_f32_16x16x32_bf16 a[208:211], v[216:219], a[96:99], a[208:211]\n"
    "ds_read_b64_tr_b16 v[232:233], v4 offset:12288\n"
    "v_cvt_pk_bf16_f32 v91, v114, v115\n"
    "v_mfma_f32_16x16x32_bf16 a[212:215], v[216:219], a[100:103], a[212:215]\n"
    "ds_read_b64_tr_b16 v[234:235], v4 offset:12800\n"
    "v_mul_f32 v92, s68, v92\n"
    "v_mfma_f32_16x16x32_bf16 a[216:219], v[216:219], a[104:107], a[216:219]\n"
    "s_waitcnt lgkmcnt(6)\n"
    "v_mul_f32 v93, s68, v93\n"
    "v_cvt_pk_bf16_f32 v92, v92, v93\n"
    "v_mfma_f32_16x16x32_bf16 a[228:231], v[220:223], a[104:107], a[228:231]\n"
    "ds_read_b64_tr_b16 v[236:237], v4 offset:12296\n"
    "v_mul_f32 v94, s68, v94\n"
    "v_mfma_f32_16x16x32_bf16 a[224:227], v[220:223], a[100:103], a[224:227]\n"
    "ds_read_b64_tr_b16 v[238:239], v4 offset:12808\n"
    "v_mul_f32 v95, s68, v95\n"
    "v_mfma_f32_16x16x32_bf16 a[220:223], v[220:223], a[96:99], a[220:223]\n"
    "s_waitcnt lgkmcnt(6)\n"
    "v_cvt_pk_bf16_f32 v93, v94, v95\n"
    "v_mul_f32 v116, s68, v116\n"
    "v_mfma_f32_16x16x32_bf16 a[232:235], v[224:227], a[96:99], a[232:235]\n"
    "ds_read_b64_tr_b16 v[240:241], v5 offset:12288\n"
    "v_mul_f32 v117, s68, v117\n"
    "v_mfma_f32_16x16x32_bf16 a[236:239], v[224:227], a[100:103], a[236:239]\n"
    "ds_read_b64_tr_b16 v[242:243], v5 offset:12800\n"
    "v_cvt_pk_bf16_f32 v94, v116, v117\n"
    "v_mfma_f32_16x16x32_bf16 a[240:243], v[224:227], a[104:107], a[240:243]\n"
    "s_waitcnt lgkmcnt(6)\n"
    "v_mul_f32 v118, s68, v118\n"
    "v_mul_f32 v119, s68, v119\n"
    "v_mfma_f32_16x16x32_bf16 a[252:255], v[228:231], a[104:107], a[252:255]\n"
    "ds_read_b64_tr_b16 v[244:245], v5 offset:12296\n"
    "v_cvt_pk_bf16_f32 v95, v118, v119\n"
    "v_mfma_f32_16x16x32_bf16 a[248:251], v[228:231], a[100:103], a[248:251]\n"
    "ds_read_b64_tr_b16 v[246:247], v5 offset:12808\n"
    "v_add_u32 v4, 0x4000, v4\n"
    "v_mfma_f32_16x16x32_bf16 a[244:247], v[228:231], a[96:99], a[244:247]\n"
    "s_waitcnt lgkmcnt(6)\n"
    "v_add_u32 v5, 0x4000, v5\n"
    "v_mov_b32 v249, 8192\n"
    "v_mfma_f32_16x16x32_bf16 a[208:211], v[232:235], a[108:111], a[208:211]\n"
    "s_barrier\n"
    "ds_read_b64_tr_b16 v[216:217], v4 offset:0\n"
    "v_mfma_f32_16x16x32_bf16 a[212:215], v[232:235], a[112:115], a[212:215]\n"
    "ds_read_b64_tr_b16 v[218:219], v4 offset:512\n"
    "v_mfma_f32_16x16x32_bf16 a[216:219], v[232:235], a[116:119], a[216:219]\n"
    "s_waitcnt lgkmcnt(6)\n"
    "v_mfma_f32_16x16x32_bf16 a[228:231], v[236:239], a[116:119], a[228:231]\n"
    "ds_read_b64_tr_b16 v[220:221], v4 offset:8\n"
    "v_mfma_f32_16x16x32_bf16 a[224:227], v[236:239], a[112:115], a[224:227]\n"
    "ds_read_b64_tr_b16 v[222:223], v4 offset:520\n"
    "v_mfma_f32_16x16x32_bf16 a[220:223], v[236:239], a[108:111], a[220:223]\n"
    "s_waitcnt lgkmcnt(6)\n"
    "v_mfma_f32_16x16x32_bf16 a[232:235], v[240:243], a[108:111], a[232:235]\n"
    "ds_read_b64_tr_b16 v[224:225], v5 offset:0\n"
    "v_mfma_f32_16x16x32_bf16 a[236:239], v[240:243], a[112:115], a[236:239]\n"
    "ds_read_b64_tr_b16 v[226:227], v5 offset:512\n"
    "v_mfma_f32_16x16x32_bf16 a[240:243], v[240:243], a[116:119], a[240:243]\n"
    "s_waitcnt lgkmcnt(6)\n"
    "v_mfma_f32_16x16x32_bf16 a[252:255], v[244:247], a[116:119], a[252:255]\n"
    "ds_read_b64_tr_b16 v[228:229], v5 offset:8\n"
    "v_mfma_f32_16x16x32_bf16 a[248:251], v[244:247], a[112:115], a[248:251]\n"
    "ds_read_b64_tr_b16 v[230:231], v5 offset:520\n"
    "v_mfma_f32_16x16x32_bf16 a[244:247], v[244:247], a[108:111], a[244:247]\n"
    "s_waitcnt lgkmcnt(6)\n"
    "v_mfma_f32_16x16x32_bf16 a[208:211], v[216:219], a[120:123], a[208:211]\n"
    "ds_read_b64_tr_b16 v[232:233], v4 offset:4096\n"
    "v_mfma_f32_16x16x32_bf16 a[212:215], v[216:219], a[124:127], a[212:215]\n"
    "ds_read_b64_tr_b16 v[234:235], v4 offset:4608\n"
    "v_mfma_f32_16x16x32_bf16 a[216:219], v[216:219], a[128:131], a[216:219]\n"
    "s_waitcnt lgkmcnt(6)\n"
    "v_mfma_f32_16x16x32_bf16 a[228:231], v[220:223], a[128:131], a[228:231]\n"
    "ds_read_b64_tr_b16 v[236:237], v4 offset:4104\n"
    "v_mfma_f32_16x16x32_bf16 a[224:227], v[220:223], a[124:127], a[224:227]\n"
    "ds_read_b64_tr_b16 v[238:239], v4 offset:4616\n"
    "v_mfma_f32_16x16x32_bf16 a[220:223], v[220:223], a[120:123], a[220:223]\n"
    "s_waitcnt lgkmcnt(6)\n"
    "v_mfma_f32_16x16x32_bf16 a[232:235], v[224:227], a[120:123], a[232:235]\n"
    "ds_read_b64_tr_b16 v[240:241], v5 offset:4096\n"
    "v_mfma_f32_16x16x32_bf16 a[236:239], v[224:227], a[124:127], a[236:239]\n"
    "ds_read_b64_tr_b16 v[242:243], v5 offset:4608\n"
    "v_mfma_f32_16x16x32_bf16 a[240:243], v[224:227], a[128:131], a[240:243]\n"
    "s_waitcnt lgkmcnt(6)\n"
    "v_mfma_f32_16x16x32_bf16 a[252:255], v[228:231], a[128:131], a[252:255]\n"
    "ds_read_b64_tr_b16 v[244:245], v5 offset:4104\n"
    "v_mfma_f32_16x16x32_bf16 a[248:251], v[228:231], a[124:127], a[248:251]\n"
    "ds_read_b64_tr_b16 v[246:247], v5 offset:4616\n"
    "v_mfma_f32_16x16x32_bf16 a[244:247], v[228:231], a[120:123], a[244:247]\n"
    "s_waitcnt lgkmcnt(6)\n"
    "v_mfma_f32_16x16x32_bf16 a[208:211], v[232:235], a[132:135], a[208:211]\n"
    "ds_read_b64_tr_b16 v[216:217], v4 offset:8192\n"
    "v_mfma_f32_16x16x32_bf16 a[212:215], v[232:235], a[136:139], a[212:215]\n"
    "ds_read_b64_tr_b16 v[218:219], v4 offset:8704\n"
    "v_mfma_f32_16x16x32_bf16 a[216:219], v[232:235], a[140:143], a[216:219]\n"
    "s_waitcnt lgkmcnt(6)\n"
    "v_mfma_f32_16x16x32_bf16 a[228:231], v[236:239], a[140:143], a[228:231]\n"
    "ds_read_b64_tr_b16 v[220:221], v4 offset:8200\n"
    "v_mfma_f32_16x16x32_bf16 a[224:227], v[236:239], a[136:139], a[224:227]\n"
    "ds_read_b64_tr_b16 v[222:223], v4 offset:8712\n"
    "v_mfma_f32_16x16x32_bf16 a[220:223], v[236:239], a[132:135], a[220:223]\n"
    "s_waitcnt lgkmcnt(6)\n"
    "v_mfma_f32_16x16x32_bf16 a[232:235], v[240:243], a[132:135], a[232:235]\n"
    "ds_read_b64_tr_b16 v[224:225], v5 offset:8192\n"
    "v_mfma_f32_16x16x32_bf16 a[236:239], v[240:243], a[136:139], a[236:239]\n"
    "ds_read_b64_tr_b16 v[226:227], v5 offset:8704\n"
    "v_mfma_f32_16x16x32_bf16 a[240:243], v[240:243], a[140:143], a[240:243]\n"
    "s_waitcnt lgkmcnt(6)\n"
    "v_mfma_f32_16x16x32_bf16 a[252:255], v[244:247], a[140:143], a[252:255]\n"
    "ds_read_b64_tr_b16 v[228:229], v5 offset:8200\n"
    "v_mfma_f32_16x16x32_bf16 a[248:251], v[244:247], a[136:139], a[248:251]\n"
    "ds_read_b64_tr_b16 v[230:231], v5 offset:8712\n"
    "v_mfma_f32_16x16x32_bf16 a[244:247], v[244:247], a[132:135], a[244:247]\n"
    "s_waitcnt lgkmcnt(6)\n"
    "v_mfma_f32_16x16x32_bf16 a[208:211], v[216:219], a[144:147], a[208:211]\n"
    "ds_read_b64_tr_b16 v[232:233], v4 offset:12288\n"
    "v_mfma_f32_16x16x32_bf16 a[212:215], v[216:219], a[148:151], a[212:215]\n"
    "ds_read_b64_tr_b16 v[234:235], v4 offset:12800\n"
    "v_mfma_f32_16x16x32_bf16 a[216:219], v[216:219], a[152:155], a[216:219]\n"
    "s_waitcnt lgkmcnt(6)\n"
    "v_mfma_f32_16x16x32_bf16 a[228:231], v[220:223], a[152:155], a[228:231]\n"
    "ds_read_b64_tr_b16 v[236:237], v4 offset:12296\n"
    "v_mfma_f32_16x16x32_bf16 a[224:227], v[220:223], a[148:151], a[224:227]\n"
    "ds_read_b64_tr_b16 v[238:239], v4 offset:12808\n"
    "v_xor_b32 v4, 64, v6\n"
    "v_mfma_f32_16x16x32_bf16 a[220:223], v[220:223], a[144:147], a[220:223]\n"
    "s_waitcnt lgkmcnt(6)\n"
    "v_mfma_f32_16x16x32_bf16 a[232:235], v[224:227], a[144:147], a[232:235]\n"
    "ds_read_b64_tr_b16 v[240:241], v5 offset:12288\n"
    "v_mfma_f32_16x16x32_bf16 a[236:239], v[224:227], a[148:151], a[236:239]\n"
    "ds_read_b64_tr_b16 v[242:243], v5 offset:12800\n"
    "v_mfma_f32_16x16x32_bf16 a[240:243], v[224:227], a[152:155], a[240:243]\n"
    "s_waitcnt lgkmcnt(6)\n"
    "v_mfma_f32_16x16x32_bf16 a[252:255], v[228:231], a[152:155], a[252:255]\n"
    "ds_read_b64_tr_b16 v[244:245], v5 offset:12296\n"
    "v_mfma_f32_16x16x32_bf16 a[248:251], v[228:231], a[148:151], a[248:251]\n"
    "ds_read_b64_tr_b16 v[246:247], v5 offset:12808\n"
    "v_xor_b32 v5, 64, v7\n"
    "v_mfma_f32_16x16x32_bf16 a[244:247], v[228:231], a[144:147], a[244:247]\n"
    "s_waitcnt lgkmcnt(6)\n"
    "v_mfma_f32_16x16x32_bf16 a[208:211], v[232:235], a[156:159], a[208:211]\n"
    "s_barrier\n"
    "ds_read_b128 v[216:219], v2 offset:0\n"
    "v_mfma_f32_16x16x32_bf16 a[212:215], v[232:235], a[160:163], a[212:215]\n"
    "v_mfma_f32_16x16x32_bf16 a[216:219], v[232:235], a[164:167], a[216:219]\n"
    "s_waitcnt lgkmcnt(5)\n"
    "v_mfma_f32_16x16x32_bf16 a[228:231], v[236:239], a[164:167], a[228:231]\n"
    "ds_read_b128 v[220:223], v2 offset:2048\n"
    "v_mfma_f32_16x16x32_bf16 a[224:227], v[236:239], a[160:163], a[224:227]\n"
    "v_accvgpr_read_b32 v15, a208\n"
    "v_accvgpr_read_b32 v16, a209\n"
    "v_mfma_f32_16x16x32_bf16 a[220:223], v[236:239], a[156:159], a[220:223]\n"
    "s_waitcnt lgkmcnt(4)\n"
    "v_mul_f32 v15, s68, v15\n"
    "v_mul_f32 v16, s68, v16\n"
    "v_mfma_f32_16x16x32_bf16 a[232:235], v[240:243], a[156:159], a[232:235]\n"
    "ds_read_b128 v[224:227], v3 offset:0\n"
    "v_cvt_pk_bf16_f32 v96, v15, v16\n"
    "v_mfma_f32_16x16x32_bf16 a[236:239], v[240:243], a[160:163], a[236:239]\n"
    "v_accvgpr_read_b32 v17, a210\n"
    "v_accvgpr_read_b32 v18, a211\n"
    "v_mfma_f32_16x16x32_bf16 a[240:243], v[240:243], a[164:167], a[240:243]\n"
    "s_waitcnt lgkmcnt(3)\n"
    "v_mul_f32 v17, s68, v17\n"
    "v_mul_f32 v18, s68, v18\n"
    "v_mfma_f32_16x16x32_bf16 a[252:255], v[244:247], a[164:167], a[252:255]\n"
    "ds_read_b128 v[228:231], v3 offset:2048\n"
    "v_cvt_pk_bf16_f32 v97, v17, v18\n"
    "v_mfma_f32_16x16x32_bf16 a[248:251], v[244:247], a[160:163], a[248:251]\n"
    "v_accvgpr_read_b32 v19, a220\n"
    "v_accvgpr_read_b32 v20, a221\n"
    "v_mfma_f32_16x16x32_bf16 a[244:247], v[244:247], a[156:159], a[244:247]\n"
    "s_waitcnt lgkmcnt(3)\n"
    "v_mul_f32 v19, s68, v19\n"
    "v_mul_f32 v20, s68, v20\n"
    "v_mfma_f32_16x16x32_bf16 v[120:123], v[216:219], v[24:27], 0\n"
    "ds_read_b128 v[232:235], v2 offset:4096\n"
    "v_cvt_pk_bf16_f32 v98, v19, v20\n"
    "v_mfma_f32_16x16x32_bf16 v[124:127], v[216:219], v[28:31], 0\n"
    "v_accvgpr_read_b32 v21, a222\n"
    "v_accvgpr_read_b32 v22, a223\n"
    "v_mfma_f32_16x16x32_bf16 v[128:131], v[216:219], v[32:35], 0\n"
    "s_waitcnt lgkmcnt(3)\n"
    "v_mul_f32 v21, s68, v21\n"
    "v_mul_f32 v22, s68, v22\n"
    "v_mfma_f32_16x16x32_bf16 v[140:143], v[220:223], v[32:35], 0\n"
    "ds_read_b128 v[236:239], v2 offset:6144\n"
    "v_cvt_pk_bf16_f32 v99, v21, v22\n"
    "v_mfma_f32_16x16x32_bf16 v[136:139], v[220:223], v[28:31], 0\n"
    "v_accvgpr_read_b32 v15, a212\n"
    "v_accvgpr_read_b32 v16, a213\n"
    "v_mfma_f32_16x16x32_bf16 v[132:135], v[220:223], v[24:27], 0\n"
    "s_waitcnt lgkmcnt(3)\n"
    "v_mul_f32 v15, s68, v15\n"
    "v_mul_f32 v16, s68, v16\n"
    "v_mfma_f32_16x16x32_bf16 v[120:123], v[224:227], v[36:39], v[120:123]\n"
    "ds_read_b128 v[240:243], v3 offset:4096\n"
    "v_cvt_pk_bf16_f32 v100, v15, v16\n"
    "v_mfma_f32_16x16x32_bf16 v[124:127], v[224:227], v[40:43], v[124:127]\n"
    "v_accvgpr_read_b32 v17, a214\n"
    "v_accvgpr_read_b32 v18, a215\n"
    "v_mfma_f32_16x16x32_bf16 v[128:131], v[224:227], v[44:47], v[128:131]\n"
    "s_waitcnt lgkmcnt(3)\n"
    "v_mul_f32 v17, s68, v17\n"
    "v_mul_f32 v18, s68, v18\n"
    "v_mfma_f32_16x16x32_bf16 v[140:143], v[228:231], v[44:47], v[140:143]\n"
    "ds_read_b128 v[244:247], v3 offset:6144\n"
    "v_cvt_pk_bf16_f32 v101, v17, v18\n"
    "v_mfma_f32_16x16x32_bf16 v[136:139], v[228:231], v[40:43], v[136:139]\n"
    "v_accvgpr_read_b32 v19, a224\n"
    "v_accvgpr_read_b32 v20, a225\n"
    "v_mfma_f32_16x16x32_bf16 v[132:135], v[228:231], v[36:39], v[132:135]\n"
    "s_waitcnt lgkmcnt(3)\n"
    "v_mul_f32 v19, s68, v19\n"
    "v_mul_f32 v20, s68, v20\n"
    "v_mfma_f32_16x16x32_bf16 v[120:123], v[232:235], v[48:51], v[120:123]\n"
    "ds_read_b128 v[216:219], v2 offset:8192\n"
    "v_cvt_pk_bf16_f32 v102, v19, v20\n"
    "v_mfma_f32_16x16x32_bf16 v[124:127], v[232:235], v[52:55], v[124:127]\n"
    "v_accvgpr_read_b32 v21, a226\n"
    "v_accvgpr_read_b32 v22, a227\n"
    "v_mfma_f32_16x16x32_bf16 v[128:131], v[232:235], v[56:59], v[128:131]\n"
    "s_waitcnt lgkmcnt(3)\n"
    "v_mul_f32 v21, s68, v21\n"
    "v_mul_f32 v22, s68, v22\n"
    "v_mfma_f32_16x16x32_bf16 v[140:143], v[236:239], v[56:59], v[140:143]\n"
    "ds_read_b128 v[220:223], v2 offset:10240\n"
    "v_cvt_pk_bf16_f32 v103, v21, v22\n"
    "v_mfma_f32_16x16x32_bf16 v[136:139], v[236:239], v[52:55], v[136:139]\n"
    "v_accvgpr_read_b32 v15, a216\n"
    "v_accvgpr_read_b32 v16, a217\n"
    "v_mfma_f32_16x16x32_bf16 v[132:135], v[236:239], v[48:51], v[132:135]\n"
    "s_waitcnt lgkmcnt(3)\n"
    "v_mul_f32 v15, s68, v15\n"
    "v_mul_f32 v16, s68, v16\n"
    "v_mfma_f32_16x16x32_bf16 v[120:123], v[240:243], v[60:63], v[120:123]\n"
    "ds_read_b128 v[224:227], v3 offset:8192\n"
    "v_cvt_pk_bf16_f32 v104, v15, v16\n"
    "v_mfma_f32_16x16x32_bf16 v[124:127], v[240:243], v[64:67], v[124:127]\n"
    "v_accvgpr_read_b32 v17, a218\n"
    "v_accvgpr_read_b32 v18, a219\n"
    "v_mfma_f32_16x16x32_bf16 v[128:131], v[240:243], v[68:71], v[128:131]\n"
    "s_waitcnt lgkmcnt(3)\n"
    "v_mul_f32 v17, s68, v17\n"
    "v_mul_f32 v18, s68, v18\n"
    "v_mfma_f32_16x16x32_bf16 v[140:143], v[244:247], v[68:71], v[140:143]\n"
    "ds_read_b128 v[228:231], v3 offset:10240\n"
    "v_cvt_pk_bf16_f32 v105, v17, v18\n"
    "v_mfma_f32_16x16x32_bf16 v[136:139], v[244:247], v[64:67], v[136:139]\n"
    "v_accvgpr_read_b32 v19, a228\n"
    "v_accvgpr_read_b32 v20, a229\n"
    "v_mfma_f32_16x16x32_bf16 v[132:135], v[244:247], v[60:63], v[132:135]\n"
    "s_waitcnt lgkmcnt(3)\n"
    "v_mul_f32 v19, s68, v19\n"
    "v_mul_f32 v20, s68, v20\n"
    "v_mfma_f32_16x16x32_bf16 v[120:123], v[216:219], v[72:75], v[120:123]\n"
    "ds_read_b128 v[232:235], v2 offset:12288\n"
    "v_cvt_pk_bf16_f32 v106, v19, v20\n"
    "v_mfma_f32_16x16x32_bf16 v[124:127], v[216:219], v[76:79], v[124:127]\n"
    "v_accvgpr_read_b32 v21, a230\n"
    "v_accvgpr_read_b32 v22, a231\n"
    "v_mfma_f32_16x16x32_bf16 v[128:131], v[216:219], v[80:83], v[128:131]\n"
    "s_waitcnt lgkmcnt(3)\n"
    "v_mul_f32 v21, s68, v21\n"
    "v_mul_f32 v22, s68, v22\n"
    "v_mfma_f32_16x16x32_bf16 v[140:143], v[220:223], v[80:83], v[140:143]\n"
    "ds_read_b128 v[236:239], v2 offset:14336\n"
    "v_cvt_pk_bf16_f32 v107, v21, v22\n"
    "v_mfma_f32_16x16x32_bf16 v[136:139], v[220:223], v[76:79], v[136:139]\n"
    "v_accvgpr_read_b32 v15, a232\n"
    "v_accvgpr_read_b32 v16, a233\n"
    "v_mfma_f32_16x16x32_bf16 v[132:135], v[220:223], v[72:75], v[132:135]\n"
    "s_waitcnt lgkmcnt(3)\n"
    "v_mul_f32 v15, s68, v15\n"
    "v_mul_f32 v16, s68, v16\n"
    "v_mfma_f32_16x16x32_bf16 v[120:123], v[224:227], v[84:87], v[120:123]\n"
    "ds_read_b128 v[240:243], v3 offset:12288\n"
    "v_cvt_pk_bf16_f32 v108, v15, v16\n"
    "v_mfma_f32_16x16x32_bf16 v[124:127], v[224:227], v[88:91], v[124:127]\n"
    "v_accvgpr_read_b32 v17, a234\n"
    "v_accvgpr_read_b32 v18, a235\n"
    "v_mfma_f32_16x16x32_bf16 v[128:131], v[224:227], v[92:95], v[128:131]\n"
    "s_waitcnt lgkmcnt(3)\n"
    "v_mul_f32 v17, s68, v17\n"
    "v_mul_f32 v18, s68, v18\n"
    "v_mfma_f32_16x16x32_bf16 v[140:143], v[228:231], v[92:95], v[140:143]\n"
    "ds_read_b128 v[244:247], v3 offset:14336\n"
    "v_cvt_pk_bf16_f32 v109, v17, v18\n"
    "v_mfma_f32_16x16x32_bf16 v[136:139], v[228:231], v[88:91], v[136:139]\n"
    "v_accvgpr_read_b32 v19, a244\n"
    "v_accvgpr_read_b32 v20, a245\n"
    "v_mfma_f32_16x16x32_bf16 v[132:135], v[228:231], v[84:87], v[132:135]\n"
    "s_waitcnt lgkmcnt(3)\n"
    "v_mul_f32 v19, s68, v19\n"
    "v_mul_f32 v20, s68, v20\n"
    "v_add_u32 v2, 0x4000, v2\n"
    "v_mfma_f32_16x16x32_bf16 v[120:123], v[232:235], v[96:99], v[120:123]\n"
    "s_barrier\n"
    "ds_read_b128 v[216:219], v2 offset:0\n"
    "v_cvt_pk_bf16_f32 v110, v19, v20\n"
    "v_mfma_f32_16x16x32_bf16 v[124:127], v[232:235], v[100:103], v[124:127]\n"
    "v_accvgpr_read_b32 v21, a246\n"
    "v_accvgpr_read_b32 v22, a247\n"
    "v_mfma_f32_16x16x32_bf16 v[128:131], v[232:235], v[104:107], v[128:131]\n"
    "s_waitcnt lgkmcnt(3)\n"
    "v_mul_f32 v21, s68, v21\n"
    "v_mul_f32 v22, s68, v22\n"
    "v_mfma_f32_16x16x32_bf16 v[140:143], v[236:239], v[104:107], v[140:143]\n"
    "ds_read_b128 v[220:223], v2 offset:2048\n"
    "v_cvt_pk_bf16_f32 v111, v21, v22\n"
    "v_mfma_f32_16x16x32_bf16 v[136:139], v[236:239], v[100:103], v[136:139]\n"
    "v_accvgpr_read_b32 v15, a236\n"
    "v_accvgpr_read_b32 v16, a237\n"
    "v_mul_f32 v15, s68, v15\n"
    "v_mul_f32 v16, s68, v16\n"
    "v_cvt_pk_bf16_f32 v112, v15, v16\n"
    "v_accvgpr_read_b32 v17, a238\n"
    "v_accvgpr_read_b32 v18, a239\n"
    "v_mul_f32 v17, s68, v17\n"
    "v_mul_f32 v18, s68, v18\n"
    "v_cvt_pk_bf16_f32 v113, v17, v18\n"
    "v_accvgpr_read_b32 v19, a248\n"
    "v_accvgpr_read_b32 v20, a249\n"
    "v_mul_f32 v19, s68, v19\n"
    "v_mul_f32 v20, s68, v20\n"
    "v_cvt_pk_bf16_f32 v114, v19, v20\n"
    "v_accvgpr_read_b32 v21, a250\n"
    "v_accvgpr_read_b32 v22, a251\n"
    "v_mul_f32 v21, s68, v21\n"
    "v_mul_f32 v22, s68, v22\n"
    "v_cvt_pk_bf16_f32 v115, v21, v22\n"
    "v_accvgpr_read_b32 v15, a240\n"
    "v_accvgpr_read_b32 v16, a241\n"
    "v_mul_f32 v15, s68, v15\n"
    "v_mul_f32 v16, s68, v16\n"
    "v_cvt_pk_bf16_f32 v116, v15, v16\n"
    "v_accvgpr_read_b32 v17, a242\n"
    "v_accvgpr_read_b32 v18, a243\n"
    "v_mul_f32 v17, s68, v17\n"
    "v_mul_f32 v18, s68, v18\n"
    "v_cvt_pk_bf16_f32 v117, v17, v18\n"
    "v_accvgpr_read_b32 v19, a252\n"
    "v_accvgpr_read_b32 v20, a253\n"
    "v_mul_f32 v19, s68, v19\n"
    "v_mul_f32 v20, s68, v20\n"
    "v_cvt_pk_bf16_f32 v118, v19, v20\n"
    "v_accvgpr_read_b32 v21, a254\n"
    "v_accvgpr_read_b32 v22, a255\n"
    "v_mul_f32 v21, s68, v21\n"
    "v_mul_f32 v22, s68, v22\n"
    "v_cvt_pk_bf16_f32 v119, v21, v22\n"
    "v_mfma_f32_16x16x32_bf16 v[132:135], v[236:239], v[96:99], v[132:135]\n"
    "s_waitcnt lgkmcnt(3)\n"
    "buffer_load_dwordx2 v[254:255], v249, s[28:31], 0 offen sc1\n"
    "v_add_u32 v3, 0x4000, v3\n"
    "v_mfma_f32_16x16x32_bf16 v[120:123], v[240:243], v[108:111], v[120:123]\n"
    "ds_read_b128 v[224:227], v3 offset:0\n"
    "v_mfma_f32_16x16x32_bf16 v[124:127], v[240:243], v[112:115], v[124:127]\n"
    "v_mfma_f32_16x16x32_bf16 v[128:131], v[240:243], v[116:119], v[128:131]\n"
    "s_waitcnt lgkmcnt(3)\n"
    "v_mfma_f32_16x16x32_bf16 v[140:143], v[244:247], v[116:119], v[140:143]\n"
    "ds_read_b128 v[228:231], v3 offset:2048\n"
    "v_mfma_f32_16x16x32_bf16 v[136:139], v[244:247], v[112:115], v[136:139]\n"
    "v_mfma_f32_16x16x32_bf16 v[132:135], v[244:247], v[108:111], v[132:135]\n"
    "s_waitcnt lgkmcnt(3)\n"
    "s_nop 7\n"
    "v_max3_f32 v12, v120, v121, v122\n"
    "v_max3_f32 v12, v12, v123, v132\n"
    "v_max3_f32 v12, v12, v133, v134\n"
    "v_max_f32 v12, v12, v135\n"
    "v_max3_f32 v13, v124, v125, v126\n"
    "v_max3_f32 v13, v13, v127, v136\n"
    "v_max3_f32 v13, v13, v137, v138\n"
    "v_max_f32 v13, v13, v139\n"
    "v_max3_f32 v14, v128, v129, v130\n"
    "v_max3_f32 v14, v14, v131, v140\n"
    "v_max3_f32 v14, v14, v141, v142\n"
    "v_max_f32 v14, v14, v143\n"
    "v_mov_b32 v15, v12\n"
    "v_mov_b32 v16, v13\n"
    "v_mov_b32 v17, v14\n"
    "s_nop 1\n"
    "v_permlane32_swap_b32 v12, v15\n"
    "v_permlane32_swap_b32 v13, v16\n"
    "v_permlane32_swap_b32 v14, v17\n"
    "s_nop 1\n"
    "v_max_f32 v12, v12, v15\n"
    "v_max_f32 v13, v13, v16\n"
    "v_max_f32 v14, v14, v17\n"
    "v_mov_b32 v15, v12\n"
    "v_mov_b32 v16, v13\n"
    "v_mov_b32 v17, v14\n"
    "s_nop 1\n"
    "v_permlane16_swap_b32 v12, v15\n"
    "v_permlane16_swap_b32 v13, v16\n"
    "v_permlane16_swap_b32 v14, v17\n"
    "s_nop 1\n"
    "v_max_f32 v12, v12, v15\n"
    "v_max_f32 v13, v13, v16\n"
    "v_max_f32 v14, v14, v17\n"
    "v_sub_f32 v204, 0, v12\n"
    "v_sub_f32 v205, 0, v12\n"
    "v_sub_f32 v206, 0, v12\n"
    "v_sub_f32 v207, 0, v12\n"
    "v_sub_f32 v208, 0, v13\n"
    "v_sub_f32 v209, 0, v13\n"
    "v_sub_f32 v210, 0, v13\n"
    "v_sub_f32 v211, 0, v13\n"
    "v_sub_f32 v212, 0, v14\n"
    "v_sub_f32 v213, 0, v14\n"
    "v_sub_f32 v214, 0, v14\n"
    "v_sub_f32 v215, 0, v14\n"
    "v_sub_f32 v120, v120, v12\n"
    "v_sub_f32 v121, v121, v12\n"
    "v_sub_f32 v122, v122, v12\n"
    "v_sub_f32 v123, v123, v12\n"
    "v_sub_f32 v132, v132, v12\n"
    "v_sub_f32 v133, v133, v12\n"
    "v_sub_f32 v134, v134, v12\n"
    "v_sub_f32 v135, v135, v12\n"
    "v_sub_f32 v124, v124, v13\n"
    "v_sub_f32 v125, v125, v13\n"
    "v_sub_f32 v126, v126, v13\n"
    "v_sub_f32 v127, v127, v13\n"
    "s_nop 0\n"
    "v_mfma_f32_16x16x32_bf16 v[168:171], v[216:219], v[24:27], v[204:207]\n"
    "ds_read_b128 v[232:235], v2 offset:4096\n"
    "v_sub_f32 v136, v136, v13\n"
    "v_mfma_f32_16x16x32_bf16 v[172:175], v[216:219], v[28:31], v[208:211]\n"
    "v_sub_f32 v142, v142, v14\n"
    "v_sub_f32 v143, v143, v14\n"
    "v_mfma_f32_16x16x32_bf16 v[176:179], v[216:219], v[32:35], v[212:215]\n"
    "s_waitcnt lgkmcnt(3)\n"
    "v_exp_f32 v120, v120\n"
    "v_mfma_f32_16x16x32_bf16 v[188:191], v[220:223], v[32:35], v[212:215]\n"
    "ds_read_b128 v[236:239], v2 offset:6144\n"
    "v_sub_f32 v137, v137, v13\n"
    "v_mfma_f32_16x16x32_bf16 v[184:187], v[220:223], v[28:31], v[208:211]\n"
    "v_exp_f32 v121, v121\n"
    "v_mfma_f32_16x16x32_bf16 v[180:183], v[220:223], v[24:27], v[204:207]\n"
    "s_waitcnt lgkmcnt(3)\n"
    "v_exp_f32 v122, v122\n"
    "v_mfma_f32_16x16x32_bf16 v[168:171], v[224:227], v[36:39], v[168:171]\n"
    "ds_read_b128 v[240:243], v3 offset:4096\n"
    "v_sub_f32 v138, v138, v13\n"
    "v_mfma_f32_16x16x32_bf16 v[172:175], v[224:227], v[40:43], v[172:175]\n"
    "v_exp_f32 v123, v123\n"
    "v_mfma_f32_16x16x32_bf16 v[176:179], v[224:227], v[44:47], v[176:179]\n"
    "s_waitcnt lgkmcnt(3)\n"
    "v_exp_f32 v132, v132\n"
    "v_mfma_f32_16x16x32_bf16 v[188:191], v[228:231], v[44:47], v[188:191]\n"
    "ds_read_b128 v[244:247], v3 offset:6144\n"
    "v_sub_f32 v139, v139, v13\n"
    "v_mfma_f32_16x16x32_bf16 v[184:187], v[228:231], v[40:43], v[184:187]\n"
    "v_exp_f32 v133, v133\n"
    "v_mfma_f32_16x16x32_bf16 v[180:183], v[228:231], v[36:39], v[180:183]\n"
    "s_waitcnt lgkmcnt(3)\n"
    "v_exp_f32 v134, v134\n"
    "v_mfma_f32_16x16x32_bf16 v[168:171], v[232:235], v[48:51], v[168:171]\n"
    "ds_read_b128 v[216:219], v2 offset:8192\n"
    "v_sub_f32 v128, v128, v14\n"
    "v_mfma_f32_16x16x32_bf16 v[172:175], v[232:235], v[52:55], v[172:175]\n"
    "v_exp_f32 v135, v135\n"
    "v_mfma_f32_16x16x32_bf16 v[176:179], v[232:235], v[56:59], v[176:179]\n"
    "s_waitcnt lgkmcnt(3)\n"
    "v_exp_f32 v124, v124\n"
    "v_mfma_f32_16x16x32_bf16 v[188:191], v[236:239], v[56:59], v[188:191]\n"
    "ds_read_b128 v[220:223], v2 offset:10240\n"
    "v_sub_f32 v129, v129, v14\n"
    "v_mfma_f32_16x16x32_bf16 v[184:187], v[236:239], v[52:55], v[184:187]\n"
    "v_exp_f32 v125, v125\n"
    "v_mfma_f32_16x16x32_bf16 v[180:183], v[236:239], v[48:51], v[180:183]\n"
    "s_waitcnt lgkmcnt(3)\n"
    "v_exp_f32 v126, v126\n"
    "v_mfma_f32_16x16x32_bf16 v[168:171], v[240:243], v[60:63], v[168:171]\n"
    "ds_read_b128 v[224:227], v3 offset:8192\n"
    "v_sub_f32 v130, v130, v14\n"
    "v_mfma_f32_16x16x32_bf16 v[172:175], v[240:243], v[64:67], v[172:175]\n"
    "v_exp_f32 v127, v127\n"
    "v_mfma_f32_16x16x32_bf16 v[176:179], v[240:243], v[68:71], v[176:179]\n"
    "s_waitcnt lgkmcnt(3)\n"
    "v_exp_f32 v136, v136\n"
    "v_mfma_f32_16x16x32_bf16 v[188:191], v[244:247], v[68:71], v[188:191]\n"
    "ds_read_b128 v[228:231], v3 offset:10240\n"
    "v_sub_f32 v131, v131, v14\n"
    "v_mfma_f32_16x16x32_bf16 v[184:187], v[244:247], v[64:67], v[184:187]\n"
    "v_exp_f32 v137, v137\n"
    "v_mfma_f32_16x16x32_bf16 v[180:183], v[244:247], v[60:63], v[180:183]\n"
    "s_waitcnt lgkmcnt(3)\n"
    "v_exp_f32 v138, v138\n"
    "v_mfma_f32_16x16x32_bf16 v[168:171], v[216:219], v[72:75], v[168:171]\n"
    "ds_read_b128 v[232:235], v2 offset:12288\n"
    "v_sub_f32 v140, v140, v14\n"
    "v_mfma_f32_16x16x32_bf16 v[172:175], v[216:219], v[76:79], v[172:175]\n"
    "v_exp_f32 v139, v139\n"
    "v_mfma_f32_16x16x32_bf16 v[176:179], v[216:219], v[80:83], v[176:179]\n"
    "s_waitcnt lgkmcnt(3)\n"
    "v_exp_f32 v128, v128\n"
    "v_mfma_f32_16x16x32_bf16 v[188:191], v[220:223], v[80:83], v[188:191]\n"
    "ds_read_b128 v[236:239], v2 offset:14336\n"
    "v_sub_f32 v141, v141, v14\n"
    "v_mfma_f32_16x16x32_bf16 v[184:187], v[220:223], v[76:79], v[184:187]\n"
    "v_exp_f32 v129, v129\n"
    "v_mfma_f32_16x16x32_bf16 v[180:183], v[220:223], v[72:75], v[180:183]\n"
    "s_waitcnt lgkmcnt(3)\n"
    "v_exp_f32 v130, v130\n"
    "v_mfma_f32_16x16x32_bf16 v[168:171], v[224:227], v[84:87], v[168:171]\n"
    "ds_read_b128 v[240:243], v3 offset:12288\n"
    "v_cvt_pk_bf16_f32 v192, v120, v121\n"
    "v_mfma_f32_16x16x32_bf16 v[172:175], v[224:227], v[88:91], v[172:175]\n"
    "v_exp_f32 v131, v131\n"
    "v_mfma_f32_16x16x32_bf16 v[176:179], v[224:227], v[92:95], v[176:179]\n"
    "s_waitcnt lgkmcnt(3)\n"
    "v_exp_f32 v140, v140\n"
    "v_mfma_f32_16x16x32_bf16 v[188:191], v[228:231], v[92:95], v[188:191]\n"
    "ds_read_b128 v[244:247], v3 offset:14336\n"
    "v_cvt_pk_bf16_f32 v193, v122, v123\n"
    "v_mfma_f32_16x16x32_bf16 v[184:187], v[228:231], v[88:91], v[184:187]\n"
    "v_exp_f32 v141, v141\n"
    "v_mfma_f32_16x16x32_bf16 v[180:183], v[228:231], v[84:87], v[180:183]\n"
    "s_waitcnt lgkmcnt(3)\n"
    "v_exp_f32 v142, v142\n"
    "v_mfma_f32_16x16x32_bf16 v[168:171], v[232:235], v[96:99], v[168:171]\n"
    "s_barrier\n"
    "ds_read_b64_tr_b16 v[216:217], v4 offset:0\n"
    "v_cvt_pk_bf16_f32 v194, v132, v133\n"
    "v_mfma_f32_16x16x32_bf16 v[172:175], v[232:235], v[100:103], v[172:175]\n"
    "ds_read_b64_tr_b16 v[218:219], v4 offset:2048\n"
    "v_cvt_pk_bf16_f32 v195, v134, v135\n"
    "v_mfma_f32_16x16x32_bf16 v[176:179], v[232:235], v[104:107], v[176:179]\n"
    "s_waitcnt lgkmcnt(4)\n"
    "v_exp_f32 v143, v143\n"
    "v_mfma_f32_16x16x32_bf16 v[188:191], v[236:239], v[104:107], v[188:191]\n"
    "ds_read_b64_tr_b16 v[220:221], v5 offset:0\n"
    "v_cvt_pk_bf16_f32 v196, v124, v125\n"
    "v_mfma_f32_16x16x32_bf16 v[184:187], v[236:239], v[100:103], v[184:187]\n"
    "ds_read_b64_tr_b16 v[222:223], v5 offset:2048\n"
    "v_cvt_pk_bf16_f32 v197, v126, v127\n"
    "v_mfma_f32_16x16x32_bf16 v[180:183], v[236:239], v[96:99], v[180:183]\n"
    "s_waitcnt lgkmcnt(5)\n"
    "v_cvt_pk_bf16_f32 v198, v136, v137\n"
    "v_cvt_pk_bf16_f32 v199, v138, v139\n"
    "v_mfma_f32_16x16x32_bf16 v[168:171], v[240:243], v[108:111], v[168:171]\n"
    "ds_read_b64_tr_b16 v[224:225], v6 offset:0\n"
    "v_cvt_pk_bf16_f32 v200, v128, v129\n"
    "v_mfma_f32_16x16x32_bf16 v[172:175], v[240:243], v[112:115], v[172:175]\n"
    "ds_read_b64_tr_b16 v[226:227], v6 offset:2048\n"
    "v_cvt_pk_bf16_f32 v201, v130, v131\n"
    "v_mfma_f32_16x16x32_bf16 v[176:179], v[240:243], v[116:119], v[176:179]\n"
    "s_waitcnt lgkmcnt(6)\n"
    "v_cvt_pk_bf16_f32 v202, v140, v141\n"
    "v_cvt_pk_bf16_f32 v203, v142, v143\n"
    "v_mfma_f32_16x16x32_bf16 v[188:191], v[244:247], v[116:119], v[188:191]\n"
    "ds_read_b64_tr_b16 v[228:229], v7 offset:0\n"
    "v_add_u32 v2, 0xfffe0000, v2\n"
    "v_mfma_f32_16x16x32_bf16 v[184:187], v[244:247], v[112:115], v[184:187]\n"
    "ds_read_b64_tr_b16 v[230:231], v7 offset:2048\n"
    "v_add_u32 v3, 0xfffe0000, v3\n"
    "v_mfma_f32_16x16x32_bf16 v[180:183], v[244:247], v[108:111], v[180:183]\n"
    "s_waitcnt lgkmcnt(6)\n"
    "v_mfma_f32_16x16x32_bf16 a[0:3], v[216:219], v[192:195], 0\n"
    "ds_read_b64_tr_b16 v[232:233], v4 offset:4096\n"
    "v_add_f32 v9, v9, v120\n"
    "v_mfma_f32_16x16x32_bf16 a[4:7], v[216:219], v[196:199], 0\n"
    "ds_read_b64_tr_b16 v[234:235], v4 offset:6144\n"
    "v_add_f32 v9, v9, v121\n"
    "v_mfma_f32_16x16x32_bf16 a[8:11], v[216:219], v[200:203], 0\n"
    "s_waitcnt lgkmcnt(6)\n"
    "v_add_f32 v9, v9, v122\n"
    "v_add_f32 v9, v9, v123\n"
    "v_mfma_f32_16x16x32_bf16 a[20:23], v[220:223], v[200:203], 0\n"
    "ds_read_b64_tr_b16 v[236:237], v5 offset:4096\n"
    "v_max3_f32 v12, v168, v169, v170\n"
    "v_mfma_f32_16x16x32_bf16 a[16:19], v[220:223], v[196:199], 0\n"
    "ds_read_b64_tr_b16 v[238:239], v5 offset:6144\n"
    "v_max3_f32 v12, v12, v171, v172\n"
    "v_mfma_f32_16x16x32_bf16 a[12:15], v[220:223], v[192:195], 0\n"
    "s_waitcnt lgkmcnt(6)\n"
    "v_max3_f32 v12, v12, v173, v174\n"
    "v_max3_f32 v12, v12, v175, v176\n"
    "v_mfma_f32_16x16x32_bf16 a[24:27], v[224:227], v[192:195], 0\n"
    "ds_read_b64_tr_b16 v[240:241], v6 offset:4096\n"
    "v_max3_f32 v12, v12, v177, v178\n"
    "v_mfma_f32_16x16x32_bf16 a[28:31], v[224:227], v[196:199], 0\n"
    "ds_read_b64_tr_b16 v[242:243], v6 offset:6144\n"
    "v_max3_f32 v12, v12, v179, v180\n"
    "v_mfma_f32_16x16x32_bf16 a[32:35], v[224:227], v[200:203], 0\n"
    "s_waitcnt lgkmcnt(6)\n"
    "v_max3_f32 v12, v12, v181, v182\n"
    "v_max3_f32 v12, v12, v183, v184\n"
    "v_mfma_f32_16x16x32_bf16 a[44:47], v[228:231], v[200:203], 0\n"
    "ds_read_b64_tr_b16 v[244:245], v7 offset:4096\n"
    "v_max3_f32 v12, v12, v185, v186\n"
    "v_mfma_f32_16x16x32_bf16 a[40:43], v[228:231], v[196:199], 0\n"
    "ds_read_b64_tr_b16 v[246:247], v7 offset:6144\n"
    "v_max3_f32 v12, v12, v187, v188\n"
    "v_mfma_f32_16x16x32_bf16 a[36:39], v[228:231], v[192:195], 0\n"
    "s_waitcnt lgkmcnt(6)\n"
    "v_max3_f32 v12, v12, v189, v190\n"
    "v_max_f32 v12, v12, v191\n"
    "v_mfma_f32_16x16x32_bf16 a[48:51], v[232:235], v[192:195], 0\n"
    "ds_read_b64_tr_b16 v[216:217], v4 offset:8192\n"
    "v_cmp_lt_f32 vcc, 0x41000000, v12\n"
    "v_mfma_f32_16x16x32_bf16 a[52:55], v[232:235], v[196:199], 0\n"
    "ds_read_b64_tr_b16 v[218:219], v4 offset:10240\n"
    "v_add_f32 v9, v9, v132\n"
    "v_mfma_f32_16x16x32_bf16 a[56:59], v[232:235], v[200:203], 0\n"
    "s_waitcnt lgkmcnt(6)\n"
    "v_add_f32 v9, v9, v133\n"
    "v_add_f32 v9, v9, v134\n"
    "v_mfma_f32_16x16x32_bf16 a[68:71], v[236:239], v[200:203], 0\n"
    "ds_read_b64_tr_b16 v[220:221], v5 offset:8192\n"
    "v_add_f32 v9, v9, v135\n"
    "v_mfma_f32_16x16x32_bf16 a[64:67], v[236:239], v[196:199], 0\n"
    "ds_read_b64_tr_b16 v[222:223], v5 offset:10240\n"
    "v_add_f32 v252, v252, v124\n"
    "v_mfma_f32_16x16x32_bf16 a[60:63], v[236:239], v[192:195], 0\n"
    "s_waitcnt lgkmcnt(6)\n"
    "v_add_f32 v252, v252, v125\n"
    "v_add_f32 v252, v252, v126\n"
    "v_mfma_f32_16x16x32_bf16 a[72:75], v[240:243], v[192:195], 0\n"
    "ds_read_b64_tr_b16 v[224:225], v6 offset:8192\n"
    "v_add_f32 v252, v252, v127\n"
    "v_mfma_f32_16x16x32_bf16 a[76:79], v[240:243], v[196:199], 0\n"
    "ds_read_b64_tr_b16 v[226:227], v6 offset:10240\n"
    "v_add_f32 v252, v252, v136\n"
    "v_mfma_f32_16x16x32_bf16 a[80:83], v[240:243], v[200:203], 0\n"
    "s_waitcnt lgkmcnt(6)\n"
    "v_add_f32 v252, v252, v137\n"
    "v_add_f32 v252, v252, v138\n"
    "v_mfma_f32_16x16x32_bf16 a[92:95], v[244:247], v[200:203], 0\n"
    "ds_read_b64_tr_b16 v[228:229], v7 offset:8192\n"
    "v_add_f32 v252, v252, v139\n"
    "v_mfma_f32_16x16x32_bf16 a[88:91], v[244:247], v[196:199], 0\n"
    "ds_read_b64_tr_b16 v[230:231], v7 offset:10240\n"
    "v_add_f32 v253, v253, v128\n"
    "v_mfma_f32_16x16x32_bf16 a[84:87], v[244:247], v[192:195], 0\n"
    "s_waitcnt lgkmcnt(6)\n"
    "v_add_f32 v253, v253, v129\n"
    "v_add_f32 v253, v253, v130\n"
    "v_mfma_f32_16x16x32_bf16 a[96:99], v[216:219], v[192:195], 0\n"
    "ds_read_b64_tr_b16 v[232:233], v4 offset:12288\n"
    "v_add_f32 v253, v253, v131\n"
    "v_mfma_f32_16x16x32_bf16 a[100:103], v[216:219], v[196:199], 0\n"
    "ds_read_b64_tr_b16 v[234:235], v4 offset:14336\n"
    "v_add_f32 v253, v253, v140\n"
    "v_mfma_f32_16x16x32_bf16 a[104:107], v[216:219], v[200:203], 0\n"
    "s_waitcnt lgkmcnt(6)\n"
    "v_add_f32 v253, v253, v141\n"
    "v_add_f32 v253, v253, v142\n"
    "v_mfma_f32_16x16x32_bf16 a[116:119], v[220:223], v[200:203], 0\n"
    "ds_read_b64_tr_b16 v[236:237], v5 offset:12288\n"
    "v_add_f32 v253, v253, v143\n"
    "v_mfma_f32_16x16x32_bf16 a[112:115], v[220:223], v[196:199], 0\n"
    "ds_read_b64_tr_b16 v[238:239], v5 offset:14336\n"
    "v_add_u32 v4, 0xfffe0000, v4\n"
    "v_mfma_f32_16x16x32_bf16 a[108:111], v[220:223], v[192:195], 0\n"
    "s_waitcnt lgkmcnt(6)\n"
    "v_add_u32 v5, 0xfffe0000, v5\n"
    "v_mfma_f32_16x16x32_bf16 a[120:123], v[224:227], v[192:195], 0\n"
    "ds_read_b64_tr_b16 v[240:241], v6 offset:12288\n"
    "v_mfma_f32_16x16x32_bf16 a[124:127], v[224:227], v[196:199], 0\n"
    "ds_read_b64_tr_b16 v[242:243], v6 offset:14336\n"
    "v_add_u32 v6, 0xfffe0000, v6\n"
    "v_mfma_f32_16x16x32_bf16 a[128:131], v[224:227], v[200:203], 0\n"
    "s_waitcnt lgkmcnt(6)\n"
    "v_mfma_f32_16x16x32_bf16 a[140:143], v[228:231], v[200:203], 0\n"
    "ds_read_b64_tr_b16 v[244:245], v7 offset:12288\n"
    "v_mfma_f32_16x16x32_bf16 a[136:139], v[228:231], v[196:199], 0\n"
    "ds_read_b64_tr_b16 v[246:247], v7 offset:14336\n"
    "v_add_u32 v7, 0xfffe0000, v7\n"
    "v_mfma_f32_16x16x32_bf16 a[132:135], v[228:231], v[192:195], 0\n"
    "s_waitcnt lgkmcnt(6)\n"
    "v_mfma_f32_16x16x32_bf16 a[144:147], v[232:235], v[192:195], 0\n"
    "s_barrier\n"
    "ds_read_b128 v[216:219], v2 offset:0\n"
    "v_mfma_f32_16x16x32_bf16 a[148:151], v[232:235], v[196:199], 0\n"
    "v_mfma_f32_16x16x32_bf16 a[152:155], v[232:235], v[200:203], 0\n"
    "s_waitcnt lgkmcnt(5)\n"
    "v_mfma_f32_16x16x32_bf16 a[164:167], v[236:239], v[200:203], 0\n"
    "ds_read_b128 v[220:223], v2 offset:2048\n"
    "v_mfma_f32_16x16x32_bf16 a[160:163], v[236:239], v[196:199], 0\n"
    "v_mfma_f32_16x16x32_bf16 a[156:159], v[236:239], v[192:195], 0\n"
    "s_waitcnt lgkmcnt(4)\n"
    "v_mfma_f32_16x16x32_bf16 a[168:171], v[240:243], v[192:195], 0\n"
    "ds_read_b128 v[224:227], v3 offset:0\n"
    "v_mfma_f32_16x16x32_bf16 a[172:175], v[240:243], v[196:199], 0\n"
    "v_mfma_f32_16x16x32_bf16 a[176:179], v[240:243], v[200:203], 0\n"
    "s_waitcnt lgkmcnt(3)\n"
    "v_mfma_f32_16x16x32_bf16 a[188:191], v[244:247], v[200:203], 0\n"
    "ds_read_b128 v[228:231], v3 offset:2048\n"
    "v_mfma_f32_16x16x32_bf16 a[184:187], v[244:247], v[196:199], 0\n"
    "v_mfma_f32_16x16x32_bf16 a[180:183], v[244:247], v[192:195], 0\n"
    "s_waitcnt lgkmcnt(3)\n"
    "s_cbranch_vccnz TRAMP_P1_%=\n"
    "CONT_P1_%=:\n"
    "LOOP_%=:\n"
    "v_mfma_f32_16x16x32_bf16 v[120:123], v[216:219], v[24:27], v[204:207]\n"
    "ds_read_b128 v[232:235], v2 offset:4096\n"
    "v_mfma_f32_16x16x32_bf16 v[124:127], v[216:219], v[28:31], v[208:211]\n"
    "v_exp_f32 v168, v168\n"
    "v_mfma_f32_16x16x32_bf16 v[128:131], v[216:219], v[32:35], v[212:215]\n"
    "s_waitcnt lgkmcnt(3)\n"
    "v_exp_f32 v169, v169\n"
    "v_mfma_f32_16x16x32_bf16 v[140:143], v[220:223], v[32:35], v[212:215]\n"
    "ds_read_b128 v[236:239], v2 offset:6144\n"
    "v_cvt_pk_bf16_f32 v192, v168, v169\n"
    "v_mfma_f32_16x16x32_bf16 v[136:139], v[220:223], v[28:31], v[208:211]\n"
    "v_exp_f32 v170, v170\n"
    "v_mfma_f32_16x16x32_bf16 v[132:135], v[220:223], v[24:27], v[204:207]\n"
    "s_waitcnt lgkmcnt(3)\n"
    "v_exp_f32 v171, v171\n"
    "v_mfma_f32_16x16x32_bf16 v[120:123], v[224:227], v[36:39], v[120:123]\n"
    "ds_read_b128 v[240:243], v3 offset:4096\n"
    "v_cvt_pk_bf16_f32 v193, v170, v171\n"
    "v_mfma_f32_16x16x32_bf16 v[124:127], v[224:227], v[40:43], v[124:127]\n"
    "v_exp_f32 v180, v180\n"
    "v_mfma_f32_16x16x32_bf16 v[128:131], v[224:227], v[44:47], v[128:131]\n"
    "s_waitcnt lgkmcnt(3)\n"
    "v_exp_f32 v181, v181\n"
    "v_mfma_f32_16x16x32_bf16 v[140:143], v[228:231], v[44:47], v[140:143]\n"
    "ds_read_b128 v[244:247], v3 offset:6144\n"
    "v_cvt_pk_bf16_f32 v194, v180, v181\n"
    "v_mfma_f32_16x16x32_bf16 v[136:139], v[228:231], v[40:43], v[136:139]\n"
    "v_exp_f32 v182, v182\n"
    "v_mfma_f32_16x16x32_bf16 v[132:135], v[228:231], v[36:39], v[132:135]\n"
    "s_waitcnt lgkmcnt(3)\n"
    "v_exp_f32 v183, v183\n"
    "v_mfma_f32_16x16x32_bf16 v[120:123], v[232:235], v[48:51], v[120:123]\n"
    "ds_read_b128 v[216:219], v2 offset:8192\n"
    "v_cvt_pk_bf16_f32 v195, v182, v183\n"
    "v_mfma_f32_16x16x32_bf16 v[124:127], v[232:235], v[52:55], v[124:127]\n"
    "v_exp_f32 v172, v172\n"
    "v_mfma_f32_16x16x32_bf16 v[128:131], v[232:235], v[56:59], v[128:131]\n"
    "s_waitcnt lgkmcnt(3)\n"
    "v_exp_f32 v173, v173\n"
    "v_mfma_f32_16x16x32_bf16 v[140:143], v[236:239], v[56:59], v[140:143]\n"
    "ds_read_b128 v[220:223], v2 offset:10240\n"
    "v_cvt_pk_bf16_f32 v196, v172, v173\n"
    "v_mfma_f32_16x16x32_bf16 v[136:139], v[236:239], v[52:55], v[136:139]\n"
    "v_exp_f32 v174, v174\n"
    "v_mfma_f32_16x16x32_bf16 v[132:135], v[236:239], v[48:51], v[132:135]\n"
    "s_waitcnt lgkmcnt(3)\n"
    "v_exp_f32 v175, v175\n"
    "v_mfma_f32_16x16x32_bf16 v[120:123], v[240:243], v[60:63], v[120:123]\n"
    "ds_read_b128 v[224:227], v3 offset:8192\n"
    "v_cvt_pk_bf16_f32 v197, v174, v175\n"
    "v_mfma_f32_16x16x32_bf16 v[124:127], v[240:243], v[64:67], v[124:127]\n"
    "v_exp_f32 v184, v184\n"
    "v_mfma_f32_16x16x32_bf16 v[128:131], v[240:243], v[68:71], v[128:131]\n"
    "s_waitcnt lgkmcnt(3)\n"
    "v_exp_f32 v185, v185\n"
    "v_mfma_f32_16x16x32_bf16 v[140:143], v[244:247], v[68:71], v[140:143]\n"
    "ds_read_b128 v[228:231], v3 offset:10240\n"
    "v_cvt_pk_bf16_f32 v198, v184, v185\n"
    "v_mfma_f32_16x16x32_bf16 v[136:139], v[244:247], v[64:67], v[136:139]\n"
    "v_exp_f32 v186, v186\n"
    "v_mfma_f32_16x16x32_bf16 v[132:135], v[244:247], v[60:63], v[132:135]\n"
    "s_waitcnt lgkmcnt(3)\n"
    "v_exp_f32 v187, v187\n"
    "v_mfma_f32_16x16x32_bf16 v[120:123], v[216:219], v[72:75], v[120:123]\n"
    "ds_read_b128 v[232:235], v2 offset:12288\n"
    "v_cvt_pk_bf16_f32 v199, v186, v187\n"
    "v_mfma_f32_16x16x32_bf16 v[124:127], v[216:219], v[76:79], v[124:127]\n"
    "v_exp_f32 v176, v176\n"
    "v_mfma_f32_16x16x32_bf16 v[128:131], v[216:219], v[80:83], v[128:131]\n"
    "s_waitcnt lgkmcnt(3)\n"
    "v_exp_f32 v177, v177\n"
    "v_mfma_f32_16x16x32_bf16 v[140:143], v[220:223], v[80:83], v[140:143]\n"
    "ds_read_b128 v[236:239], v2 offset:14336\n"
    "v_cvt_pk_bf16_f32 v200, v176, v177\n"
    "v_mfma_f32_16x16x32_bf16 v[136:139], v[220:223], v[76:79], v[136:139]\n"
    "v_exp_f32 v178, v178\n"
    "v_mfma_f32_16x16x32_bf16 v[132:135], v[220:223], v[72:75], v[132:135]\n"
    "s_waitcnt lgkmcnt(3)\n"
    "v_exp_f32 v179, v179\n"
    "v_mfma_f32_16x16x32_bf16 v[120:123], v[224:227], v[84:87], v[120:123]\n"
    "ds_read_b128 v[240:243], v3 offset:12288\n"
    "v_cvt_pk_bf16_f32 v201, v178, v179\n"
    "v_mfma_f32_16x16x32_bf16 v[124:127], v[224:227], v[88:91], v[124:127]\n"
    "v_exp_f32 v188, v188\n"
    "v_mfma_f32_16x16x32_bf16 v[128:131], v[224:227], v[92:95], v[128:131]\n"
    "s_waitcnt lgkmcnt(3)\n"
    "v_exp_f32 v189, v189\n"
    "v_mfma_f32_16x16x32_bf16 v[140:143], v[228:231], v[92:95], v[140:143]\n"
    "ds_read_b128 v[244:247], v3 offset:14336\n"
    "v_cvt_pk_bf16_f32 v202, v188, v189\n"
    "v_mfma_f32_16x16x32_bf16 v[136:139], v[228:231], v[88:91], v[136:139]\n"
    "v_exp_f32 v190, v190\n"
    "v_mfma_f32_16x16x32_bf16 v[132:135], v[228:231], v[84:87], v[132:135]\n"
    "s_waitcnt lgkmcnt(3)\n"
    "v_exp_f32 v191, v191\n"
    "v_mfma_f32_16x16x32_bf16 v[120:123], v[232:235], v[96:99], v[120:123]\n"
    "s_barrier\n"
    "ds_read_b64_tr_b16 v[216:217], v4 offset:0\n"
    "v_cvt_pk_bf16_f32 v203, v190, v191\n"
    "v_mfma_f32_16x16x32_bf16 v[124:127], v[232:235], v[100:103], v[124:127]\n"
    "ds_read_b64_tr_b16 v[218:219], v4 offset:2048\n"
    "v_mfma_f32_16x16x32_bf16 v[128:131], v[232:235], v[104:107], v[128:131]\n"
    "s_waitcnt lgkmcnt(4)\n"
    "v_mfma_f32_16x16x32_bf16 v[140:143], v[236:239], v[104:107], v[140:143]\n"
    "ds_read_b64_tr_b16 v[220:221], v5 offset:0\n"
    "v_mfma_f32_16x16x32_bf16 v[136:139], v[236:239], v[100:103], v[136:139]\n"
    "ds_read_b64_tr_b16 v[222:223], v5 offset:2048\n"
    "v_mfma_f32_16x16x32_bf16 v[132:135], v[236:239], v[96:99], v[132:135]\n"
    "s_waitcnt lgkmcnt(5)\n"
    "v_mfma_f32_16x16x32_bf16 v[120:123], v[240:243], v[108:111], v[120:123]\n"
    "ds_read_b64_tr_b16 v[224:225], v6 offset:0\n"
    "v_mfma_f32_16x16x32_bf16 v[124:127], v[240:243], v[112:115], v[124:127]\n"
    "ds_read_b64_tr_b16 v[226:227], v6 offset:2048\n"
    "v_mfma_f32_16x16x32_bf16 v[128:131], v[240:243], v[116:119], v[128:131]\n"
    "s_waitcnt lgkmcnt(6)\n"
    "v_mfma_f32_16x16x32_bf16 v[140:143], v[244:247], v[116:119], v[140:143]\n"
    "ds_read_b64_tr_b16 v[228:229], v7 offset:0\n"
    "v_mfma_f32_16x16x32_bf16 v[136:139], v[244:247], v[112:115], v[136:139]\n"
    "ds_read_b64_tr_b16 v[230:231], v7 offset:2048\n"
    "v_mfma_f32_16x16x32_bf16 v[132:135], v[244:247], v[108:111], v[132:135]\n"
    "s_waitcnt lgkmcnt(6)\n"
    "v_mfma_f32_16x16x32_bf16 a[0:3], v[216:219], v[192:195], a[0:3]\n"
    "ds_read_b64_tr_b16 v[232:233], v4 offset:4096\n"
    "v_add_f32 v9, v9, v168\n"
    "v_mfma_f32_16x16x32_bf16 a[4:7], v[216:219], v[196:199], a[4:7]\n"
    "s_add_u32 s58, s58, 1\n"
    "ds_read_b64_tr_b16 v[234:235], v4 offset:6144\n"
    "v_add_f32 v9, v9, v169\n"
    "v_mfma_f32_16x16x32_bf16 a[8:11], v[216:219], v[200:203], a[8:11]\n"
    "s_waitcnt lgkmcnt(6)\n"
    "v_add_f32 v9, v9, v170\n"
    "v_add_f32 v9, v9, v171\n"
    "v_mfma_f32_16x16x32_bf16 a[20:23], v[220:223], v[200:203], a[20:23]\n"
    "s_cmp_eq_u32 s58, 5\n"
    "ds_read_b64_tr_b16 v[236:237], v5 offset:4096\n"
    "v_max3_f32 v12, v120, v121, v122\n"
    "v_mfma_f32_16x16x32_bf16 a[16:19], v[220:223], v[196:199], a[16:19]\n"
    "s_cselect_b32 s57, s60, s59\n"
    "ds_read_b64_tr_b16 v[238:239], v5 offset:6144\n"
    "v_max3_f32 v12, v12, v123, v124\n"
    "v_mfma_f32_16x16x32_bf16 a[12:15], v[220:223], v[192:195], a[12:15]\n"
    "s_waitcnt lgkmcnt(6)\n"
    "v_max3_f32 v12, v12, v125, v126\n"
    "v_max3_f32 v12, v12, v127, v128\n"
    "v_mfma_f32_16x16x32_bf16 a[24:27], v[224:227], v[192:195], a[24:27]\n"
    "s_cselect_b32 s58, 0, s58\n"
    "ds_read_b64_tr_b16 v[240:241], v6 offset:4096\n"
    "v_max3_f32 v12, v12, v129, v130\n"
    "v_mfma_f32_16x16x32_bf16 a[28:31], v[224:227], v[196:199], a[28:31]\n"
    "ds_read_b64_tr_b16 v[242:243], v6 offset:6144\n"
    "v_max3_f32 v12, v12, v131, v132\n"
    "v_mfma_f32_16x16x32_bf16 a[32:35], v[224:227], v[200:203], a[32:35]\n"
    "s_waitcnt lgkmcnt(6)\n"
    "v_max3_f32 v12, v12, v133, v134\n"
    "v_max3_f32 v12, v12, v135, v136\n"
    "v_mfma_f32_16x16x32_bf16 a[44:47], v[228:231], v[200:203], a[44:47]\n"
    "ds_read_b64_tr_b16 v[244:245], v7 offset:4096\n"
    "v_max3_f32 v12, v12, v137, v138\n"
    "v_mfma_f32_16x16x32_bf16 a[40:43], v[228:231], v[196:199], a[40:43]\n"
    "ds_read_b64_tr_b16 v[246:247], v7 offset:6144\n"
    "v_max3_f32 v12, v12, v139, v140\n"
    "v_mfma_f32_16x16x32_bf16 a[36:39], v[228:231], v[192:195], a[36:39]\n"
    "s_waitcnt lgkmcnt(6)\n"
    "v_max3_f32 v12, v12, v141, v142\n"
    "v_max_f32 v12, v12, v143\n"
    "v_mfma_f32_16x16x32_bf16 a[48:51], v[232:235], v[192:195], a[48:51]\n"
    "ds_read_b64_tr_b16 v[216:217], v4 offset:8192\n"
    "v_cmp_lt_f32 vcc, 0x41000000, v12\n"
    "v_mfma_f32_16x16x32_bf16 a[52:55], v[232:235], v[196:199], a[52:55]\n"
    "ds_read_b64_tr_b16 v[218:219], v4 offset:10240\n"
    "v_add_f32 v9, v9, v180\n"
    "v_mfma_f32_16x16x32_bf16 a[56:59], v[232:235], v[200:203], a[56:59]\n"
    "s_waitcnt lgkmcnt(6)\n"
    "v_add_f32 v9, v9, v181\n"
    "v_add_f32 v9, v9, v182\n"
    "v_mfma_f32_16x16x32_bf16 a[68:71], v[236:239], v[200:203], a[68:71]\n"
    "ds_read_b64_tr_b16 v[220:221], v5 offset:8192\n"
    "v_add_f32 v9, v9, v183\n"
    "v_mfma_f32_16x16x32_bf16 a[64:67], v[236:239], v[196:199], a[64:67]\n"
    "ds_read_b64_tr_b16 v[222:223], v5 offset:10240\n"
    "v_add_f32 v252, v252, v172\n"
    "v_mfma_f32_16x16x32_bf16 a[60:63], v[236:239], v[192:195], a[60:63]\n"
    "s_waitcnt lgkmcnt(6)\n"
    "v_add_f32 v252, v252, v173\n"
    "v_add_f32 v252, v252, v174\n"
    "v_mfma_f32_16x16x32_bf16 a[72:75], v[240:243], v[192:195], a[72:75]\n"
    "ds_read_b64_tr_b16 v[224:225], v6 offset:8192\n"
    "v_add_f32 v252, v252, v175\n"
    "v_mfma_f32_16x16x32_bf16 a[76:79], v[240:243], v[196:199], a[76:79]\n"
    "ds_read_b64_tr_b16 v[226:227], v6 offset:10240\n"
    "v_add_f32 v252, v252, v184\n"
    "v_mfma_f32_16x16x32_bf16 a[80:83], v[240:243], v[200:203], a[80:83]\n"
    "s_waitcnt lgkmcnt(6)\n"
    "v_add_f32 v252, v252, v185\n"
    "v_add_f32 v252, v252, v186\n"
    "v_mfma_f32_16x16x32_bf16 a[92:95], v[244:247], v[200:203], a[92:95]\n"
    "ds_read_b64_tr_b16 v[228:229], v7 offset:8192\n"
    "v_add_f32 v252, v252, v187\n"
    "v_mfma_f32_16x16x32_bf16 a[88:91], v[244:247], v[196:199], a[88:91]\n"
    "ds_read_b64_tr_b16 v[230:231], v7 offset:10240\n"
    "v_add_f32 v253, v253, v176\n"
    "v_mfma_f32_16x16x32_bf16 a[84:87], v[244:247], v[192:195], a[84:87]\n"
    "s_waitcnt lgkmcnt(6)\n"
    "v_add_f32 v253, v253, v177\n"
    "v_add_f32 v253, v253, v178\n"
    "v_mfma_f32_16x16x32_bf16 a[96:99], v[216:219], v[192:195], a[96:99]\n"
    "ds_read_b64_tr_b16 v[232:233], v4 offset:12288\n"
    "v_add_f32 v253, v253, v179\n"
    "v_mfma_f32_16x16x32_bf16 a[100:103], v[216:219], v[196:199], a[100:103]\n"
    "ds_read_b64_tr_b16 v[234:235], v4 offset:14336\n"
    "v_add_f32 v253, v253, v188\n"
    "v_mfma_f32_16x16x32_bf16 a[104:107], v[216:219], v[200:203], a[104:107]\n"
    "s_waitcnt lgkmcnt(6)\n"
    "v_add_f32 v253, v253, v189\n"
    "v_add_f32 v253, v253, v190\n"
    "v_mfma_f32_16x16x32_bf16 a[116:119], v[220:223], v[200:203], a[116:119]\n"
    "ds_read_b64_tr_b16 v[236:237], v5 offset:12288\n"
    "v_add_f32 v253, v253, v191\n"
    "v_mfma_f32_16x16x32_bf16 a[112:115], v[220:223], v[196:199], a[112:115]\n"
    "ds_read_b64_tr_b16 v[238:239], v5 offset:14336\n"
    "v_add_u32 v2, s57, v2\n"
    "v_mfma_f32_16x16x32_bf16 a[108:111], v[220:223], v[192:195], a[108:111]\n"
    "s_waitcnt lgkmcnt(6)\n"
    "v_add_u32 v3, s57, v3\n"
    "v_add_u32 v4, s57, v4\n"
    "v_mfma_f32_16x16x32_bf16 a[120:123], v[224:227], v[192:195], a[120:123]\n"
    "ds_read_b64_tr_b16 v[240:241], v6 offset:12288\n"
    "v_add_u32 v5, s57, v5\n"
    "v_mfma_f32_16x16x32_bf16 a[124:127], v[224:227], v[196:199], a[124:127]\n"
    "ds_read_b64_tr_b16 v[242:243], v6 offset:14336\n"
    "v_add_u32 v6, s57, v6\n"
    "v_mfma_f32_16x16x32_bf16 a[128:131], v[224:227], v[200:203], a[128:131]\n"
    "s_waitcnt lgkmcnt(6)\n"
    "v_mfma_f32_16x16x32_bf16 a[140:143], v[228:231], v[200:203], a[140:143]\n"
    "ds_read_b64_tr_b16 v[244:245], v7 offset:12288\n"
    "v_mfma_f32_16x16x32_bf16 a[136:139], v[228:231], v[196:199], a[136:139]\n"
    "ds_read_b64_tr_b16 v[246:247], v7 offset:14336\n"
    "v_add_u32 v7, s57, v7\n"
    "v_mfma_f32_16x16x32_bf16 a[132:135], v[228:231], v[192:195], a[132:135]\n"
    "s_waitcnt lgkmcnt(6)\n"
    "v_mfma_f32_16x16x32_bf16 a[144:147], v[232:235], v[192:195], a[144:147]\n"
    "s_barrier\n"
    "ds_read_b128 v[216:219], v2 offset:0\n"
    "v_mfma_f32_16x16x32_bf16 a[148:151], v[232:235], v[196:199], a[148:151]\n"
    "v_mfma_f32_16x16x32_bf16 a[152:155], v[232:235], v[200:203], a[152:155]\n"
    "s_waitcnt lgkmcnt(5)\n"
    "v_mfma_f32_16x16x32_bf16 a[164:167], v[236:239], v[200:203], a[164:167]\n"
    "ds_read_b128 v[220:223], v2 offset:2048\n"
    "v_mfma_f32_16x16x32_bf16 a[160:163], v[236:239], v[196:199], a[160:163]\n"
    "v_mfma_f32_16x16x32_bf16 a[156:159], v[236:239], v[192:195], a[156:159]\n"
    "s_waitcnt lgkmcnt(4)\n"
    "v_mfma_f32_16x16x32_bf16 a[168:171], v[240:243], v[192:195], a[168:171]\n"
    "ds_read_b128 v[224:227], v3 offset:0\n"
    "v_mfma_f32_16x16x32_bf16 a[172:175], v[240:243], v[196:199], a[172:175]\n"
    "v_mfma_f32_16x16x32_bf16 a[176:179], v[240:243], v[200:203], a[176:179]\n"
    "s_waitcnt lgkmcnt(3)\n"
    "v_mfma_f32_16x16x32_bf16 a[188:191], v[244:247], v[200:203], a[188:191]\n"
    "ds_read_b128 v[228:231], v3 offset:2048\n"
    "v_mfma_f32_16x16x32_bf16 a[184:187], v[244:247], v[196:199], a[184:187]\n"
    "v_mfma_f32_16x16x32_bf16 a[180:183], v[244:247], v[192:195], a[180:183]\n"
    "s_waitcnt lgkmcnt(3)\n"
    "s_cbranch_vccnz TRAMP_L0_%=\n"
    "CONT_L0_%=:\n"
    "v_mfma_f32_16x16x32_bf16 v[168:171], v[216:219], v[24:27], v[204:207]\n"
    "ds_read_b128 v[232:235], v2 offset:4096\n"
    "v_mfma_f32_16x16x32_bf16 v[172:175], v[216:219], v[28:31], v[208:211]\n"
    "v_exp_f32 v120, v120\n"
    "v_mfma_f32_16x16x32_bf16 v[176:179], v[216:219], v[32:35], v[212:215]\n"
    "s_waitcnt lgkmcnt(3)\n"
    "v_exp_f32 v121, v121\n"
    "v_mfma_f32_16x16x32_bf16 v[188:191], v[220:223], v[32:35], v[212:215]\n"
    "ds_read_b128 v[236:239], v2 offset:6144\n"
    "v_cvt_pk_bf16_f32 v192, v120, v121\n"
    "v_mfma_f32_16x16x32_bf16 v[184:187], v[220:223], v[28:31], v[208:211]\n"
    "v_exp_f32 v122, v122\n"
    "v_mfma_f32_16x16x32_bf16 v[180:183], v[220:223], v[24:27], v[204:207]\n"
    "s_waitcnt lgkmcnt(3)\n"
    "v_exp_f32 v123, v123\n"
    "v_mfma_f32_16x16x32_bf16 v[168:171], v[224:227], v[36:39], v[168:171]\n"
    "ds_read_b128 v[240:243], v3 offset:4096\n"
    "v_cvt_pk_bf16_f32 v193, v122, v123\n"
    "v_mfma_f32_16x16x32_bf16 v[172:175], v[224:227], v[40:43], v[172:175]\n"
    "v_exp_f32 v132, v132\n"
    "v_mfma_f32_16x16x32_bf16 v[176:179], v[224:227], v[44:47], v[176:179]\n"
    "s_waitcnt lgkmcnt(3)\n"
    "v_exp_f32 v133, v133\n"
    "v_mfma_f32_16x16x32_bf16 v[188:191], v[228:231], v[44:47], v[188:191]\n"
    "ds_read_b128 v[244:247], v3 offset:6144\n"
    "v_cvt_pk_bf16_f32 v194, v132, v133\n"
    "v_mfma_f32_16x16x32_bf16 v[184:187], v[228:231], v[40:43], v[184:187]\n"
    "v_exp_f32 v134, v134\n"
    "v_mfma_f32_16x16x32_bf16 v[180:183], v[228:231], v[36:39], v[180:183]\n"
    "s_waitcnt lgkmcnt(3)\n"
    "v_exp_f32 v135, v135\n"
    "v_mfma_f32_16x16x32_bf16 v[168:171], v[232:235], v[48:51], v[168:171]\n"
    "ds_read_b128 v[216:219], v2 offset:8192\n"
    "v_cvt_pk_bf16_f32 v195, v134, v135\n"
    "v_mfma_f32_16x16x32_bf16 v[172:175], v[232:235], v[52:55], v[172:175]\n"
    "v_exp_f32 v124, v124\n"
    "v_mfma_f32_16x16x32_bf16 v[176:179], v[232:235], v[56:59], v[176:179]\n"
    "s_waitcnt lgkmcnt(3)\n"
    "v_exp_f32 v125, v125\n"
    "v_mfma_f32_16x16x32_bf16 v[188:191], v[236:239], v[56:59], v[188:191]\n"
    "ds_read_b128 v[220:223], v2 offset:10240\n"
    "v_cvt_pk_bf16_f32 v196, v124, v125\n"
    "v_mfma_f32_16x16x32_bf16 v[184:187], v[236:239], v[52:55], v[184:187]\n"
    "v_exp_f32 v126, v126\n"
    "v_mfma_f32_16x16x32_bf16 v[180:183], v[236:239], v[48:51], v[180:183]\n"
    "s_waitcnt lgkmcnt(3)\n"
    "v_exp_f32 v127, v127\n"
    "v_mfma_f32_16x16x32_bf16 v[168:171], v[240:243], v[60:63], v[168:171]\n"
    "ds_read_b128 v[224:227], v3 offset:8192\n"
    "v_cvt_pk_bf16_f32 v197, v126, v127\n"
    "v_mfma_f32_16x16x32_bf16 v[172:175], v[240:243], v[64:67], v[172:175]\n"
    "v_exp_f32 v136, v136\n"
    "v_mfma_f32_16x16x32_bf16 v[176:179], v[240:243], v[68:71], v[176:179]\n"
    "s_waitcnt lgkmcnt(3)\n"
    "v_exp_f32 v137, v137\n"
    "v_mfma_f32_16x16x32_bf16 v[188:191], v[244:247], v[68:71], v[188:191]\n"
    "ds_read_b128 v[228:231], v3 offset:10240\n"
    "v_cvt_pk_bf16_f32 v198, v136, v137\n"
    "v_mfma_f32_16x16x32_bf16 v[184:187], v[244:247], v[64:67], v[184:187]\n"
    "v_exp_f32 v138, v138\n"
    "v_mfma_f32_16x16x32_bf16 v[180:183], v[244:247], v[60:63], v[180:183]\n"
    "s_waitcnt lgkmcnt(3)\n"
    "v_exp_f32 v139, v139\n"
    "v_mfma_f32_16x16x32_bf16 v[168:171], v[216:219], v[72:75], v[168:171]\n"
    "ds_read_b128 v[232:235], v2 offset:12288\n"
    "v_cvt_pk_bf16_f32 v199, v138, v139\n"
    "v_mfma_f32_16x16x32_bf16 v[172:175], v[216:219], v[76:79], v[172:175]\n"
    "v_exp_f32 v128, v128\n"
    "v_mfma_f32_16x16x32_bf16 v[176:179], v[216:219], v[80:83], v[176:179]\n"
    "s_waitcnt lgkmcnt(3)\n"
    "v_exp_f32 v129, v129\n"
    "v_mfma_f32_16x16x32_bf16 v[188:191], v[220:223], v[80:83], v[188:191]\n"
    "ds_read_b128 v[236:239], v2 offset:14336\n"
    "v_cvt_pk_bf16_f32 v200, v128, v129\n"
    "v_mfma_f32_16x16x32_bf16 v[184:187], v[220:223], v[76:79], v[184:187]\n"
    "v_exp_f32 v130, v130\n"
    "v_mfma_f32_16x16x32_bf16 v[180:183], v[220:223], v[72:75], v[180:183]\n"
    "s_waitcnt lgkmcnt(3)\n"
    "v_exp_f32 v131, v131\n"
    "v_mfma_f32_16x16x32_bf16 v[168:171], v[224:227], v[84:87], v[168:171]\n"
    "ds_read_b128 v[240:243], v3 offset:12288\n"
    "v_cvt_pk_bf16_f32 v201, v130, v131\n"
    "v_mfma_f32_16x16x32_bf16 v[172:175], v[224:227], v[88:91], v[172:175]\n"
    "v_exp_f32 v140, v140\n"
    "v_mfma_f32_16x16x32_bf16 v[176:179], v[224:227], v[92:95], v[176:179]\n"
    "s_waitcnt lgkmcnt(3)\n"
    "v_exp_f32 v141, v141\n"
    "v_mfma_f32_16x16x32_bf16 v[188:191], v[228:231], v[92:95], v[188:191]\n"
    "ds_read_b128 v[244:247], v3 offset:14336\n"
    "v_cvt_pk_bf16_f32 v202, v140, v141\n"
    "v_mfma_f32_16x16x32_bf16 v[184:187], v[228:231], v[88:91], v[184:187]\n"
    "v_exp_f32 v142, v142\n"
    "v_mfma_f32_16x16x32_bf16 v[180:183], v[228:231], v[84:87], v[180:183]\n"
    "s_waitcnt lgkmcnt(3)\n"
    "v_exp_f32 v143, v143\n"
    "v_mfma_f32_16x16x32_bf16 v[168:171], v[232:235], v[96:99], v[168:171]\n"
    "s_barrier\n"
    "ds_read_b64_tr_b16 v[216:217], v4 offset:0\n"
    "v_cvt_pk_bf16_f32 v203, v142, v143\n"
    "v_mfma_f32_16x16x32_bf16 v[172:175], v[232:235], v[100:103], v[172:175]\n"
    "ds_read_b64_tr_b16 v[218:219], v4 offset:2048\n"
    "v_mfma_f32_16x16x32_bf16 v[176:179], v[232:235], v[104:107], v[176:179]\n"
    "s_waitcnt lgkmcnt(4)\n"
    "v_mfma_f32_16x16x32_bf16 v[188:191], v[236:239], v[104:107], v[188:191]\n"
    "ds_read_b64_tr_b16 v[220:221], v5 offset:0\n"
    "v_mfma_f32_16x16x32_bf16 v[184:187], v[236:239], v[100:103], v[184:187]\n"
    "ds_read_b64_tr_b16 v[222:223], v5 offset:2048\n"
    "v_mfma_f32_16x16x32_bf16 v[180:183], v[236:239], v[96:99], v[180:183]\n"
    "s_waitcnt lgkmcnt(5)\n"
    "v_mfma_f32_16x16x32_bf16 v[168:171], v[240:243], v[108:111], v[168:171]\n"
    "ds_read_b64_tr_b16 v[224:225], v6 offset:0\n"
    "v_mfma_f32_16x16x32_bf16 v[172:175], v[240:243], v[112:115], v[172:175]\n"
    "ds_read_b64_tr_b16 v[226:227], v6 offset:2048\n"
    "v_mfma_f32_16x16x32_bf16 v[176:179], v[240:243], v[116:119], v[176:179]\n"
    "s_waitcnt lgkmcnt(6)\n"
    "v_mfma_f32_16x16x32_bf16 v[188:191], v[244:247], v[116:119], v[188:191]\n"
    "ds_read_b64_tr_b16 v[228:229], v7 offset:0\n"
    "v_mfma_f32_16x16x32_bf16 v[184:187], v[244:247], v[112:115], v[184:187]\n"
    "ds_read_b64_tr_b16 v[230:231], v7 offset:2048\n"
    "v_mfma_f32_16x16x32_bf16 v[180:183], v[244:247], v[108:111], v[180:183]\n"
    "s_waitcnt lgkmcnt(6)\n"
    "v_mfma_f32_16x16x32_bf16 a[0:3], v[216:219], v[192:195], a[0:3]\n"
    "ds_read_b64_tr_b16 v[232:233], v4 offset:4096\n"
    "v_add_f32 v9, v9, v120\n"
    "v_mfma_f32_16x16x32_bf16 a[4:7], v[216:219], v[196:199], a[4:7]\n"
    "s_add_u32 s58, s58, 1\n"
    "ds_read_b64_tr_b16 v[234:235], v4 offset:6144\n"
    "v_add_f32 v9, v9, v121\n"
    "v_mfma_f32_16x16x32_bf16 a[8:11], v[216:219], v[200:203], a[8:11]\n"
    "s_waitcnt lgkmcnt(6)\n"
    "v_add_f32 v9, v9, v122\n"
    "v_add_f32 v9, v9, v123\n"
    "v_mfma_f32_16x16x32_bf16 a[20:23], v[220:223], v[200:203], a[20:23]\n"
    "s_cmp_eq_u32 s58, 5\n"
    "ds_read_b64_tr_b16 v[236:237], v5 offset:4096\n"
    "v_max3_f32 v12, v168, v169, v170\n"
    "v_mfma_f32_16x16x32_bf16 a[16:19], v[220:223], v[196:199], a[16:19]\n"
    "s_cselect_b32 s57, s60, s59\n"
    "ds_read_b64_tr_b16 v[238:239], v5 offset:6144\n"
    "v_max3_f32 v12, v12, v171, v172\n"
    "v_mfma_f32_16x16x32_bf16 a[12:15], v[220:223], v[192:195], a[12:15]\n"
    "s_waitcnt lgkmcnt(6)\n"
    "v_max3_f32 v12, v12, v173, v174\n"
    "v_max3_f32 v12, v12, v175, v176\n"
    "v_mfma_f32_16x16x32_bf16 a[24:27], v[224:227], v[192:195], a[24:27]\n"
    "s_cselect_b32 s58, 0, s58\n"
    "ds_read_b64_tr_b16 v[240:241], v6 offset:4096\n"
    "v_max3_f32 v12, v12, v177, v178\n"
    "v_mfma_f32_16x16x32_bf16 a[28:31], v[224:227], v[196:199], a[28:31]\n"
    "ds_read_b64_tr_b16 v[242:243], v6 offset:6144\n"
    "v_max3_f32 v12, v12, v179, v180\n"
    "v_mfma_f32_16x16x32_bf16 a[32:35], v[224:227], v[200:203], a[32:35]\n"
    "s_waitcnt lgkmcnt(6)\n"
    "v_max3_f32 v12, v12, v181, v182\n"
    "v_max3_f32 v12, v12, v183, v184\n"
    "v_mfma_f32_16x16x32_bf16 a[44:47], v[228:231], v[200:203], a[44:47]\n"
    "ds_read_b64_tr_b16 v[244:245], v7 offset:4096\n"
    "v_max3_f32 v12, v12, v185, v186\n"
    "v_mfma_f32_16x16x32_bf16 a[40:43], v[228:231], v[196:199], a[40:43]\n"
    "ds_read_b64_tr_b16 v[246:247], v7 offset:6144\n"
    "v_max3_f32 v12, v12, v187, v188\n"
    "v_mfma_f32_16x16x32_bf16 a[36:39], v[228:231], v[192:195], a[36:39]\n"
    "s_waitcnt lgkmcnt(6)\n"
    "v_max3_f32 v12, v12, v189, v190\n"
    "v_max_f32 v12, v12, v191\n"
    "v_mfma_f32_16x16x32_bf16 a[48:51], v[232:235], v[192:195], a[48:51]\n"
    "ds_read_b64_tr_b16 v[216:217], v4 offset:8192\n"
    "v_cmp_lt_f32 vcc, 0x41000000, v12\n"
    "v_mfma_f32_16x16x32_bf16 a[52:55], v[232:235], v[196:199], a[52:55]\n"
    "ds_read_b64_tr_b16 v[218:219], v4 offset:10240\n"
    "v_add_f32 v9, v9, v132\n"
    "v_mfma_f32_16x16x32_bf16 a[56:59], v[232:235], v[200:203], a[56:59]\n"
    "s_waitcnt lgkmcnt(6)\n"
    "v_add_f32 v9, v9, v133\n"
    "v_add_f32 v9, v9, v134\n"
    "v_mfma_f32_16x16x32_bf16 a[68:71], v[236:239], v[200:203], a[68:71]\n"
    "ds_read_b64_tr_b16 v[220:221], v5 offset:8192\n"
    "v_add_f32 v9, v9, v135\n"
    "v_mfma_f32_16x16x32_bf16 a[64:67], v[236:239], v[196:199], a[64:67]\n"
    "ds_read_b64_tr_b16 v[222:223], v5 offset:10240\n"
    "v_add_f32 v252, v252, v124\n"
    "v_mfma_f32_16x16x32_bf16 a[60:63], v[236:239], v[192:195], a[60:63]\n"
    "s_waitcnt lgkmcnt(6)\n"
    "v_add_f32 v252, v252, v125\n"
    "v_add_f32 v252, v252, v126\n"
    "v_mfma_f32_16x16x32_bf16 a[72:75], v[240:243], v[192:195], a[72:75]\n"
    "ds_read_b64_tr_b16 v[224:225], v6 offset:8192\n"
    "v_add_f32 v252, v252, v127\n"
    "v_mfma_f32_16x16x32_bf16 a[76:79], v[240:243], v[196:199], a[76:79]\n"
    "ds_read_b64_tr_b16 v[226:227], v6 offset:10240\n"
    "v_add_f32 v252, v252, v136\n"
    "v_mfma_f32_16x16x32_bf16 a[80:83], v[240:243], v[200:203], a[80:83]\n"
    "s_waitcnt lgkmcnt(6)\n"
    "v_add_f32 v252, v252, v137\n"
    "v_add_f32 v252, v252, v138\n"
    "v_mfma_f32_16x16x32_bf16 a[92:95], v[244:247], v[200:203], a[92:95]\n"
    "ds_read_b64_tr_b16 v[228:229], v7 offset:8192\n"
    "v_add_f32 v252, v252, v139\n"
    "v_mfma_f32_16x16x32_bf16 a[88:91], v[244:247], v[196:199], a[88:91]\n"
    "ds_read_b64_tr_b16 v[230:231], v7 offset:10240\n"
    "v_add_f32 v253, v253, v128\n"
    "v_mfma_f32_16x16x32_bf16 a[84:87], v[244:247], v[192:195], a[84:87]\n"
    "s_waitcnt lgkmcnt(6)\n"
    "v_add_f32 v253, v253, v129\n"
    "v_add_f32 v253, v253, v130\n"
    "v_mfma_f32_16x16x32_bf16 a[96:99], v[216:219], v[192:195], a[96:99]\n"
    "ds_read_b64_tr_b16 v[232:233], v4 offset:12288\n"
    "v_add_f32 v253, v253, v131\n"
    "v_mfma_f32_16x16x32_bf16 a[100:103], v[216:219], v[196:199], a[100:103]\n"
    "ds_read_b64_tr_b16 v[234:235], v4 offset:14336\n"
    "v_add_f32 v253, v253, v140\n"
    "v_mfma_f32_16x16x32_bf16 a[104:107], v[216:219], v[200:203], a[104:107]\n"
    "s_waitcnt lgkmcnt(6)\n"
    "v_add_f32 v253, v253, v141\n"
    "v_add_f32 v253, v253, v142\n"
    "v_mfma_f32_16x16x32_bf16 a[116:119], v[220:223], v[200:203], a[116:119]\n"
    "ds_read_b64_tr_b16 v[236:237], v5 offset:12288\n"
    "v_add_f32 v253, v253, v143\n"
    "v_mfma_f32_16x16x32_bf16 a[112:115], v[220:223], v[196:199], a[112:115]\n"
    "ds_read_b64_tr_b16 v[238:239], v5 offset:14336\n"
    "v_add_u32 v2, s57, v2\n"
    "v_mfma_f32_16x16x32_bf16 a[108:111], v[220:223], v[192:195], a[108:111]\n"
    "s_waitcnt lgkmcnt(6)\n"
    "v_add_u32 v3, s57, v3\n"
    "v_add_u32 v4, s57, v4\n"
    "v_mfma_f32_16x16x32_bf16 a[120:123], v[224:227], v[192:195], a[120:123]\n"
    "ds_read_b64_tr_b16 v[240:241], v6 offset:12288\n"
    "v_add_u32 v5, s57, v5\n"
    "v_mfma_f32_16x16x32_bf16 a[124:127], v[224:227], v[196:199], a[124:127]\n"
    "ds_read_b64_tr_b16 v[242:243], v6 offset:14336\n"
    "v_add_u32 v6, s57, v6\n"
    "v_mfma_f32_16x16x32_bf16 a[128:131], v[224:227], v[200:203], a[128:131]\n"
    "s_waitcnt lgkmcnt(6)\n"
    "v_mfma_f32_16x16x32_bf16 a[140:143], v[228:231], v[200:203], a[140:143]\n"
    "ds_read_b64_tr_b16 v[244:245], v7 offset:12288\n"
    "v_mfma_f32_16x16x32_bf16 a[136:139], v[228:231], v[196:199], a[136:139]\n"
    "ds_read_b64_tr_b16 v[246:247], v7 offset:14336\n"
    "v_add_u32 v7, s57, v7\n"
    "v_mfma_f32_16x16x32_bf16 a[132:135], v[228:231], v[192:195], a[132:135]\n"
    "s_waitcnt lgkmcnt(6)\n"
    "v_mfma_f32_16x16x32_bf16 a[144:147], v[232:235], v[192:195], a[144:147]\n"
    "s_barrier\n"
    "ds_read_b128 v[216:219], v2 offset:0\n"
    "v_mfma_f32_16x16x32_bf16 a[148:151], v[232:235], v[196:199], a[148:151]\n"
    "s_add_u32 s61, s61, 1\n"
    "v_mfma_f32_16x16x32_bf16 a[152:155], v[232:235], v[200:203], a[152:155]\n"
    "s_waitcnt lgkmcnt(5)\n"
    "v_mfma_f32_16x16x32_bf16 a[164:167], v[236:239], v[200:203], a[164:167]\n"
    "s_cmp_lt_u32 s61, 14\n"
    "ds_read_b128 v[220:223], v2 offset:2048\n"
    "v_mfma_f32_16x16x32_bf16 a[160:163], v[236:239], v[196:199], a[160:163]\n"
    "v_mfma_f32_16x16x32_bf16 a[156:159], v[236:239], v[192:195], a[156:159]\n"
    "s_waitcnt lgkmcnt(4)\n"
    "v_mfma_f32_16x16x32_bf16 a[168:171], v[240:243], v[192:195], a[168:171]\n"
    "ds_read_b128 v[224:227], v3 offset:0\n"
    "v_mfma_f32_16x16x32_bf16 a[172:175], v[240:243], v[196:199], a[172:175]\n"
    "v_mfma_f32_16x16x32_bf16 a[176:179], v[240:243], v[200:203], a[176:179]\n"
    "s_waitcnt lgkmcnt(3)\n"
    "v_mfma_f32_16x16x32_bf16 a[188:191], v[244:247], v[200:203], a[188:191]\n"
    "ds_read_b128 v[228:231], v3 offset:2048\n"
    "v_mfma_f32_16x16x32_bf16 a[184:187], v[244:247], v[196:199], a[184:187]\n"
    "v_mfma_f32_16x16x32_bf16 a[180:183], v[244:247], v[192:195], a[180:183]\n"
    "s_waitcnt lgkmcnt(3)\n"
    "s_cbranch_vccnz TRAMP_L1_%=\n"
    "CONT_L1_%=:\n"
    "s_cbranch_scc1 LOOP_%=\n"
    "v_mfma_f32_16x16x32_bf16 v[120:123], v[216:219], v[24:27], v[204:207]\n"
    "ds_read_b128 v[232:235], v2 offset:4096\n"
    "v_mfma_f32_16x16x32_bf16 v[124:127], v[216:219], v[28:31], v[208:211]\n"
    "v_exp_f32 v168, v168\n"
    "v_mfma_f32_16x16x32_bf16 v[128:131], v[216:219], v[32:35], v[212:215]\n"
    "s_waitcnt lgkmcnt(3)\n"
    "v_exp_f32 v169, v169\n"
    "v_mfma_f32_16x16x32_bf16 v[140:143], v[220:223], v[32:35], v[212:215]\n"
    "ds_read_b128 v[236:239], v2 offset:6144\n"
    "v_cvt_pk_bf16_f32 v192, v168, v169\n"
    "v_mfma_f32_16x16x32_bf16 v[136:139], v[220:223], v[28:31], v[208:211]\n"
    "v_exp_f32 v170, v170\n"
    "v_mfma_f32_16x16x32_bf16 v[132:135], v[220:223], v[24:27], v[204:207]\n"
    "s_waitcnt lgkmcnt(3)\n"
    "v_exp_f32 v171, v171\n"
    "v_mfma_f32_16x16x32_bf16 v[120:123], v[224:227], v[36:39], v[120:123]\n"
    "ds_read_b128 v[240:243], v3 offset:4096\n"
    "v_cvt_pk_bf16_f32 v193, v170, v171\n"
    "v_mfma_f32_16x16x32_bf16 v[124:127], v[224:227], v[40:43], v[124:127]\n"
    "v_exp_f32 v180, v180\n"
    "v_mfma_f32_16x16x32_bf16 v[128:131], v[224:227], v[44:47], v[128:131]\n"
    "s_waitcnt lgkmcnt(3)\n"
    "v_exp_f32 v181, v181\n"
    "v_mfma_f32_16x16x32_bf16 v[140:143], v[228:231], v[44:47], v[140:143]\n"
    "ds_read_b128 v[244:247], v3 offset:6144\n"
    "v_cvt_pk_bf16_f32 v194, v180, v181\n"
    "v_mfma_f32_16x16x32_bf16 v[136:139], v[228:231], v[40:43], v[136:139]\n"
    "v_exp_f32 v182, v182\n"
    "v_mfma_f32_16x16x32_bf16 v[132:135], v[228:231], v[36:39], v[132:135]\n"
    "s_waitcnt lgkmcnt(3)\n"
    "v_exp_f32 v183, v183\n"
    "v_mfma_f32_16x16x32_bf16 v[120:123], v[232:235], v[48:51], v[120:123]\n"
    "ds_read_b128 v[216:219], v2 offset:8192\n"
    "v_cvt_pk_bf16_f32 v195, v182, v183\n"
    "v_mfma_f32_16x16x32_bf16 v[124:127], v[232:235], v[52:55], v[124:127]\n"
    "v_exp_f32 v172, v172\n"
    "v_mfma_f32_16x16x32_bf16 v[128:131], v[232:235], v[56:59], v[128:131]\n"
    "s_waitcnt lgkmcnt(3)\n"
    "v_exp_f32 v173, v173\n"
    "v_mfma_f32_16x16x32_bf16 v[140:143], v[236:239], v[56:59], v[140:143]\n"
    "ds_read_b128 v[220:223], v2 offset:10240\n"
    "v_cvt_pk_bf16_f32 v196, v172, v173\n"
    "v_mfma_f32_16x16x32_bf16 v[136:139], v[236:239], v[52:55], v[136:139]\n"
    "v_exp_f32 v174, v174\n"
    "v_mfma_f32_16x16x32_bf16 v[132:135], v[236:239], v[48:51], v[132:135]\n"
    "s_waitcnt lgkmcnt(3)\n"
    "v_exp_f32 v175, v175\n"
    "v_mfma_f32_16x16x32_bf16 v[120:123], v[240:243], v[60:63], v[120:123]\n"
    "ds_read_b128 v[224:227], v3 offset:8192\n"
    "v_cvt_pk_bf16_f32 v197, v174, v175\n"
    "v_mfma_f32_16x16x32_bf16 v[124:127], v[240:243], v[64:67], v[124:127]\n"
    "v_exp_f32 v184, v184\n"
    "v_mfma_f32_16x16x32_bf16 v[128:131], v[240:243], v[68:71], v[128:131]\n"
    "s_waitcnt lgkmcnt(3)\n"
    "v_exp_f32 v185, v185\n"
    "v_mfma_f32_16x16x32_bf16 v[140:143], v[244:247], v[68:71], v[140:143]\n"
    "ds_read_b128 v[228:231], v3 offset:10240\n"
    "v_cvt_pk_bf16_f32 v198, v184, v185\n"
    "v_mfma_f32_16x16x32_bf16 v[136:139], v[244:247], v[64:67], v[136:139]\n"
    "v_exp_f32 v186, v186\n"
    "v_mfma_f32_16x16x32_bf16 v[132:135], v[244:247], v[60:63], v[132:135]\n"
    "s_waitcnt lgkmcnt(3)\n"
    "v_exp_f32 v187, v187\n"
    "v_mfma_f32_16x16x32_bf16 v[120:123], v[216:219], v[72:75], v[120:123]\n"
    "ds_read_b128 v[232:235], v2 offset:12288\n"
    "v_cvt_pk_bf16_f32 v199, v186, v187\n"
    "v_mfma_f32_16x16x32_bf16 v[124:127], v[216:219], v[76:79], v[124:127]\n"
    "v_exp_f32 v176, v176\n"
    "v_mfma_f32_16x16x32_bf16 v[128:131], v[216:219], v[80:83], v[128:131]\n"
    "s_waitcnt lgkmcnt(3)\n"
    "v_exp_f32 v177, v177\n"
    "v_mfma_f32_16x16x32_bf16 v[140:143], v[220:223], v[80:83], v[140:143]\n"
    "ds_read_b128 v[236:239], v2 offset:14336\n"
    "v_cvt_pk_bf16_f32 v200, v176, v177\n"
    "v_mfma_f32_16x16x32_bf16 v[136:139], v[220:223], v[76:79], v[136:139]\n"
    "v_exp_f32 v178, v178\n"
    "v_mfma_f32_16x16x32_bf16 v[132:135], v[220:223], v[72:75], v[132:135]\n"
    "s_waitcnt lgkmcnt(3)\n"
    "v_exp_f32 v179, v179\n"
    "v_mfma_f32_16x16x32_bf16 v[120:123], v[224:227], v[84:87], v[120:123]\n"
    "ds_read_b128 v[240:243], v3 offset:12288\n"
    "v_cvt_pk_bf16_f32 v201, v178, v179\n"
    "v_mfma_f32_16x16x32_bf16 v[124:127], v[224:227], v[88:91], v[124:127]\n"
    "v_exp_f32 v188, v188\n"
    "v_mfma_f32_16x16x32_bf16 v[128:131], v[224:227], v[92:95], v[128:131]\n"
    "s_waitcnt lgkmcnt(3)\n"
    "v_exp_f32 v189, v189\n"
    "v_mfma_f32_16x16x32_bf16 v[140:143], v[228:231], v[92:95], v[140:143]\n"
    "ds_read_b128 v[244:247], v3 offset:14336\n"
    "v_cvt_pk_bf16_f32 v202, v188, v189\n"
    "v_mfma_f32_16x16x32_bf16 v[136:139], v[228:231], v[88:91], v[136:139]\n"
    "v_exp_f32 v190, v190\n"
    "v_mfma_f32_16x16x32_bf16 v[132:135], v[228:231], v[84:87], v[132:135]\n"
    "s_waitcnt lgkmcnt(3)\n"
    "v_exp_f32 v191, v191\n"
    "v_mfma_f32_16x16x32_bf16 v[120:123], v[232:235], v[96:99], v[120:123]\n"
    "s_barrier\n"
    "ds_read_b64_tr_b16 v[216:217], v4 offset:0\n"
    "v_cvt_pk_bf16_f32 v203, v190, v191\n"
    "v_mfma_f32_16x16x32_bf16 v[124:127], v[232:235], v[100:103], v[124:127]\n"
    "ds_read_b64_tr_b16 v[218:219], v4 offset:2048\n"
    "v_add_u32 v2, 0x8000, v2\n"
    "v_mfma_f32_16x16x32_bf16 v[128:131], v[232:235], v[104:107], v[128:131]\n"
    "s_waitcnt lgkmcnt(4)\n"
    "v_add_u32 v3, 0x8000, v3\n"
    "v_mfma_f32_16x16x32_bf16 v[140:143], v[236:239], v[104:107], v[140:143]\n"
    "ds_read_b64_tr_b16 v[220:221], v5 offset:0\n"
    "v_mfma_f32_16x16x32_bf16 v[136:139], v[236:239], v[100:103], v[136:139]\n"
    "ds_read_b64_tr_b16 v[222:223], v5 offset:2048\n"
    "v_mfma_f32_16x16x32_bf16 v[132:135], v[236:239], v[96:99], v[132:135]\n"
    "s_waitcnt lgkmcnt(5)\n"
    "v_mfma_f32_16x16x32_bf16 v[120:123], v[240:243], v[108:111], v[120:123]\n"
    "ds_read_b64_tr_b16 v[224:225], v6 offset:0\n"
    "v_mfma_f32_16x16x32_bf16 v[124:127], v[240:243], v[112:115], v[124:127]\n"
    "ds_read_b64_tr_b16 v[226:227], v6 offset:2048\n"
    "v_mfma_f32_16x16x32_bf16 v[128:131], v[240:243], v[116:119], v[128:131]\n"
    "s_waitcnt lgkmcnt(6)\n"
    "v_mfma_f32_16x16x32_bf16 v[140:143], v[244:247], v[116:119], v[140:143]\n"
    "ds_read_b64_tr_b16 v[228:229], v7 offset:0\n"
    "v_mfma_f32_16x16x32_bf16 v[136:139], v[244:247], v[112:115], v[136:139]\n"
    "ds_read_b64_tr_b16 v[230:231], v7 offset:2048\n"
    "v_mfma_f32_16x16x32_bf16 v[132:135], v[244:247], v[108:111], v[132:135]\n"
    "s_waitcnt lgkmcnt(6)\n"
    "v_mfma_f32_16x16x32_bf16 a[0:3], v[216:219], v[192:195], a[0:3]\n"
    "ds_read_b64_tr_b16 v[232:233], v4 offset:4096\n"
    "v_add_f32 v9, v9, v168\n"
    "v_mfma_f32_16x16x32_bf16 a[4:7], v[216:219], v[196:199], a[4:7]\n"
    "s_waitcnt vmcnt(0)\n"
    "ds_read_b64_tr_b16 v[234:235], v4 offset:6144\n"
    "v_add_f32 v9, v9, v169\n"
    "v_mfma_f32_16x16x32_bf16 a[8:11], v[216:219], v[200:203], a[8:11]\n"
    "s_waitcnt lgkmcnt(6)\n"
    "v_add_f32 v9, v9, v170\n"
    "v_add_f32 v9, v9, v171\n"
    "v_mfma_f32_16x16x32_bf16 a[20:23], v[220:223], v[200:203], a[20:23]\n"
    "ds_read_b64_tr_b16 v[236:237], v5 offset:4096\n"
    "v_max3_f32 v12, v120, v121, v122\n"
    "v_mfma_f32_16x16x32_bf16 a[16:19], v[220:223], v[196:199], a[16:19]\n"
    "ds_read_b64_tr_b16 v[238:239], v5 offset:6144\n"
    "v_max3_f32 v12, v12, v123, v124\n"
    "v_mfma_f32_16x16x32_bf16 a[12:15], v[220:223], v[192:195], a[12:15]\n"
    "s_waitcnt lgkmcnt(6)\n"
    "v_max3_f32 v12, v12, v125, v126\n"
    "v_max3_f32 v12, v12, v127, v128\n"
    "v_mfma_f32_16x16x32_bf16 a[24:27], v[224:227], v[192:195], a[24:27]\n"
    "ds_read_b64_tr_b16 v[240:241], v6 offset:4096\n"
    "v_max3_f32 v12, v12, v129, v130\n"
    "v_mfma_f32_16x16x32_bf16 a[28:31], v[224:227], v[196:199], a[28:31]\n"
    "ds_read_b64_tr_b16 v[242:243], v6 offset:6144\n"
    "v_max3_f32 v12, v12, v131, v132\n"
    "v_mfma_f32_16x16x32_bf16 a[32:35], v[224:227], v[200:203], a[32:35]\n"
    "s_waitcnt lgkmcnt(6)\n"
    "v_max3_f32 v12, v12, v133, v134\n"
    "v_max3_f32 v12, v12, v135, v136\n"
    "v_mfma_f32_16x16x32_bf16 a[44:47], v[228:231], v[200:203], a[44:47]\n"
    "ds_read_b64_tr_b16 v[244:245], v7 offset:4096\n"
    "v_max3_f32 v12, v12, v137, v138\n"
    "v_mfma_f32_16x16x32_bf16 a[40:43], v[228:231], v[196:199], a[40:43]\n"
    "ds_read_b64_tr_b16 v[246:247], v7 offset:6144\n"
    "v_max3_f32 v12, v12, v139, v140\n"
    "v_mfma_f32_16x16x32_bf16 a[36:39], v[228:231], v[192:195], a[36:39]\n"
    "s_waitcnt lgkmcnt(6)\n"
    "v_max3_f32 v12, v12, v141, v142\n"
    "v_max_f32 v12, v12, v143\n"
    "v_mfma_f32_16x16x32_bf16 a[48:51], v[232:235], v[192:195], a[48:51]\n"
    "ds_read_b64_tr_b16 v[216:217], v4 offset:8192\n"
    "v_cmp_lt_f32 vcc, 0x41000000, v12\n"
    "v_mfma_f32_16x16x32_bf16 a[52:55], v[232:235], v[196:199], a[52:55]\n"
    "ds_read_b64_tr_b16 v[218:219], v4 offset:10240\n"
    "v_add_f32 v9, v9, v180\n"
    "v_mfma_f32_16x16x32_bf16 a[56:59], v[232:235], v[200:203], a[56:59]\n"
    "s_waitcnt lgkmcnt(6)\n"
    "v_add_f32 v9, v9, v181\n"
    "v_add_f32 v9, v9, v182\n"
    "v_mfma_f32_16x16x32_bf16 a[68:71], v[236:239], v[200:203], a[68:71]\n"
    "ds_read_b64_tr_b16 v[220:221], v5 offset:8192\n"
    "v_add_f32 v9, v9, v183\n"
    "v_mfma_f32_16x16x32_bf16 a[64:67], v[236:239], v[196:199], a[64:67]\n"
    "ds_read_b64_tr_b16 v[222:223], v5 offset:10240\n"
    "v_add_f32 v252, v252, v172\n"
    "v_mfma_f32_16x16x32_bf16 a[60:63], v[236:239], v[192:195], a[60:63]\n"
    "s_waitcnt lgkmcnt(6)\n"
    "v_add_f32 v252, v252, v173\n"
    "v_add_f32 v252, v252, v174\n"
    "v_mfma_f32_16x16x32_bf16 a[72:75], v[240:243], v[192:195], a[72:75]\n"
    "ds_read_b64_tr_b16 v[224:225], v6 offset:8192\n"
    "v_add_f32 v252, v252, v175\n"
    "v_mfma_f32_16x16x32_bf16 a[76:79], v[240:243], v[196:199], a[76:79]\n"
    "ds_read_b64_tr_b16 v[226:227], v6 offset:10240\n"
    "v_add_f32 v252, v252, v184\n"
    "v_mfma_f32_16x16x32_bf16 a[80:83], v[240:243], v[200:203], a[80:83]\n"
    "s_waitcnt lgkmcnt(6)\n"
    "v_add_f32 v252, v252, v185\n"
    "v_add_f32 v252, v252, v186\n"
    "v_mfma_f32_16x16x32_bf16 a[92:95], v[244:247], v[200:203], a[92:95]\n"
    "ds_read_b64_tr_b16 v[228:229], v7 offset:8192\n"
    "v_add_f32 v252, v252, v187\n"
    "v_mfma_f32_16x16x32_bf16 a[88:91], v[244:247], v[196:199], a[88:91]\n"
    "ds_read_b64_tr_b16 v[230:231], v7 offset:10240\n"
    "v_add_f32 v253, v253, v176\n"
    "v_mfma_f32_16x16x32_bf16 a[84:87], v[244:247], v[192:195], a[84:87]\n"
    "s_waitcnt lgkmcnt(6)\n"
    "v_add_f32 v253, v253, v177\n"
    "v_add_f32 v253, v253, v178\n"
    "v_mfma_f32_16x16x32_bf16 a[96:99], v[216:219], v[192:195], a[96:99]\n"
    "ds_read_b64_tr_b16 v[232:233], v4 offset:12288\n"
    "v_add_f32 v253, v253, v179\n"
    "v_mfma_f32_16x16x32_bf16 a[100:103], v[216:219], v[196:199], a[100:103]\n"
    "ds_read_b64_tr_b16 v[234:235], v4 offset:14336\n"
    "v_add_f32 v253, v253, v188\n"
    "v_mfma_f32_16x16x32_bf16 a[104:107], v[216:219], v[200:203], a[104:107]\n"
    "s_waitcnt lgkmcnt(6)\n"
    "v_add_f32 v253, v253, v189\n"
    "v_add_f32 v253, v253, v190\n"
    "v_mfma_f32_16x16x32_bf16 a[116:119], v[220:223], v[200:203], a[116:119]\n"
    "ds_read_b64_tr_b16 v[236:237], v5 offset:12288\n"
    "v_add_f32 v253, v253, v191\n"
    "v_mfma_f32_16x16x32_bf16 a[112:115], v[220:223], v[196:199], a[112:115]\n"
    "ds_read_b64_tr_b16 v[238:239], v5 offset:14336\n"
    "v_add_u32 v4, 0x8000, v4\n"
    "v_mfma_f32_16x16x32_bf16 a[108:111], v[220:223], v[192:195], a[108:111]\n"
    "s_waitcnt lgkmcnt(6)\n"
    "v_add_u32 v5, 0x8000, v5\n"
    "v_add_u32 v254, 1, v254\n"
    "v_mfma_f32_16x16x32_bf16 a[120:123], v[224:227], v[192:195], a[120:123]\n"
    "ds_read_b64_tr_b16 v[240:241], v6 offset:12288\n"
    "v_lshlrev_b32 v254, 4, v254\n"
    "v_mfma_f32_16x16x32_bf16 a[124:127], v[224:227], v[196:199], a[124:127]\n"
    "ds_read_b64_tr_b16 v[242:243], v6 offset:14336\n"
    "v_add_u32 v6, 0x8000, v6\n"
    "v_mfma_f32_16x16x32_bf16 a[128:131], v[224:227], v[200:203], a[128:131]\n"
    "s_waitcnt lgkmcnt(6)\n"
    "v_xor_b32 v255, 0x5a5a5a5a, v255\n"
    "v_mov_b32 v10, v23\n"
    "v_mfma_f32_16x16x32_bf16 a[140:143], v[228:231], v[200:203], a[140:143]\n"
    "ds_read_b64_tr_b16 v[244:245], v7 offset:12288\n"
    "v_mfma_f32_16x16x32_bf16 a[136:139], v[228:231], v[196:199], a[136:139]\n"
    "ds_read_b64_tr_b16 v[246:247], v7 offset:14336\n"
    "v_add_u32 v7, 0x8000, v7\n"
    "v_mfma_f32_16x16x32_bf16 a[132:135], v[228:231], v[192:195], a[132:135]\n"
    "s_waitcnt lgkmcnt(6)\n"
    "v_mfma_f32_16x16x32_bf16 a[144:147], v[232:235], v[192:195], a[144:147]\n"
    "s_barrier\n"
    "ds_read_b128 v[216:219], v2 offset:0\n"
    "v_mfma_f32_16x16x32_bf16 a[148:151], v[232:235], v[196:199], a[148:151]\n"
    "v_mfma_f32_16x16x32_bf16 a[152:155], v[232:235], v[200:203], a[152:155]\n"
    "s_waitcnt lgkmcnt(5)\n"
    "v_mfma_f32_16x16x32_bf16 a[164:167], v[236:239], v[200:203], a[164:167]\n"
    "ds_read_b128 v[220:223], v2 offset:2048\n"
    "v_mfma_f32_16x16x32_bf16 a[160:163], v[236:239], v[196:199], a[160:163]\n"
    "v_mfma_f32_16x16x32_bf16 a[156:159], v[236:239], v[192:195], a[156:159]\n"
    "s_waitcnt lgkmcnt(4)\n"
    "v_mfma_f32_16x16x32_bf16 a[168:171], v[240:243], v[192:195], a[168:171]\n"
    "ds_read_b128 v[224:227], v3 offset:0\n"
    "v_mfma_f32_16x16x32_bf16 a[172:175], v[240:243], v[196:199], a[172:175]\n"
    "v_mfma_f32_16x16x32_bf16 a[176:179], v[240:243], v[200:203], a[176:179]\n"
    "s_waitcnt lgkmcnt(3)\n"
    "v_mfma_f32_16x16x32_bf16 a[188:191], v[244:247], v[200:203], a[188:191]\n"
    "ds_read_b128 v[228:231], v3 offset:2048\n"
    "v_mfma_f32_16x16x32_bf16 a[184:187], v[244:247], v[196:199], a[184:187]\n"
    "v_mfma_f32_16x16x32_bf16 a[180:183], v[244:247], v[192:195], a[180:183]\n"
    "s_waitcnt lgkmcnt(3)\n"
    "s_cbranch_vccnz TRAMP_P30_%=\n"
    "CONT_P30_%=:\n"
    "v_mfma_f32_16x16x32_bf16 v[168:171], v[216:219], v[24:27], v[204:207]\n"
    "ds_read_b128 v[232:235], v2 offset:4096\n"
    "v_mfma_f32_16x16x32_bf16 v[172:175], v[216:219], v[28:31], v[208:211]\n"
    "v_exp_f32 v120, v120\n"
    "v_mfma_f32_16x16x32_bf16 v[176:179], v[216:219], v[32:35], v[212:215]\n"
    "s_waitcnt lgkmcnt(3)\n"
    "v_exp_f32 v121, v121\n"
    "v_mfma_f32_16x16x32_bf16 v[188:191], v[220:223], v[32:35], v[212:215]\n"
    "ds_read_b128 v[236:239], v2 offset:6144\n"
    "v_cvt_pk_bf16_f32 v192, v120, v121\n"
    "v_mfma_f32_16x16x32_bf16 v[184:187], v[220:223], v[28:31], v[208:211]\n"
    "v_exp_f32 v122, v122\n"
    "v_mfma_f32_16x16x32_bf16 v[180:183], v[220:223], v[24:27], v[204:207]\n"
    "s_waitcnt lgkmcnt(3)\n"
    "v_exp_f32 v123, v123\n"
    "v_mfma_f32_16x16x32_bf16 v[168:171], v[224:227], v[36:39], v[168:171]\n"
    "ds_read_b128 v[240:243], v3 offset:4096\n"
    "v_cvt_pk_bf16_f32 v193, v122, v123\n"
    "v_mfma_f32_16x16x32_bf16 v[172:175], v[224:227], v[40:43], v[172:175]\n"
    "v_exp_f32 v132, v132\n"
    "v_mfma_f32_16x16x32_bf16 v[176:179], v[224:227], v[44:47], v[176:179]\n"
    "s_waitcnt lgkmcnt(3)\n"
    "v_exp_f32 v133, v133\n"
    "v_mfma_f32_16x16x32_bf16 v[188:191], v[228:231], v[44:47], v[188:191]\n"
    "ds_read_b128 v[244:247], v3 offset:6144\n"
    "v_cvt_pk_bf16_f32 v194, v132, v133\n"
    "v_mfma_f32_16x16x32_bf16 v[184:187], v[228:231], v[40:43], v[184:187]\n"
    "v_exp_f32 v134, v134\n"
    "v_mfma_f32_16x16x32_bf16 v[180:183], v[228:231], v[36:39], v[180:183]\n"
    "s_waitcnt lgkmcnt(3)\n"
    "v_exp_f32 v135, v135\n"
    "v_mfma_f32_16x16x32_bf16 v[168:171], v[232:235], v[48:51], v[168:171]\n"
    "ds_read_b128 v[216:219], v2 offset:8192\n"
    "v_cvt_pk_bf16_f32 v195, v134, v135\n"
    "v_mfma_f32_16x16x32_bf16 v[172:175], v[232:235], v[52:55], v[172:175]\n"
    "v_exp_f32 v124, v124\n"
    "v_mfma_f32_16x16x32_bf16 v[176:179], v[232:235], v[56:59], v[176:179]\n"
    "s_waitcnt lgkmcnt(3)\n"
    "v_exp_f32 v125, v125\n"
    "v_mfma_f32_16x16x32_bf16 v[188:191], v[236:239], v[56:59], v[188:191]\n"
    "ds_read_b128 v[220:223], v2 offset:10240\n"
    "v_cvt_pk_bf16_f32 v196, v124, v125\n"
    "v_mfma_f32_16x16x32_bf16 v[184:187], v[236:239], v[52:55], v[184:187]\n"
    "v_exp_f32 v126, v126\n"
    "v_mfma_f32_16x16x32_bf16 v[180:183], v[236:239], v[48:51], v[180:183]\n"
    "s_waitcnt lgkmcnt(3)\n"
    "v_exp_f32 v127, v127\n"
    "v_mfma_f32_16x16x32_bf16 v[168:171], v[240:243], v[60:63], v[168:171]\n"
    "ds_read_b128 v[224:227], v3 offset:8192\n"
    "v_cvt_pk_bf16_f32 v197, v126, v127\n"
    "v_mfma_f32_16x16x32_bf16 v[172:175], v[240:243], v[64:67], v[172:175]\n"
    "v_exp_f32 v136, v136\n"
    "v_mfma_f32_16x16x32_bf16 v[176:179], v[240:243], v[68:71], v[176:179]\n"
    "s_waitcnt lgkmcnt(3)\n"
    "v_exp_f32 v137, v137\n"
    "v_mfma_f32_16x16x32_bf16 v[188:191], v[244:247], v[68:71], v[188:191]\n"
    "ds_read_b128 v[228:231], v3 offset:10240\n"
    "v_cvt_pk_bf16_f32 v198, v136, v137\n"
    "v_mfma_f32_16x16x32_bf16 v[184:187], v[244:247], v[64:67], v[184:187]\n"
    "v_exp_f32 v138, v138\n"
    "v_mfma_f32_16x16x32_bf16 v[180:183], v[244:247], v[60:63], v[180:183]\n"
    "s_waitcnt lgkmcnt(3)\n"
    "v_exp_f32 v139, v139\n"
    "v_mfma_f32_16x16x32_bf16 v[168:171], v[216:219], v[72:75], v[168:171]\n"
    "ds_read_b128 v[232:235], v2 offset:12288\n"
    "v_cvt_pk_bf16_f32 v199, v138, v139\n"
    "v_mfma_f32_16x16x32_bf16 v[172:175], v[216:219], v[76:79], v[172:175]\n"
    "v_exp_f32 v128, v128\n"
    "v_mfma_f32_16x16x32_bf16 v[176:179], v[216:219], v[80:83], v[176:179]\n"
    "s_waitcnt lgkmcnt(3)\n"
    "v_exp_f32 v129, v129\n"
    "v_mfma_f32_16x16x32_bf16 v[188:191], v[220:223], v[80:83], v[188:191]\n"
    "ds_read_b128 v[236:239], v2 offset:14336\n"
    "v_cvt_pk_bf16_f32 v200, v128, v129\n"
    "v_mfma_f32_16x16x32_bf16 v[184:187], v[220:223], v[76:79], v[184:187]\n"
    "v_exp_f32 v130, v130\n"
    "v_mfma_f32_16x16x32_bf16 v[180:183], v[220:223], v[72:75], v[180:183]\n"
    "s_waitcnt lgkmcnt(3)\n"
    "v_exp_f32 v131, v131\n"
    "v_mfma_f32_16x16x32_bf16 v[168:171], v[224:227], v[84:87], v[168:171]\n"
    "ds_read_b128 v[240:243], v3 offset:12288\n"
    "v_cvt_pk_bf16_f32 v201, v130, v131\n"
    "v_mfma_f32_16x16x32_bf16 v[172:175], v[224:227], v[88:91], v[172:175]\n"
    "v_exp_f32 v140, v140\n"
    "v_mfma_f32_16x16x32_bf16 v[176:179], v[224:227], v[92:95], v[176:179]\n"
    "s_waitcnt lgkmcnt(3)\n"
    "v_exp_f32 v141, v141\n"
    "v_mfma_f32_16x16x32_bf16 v[188:191], v[228:231], v[92:95], v[188:191]\n"
    "ds_read_b128 v[244:247], v3 offset:14336\n"
    "v_cvt_pk_bf16_f32 v202, v140, v141\n"
    "v_mfma_f32_16x16x32_bf16 v[184:187], v[228:231], v[88:91], v[184:187]\n"
    "v_exp_f32 v142, v142\n"
    "v_mfma_f32_16x16x32_bf16 v[180:183], v[228:231], v[84:87], v[180:183]\n"
    "s_waitcnt lgkmcnt(3)\n"
    "v_exp_f32 v143, v143\n"
    "v_mfma_f32_16x16x32_bf16 v[168:171], v[232:235], v[96:99], v[168:171]\n"
    "s_barrier\n"
    "ds_read_b64_tr_b16 v[216:217], v4 offset:0\n"
    "v_cvt_pk_bf16_f32 v203, v142, v143\n"
    "v_mfma_f32_16x16x32_bf16 v[172:175], v[232:235], v[100:103], v[172:175]\n"
    "ds_read_b64_tr_b16 v[218:219], v4 offset:2048\n"
    "v_mfma_f32_16x16x32_bf16 v[176:179], v[232:235], v[104:107], v[176:179]\n"
    "s_waitcnt lgkmcnt(4)\n"
    "v_mfma_f32_16x16x32_bf16 v[188:191], v[236:239], v[104:107], v[188:191]\n"
    "ds_read_b64_tr_b16 v[220:221], v5 offset:0\n"
    "v_mfma_f32_16x16x32_bf16 v[184:187], v[236:239], v[100:103], v[184:187]\n"
    "ds_read_b64_tr_b16 v[222:223], v5 offset:2048\n"
    "v_mfma_f32_16x16x32_bf16 v[180:183], v[236:239], v[96:99], v[180:183]\n"
    "s_waitcnt lgkmcnt(5)\n"
    "v_mfma_f32_16x16x32_bf16 v[168:171], v[240:243], v[108:111], v[168:171]\n"
    "ds_read_b64_tr_b16 v[224:225], v6 offset:0\n"
    "v_mfma_f32_16x16x32_bf16 v[172:175], v[240:243], v[112:115], v[172:175]\n"
    "ds_read_b64_tr_b16 v[226:227], v6 offset:2048\n"
    "v_mfma_f32_16x16x32_bf16 v[176:179], v[240:243], v[116:119], v[176:179]\n"
    "s_waitcnt lgkmcnt(6)\n"
    "v_mfma_f32_16x16x32_bf16 v[188:191], v[244:247], v[116:119], v[188:191]\n"
    "ds_read_b64_tr_b16 v[228:229], v7 offset:0\n"
    "v_mfma_f32_16x16x32_bf16 v[184:187], v[244:247], v[112:115], v[184:187]\n"
    "ds_read_b64_tr_b16 v[230:231], v7 offset:2048\n"
    "v_mfma_f32_16x16x32_bf16 v[180:183], v[244:247], v[108:111], v[180:183]\n"
    "s_waitcnt lgkmcnt(6)\n"
    "v_mfma_f32_16x16x32_bf16 a[0:3], v[216:219], v[192:195], a[0:3]\n"
    "ds_read_b64_tr_b16 v[232:233], v4 offset:4096\n"
    "v_add_f32 v9, v9, v120\n"
    "v_mfma_f32_16x16x32_bf16 a[4:7], v[216:219], v[196:199], a[4:7]\n"
    "ds_read_b64_tr_b16 v[234:235], v4 offset:6144\n"
    "v_add_f32 v9, v9, v121\n"
    "v_mfma_f32_16x16x32_bf16 a[8:11], v[216:219], v[200:203], a[8:11]\n"
    "s_waitcnt lgkmcnt(6)\n"
    "v_add_f32 v9, v9, v122\n"
    "v_add_f32 v9, v9, v123\n"
    "v_mfma_f32_16x16x32_bf16 a[20:23], v[220:223], v[200:203], a[20:23]\n"
    "ds_read_b64_tr_b16 v[236:237], v5 offset:4096\n"
    "v_max3_f32 v12, v168, v169, v170\n"
    "v_mfma_f32_16x16x32_bf16 a[16:19], v[220:223], v[196:199], a[16:19]\n"
    "ds_read_b64_tr_b16 v[238:239], v5 offset:6144\n"
    "v_max3_f32 v12, v12, v171, v172\n"
    "v_mfma_f32_16x16x32_bf16 a[12:15], v[220:223], v[192:195], a[12:15]\n"
    "s_waitcnt lgkmcnt(6)\n"
    "v_max3_f32 v12, v12, v173, v174\n"
    "v_max3_f32 v12, v12, v175, v176\n"
    "v_mfma_f32_16x16x32_bf16 a[24:27], v[224:227], v[192:195], a[24:27]\n"
    "ds_read_b64_tr_b16 v[240:241], v6 offset:4096\n"
    "v_max3_f32 v12, v12, v177, v178\n"
    "v_mfma_f32_16x16x32_bf16 a[28:31], v[224:227], v[196:199], a[28:31]\n"
    "ds_read_b64_tr_b16 v[242:243], v6 offset:6144\n"
    "v_max3_f32 v12, v12, v179, v180\n"
    "v_mfma_f32_16x16x32_bf16 a[32:35], v[224:227], v[200:203], a[32:35]\n"
    "s_waitcnt lgkmcnt(6)\n"
    "v_max3_f32 v12, v12, v181, v182\n"
    "v_max3_f32 v12, v12, v183, v184\n"
    "v_mfma_f32_16x16x32_bf16 a[44:47], v[228:231], v[200:203], a[44:47]\n"
    "ds_read_b64_tr_b16 v[244:245], v7 offset:4096\n"
    "v_max3_f32 v12, v12, v185, v186\n"
    "v_mfma_f32_16x16x32_bf16 a[40:43], v[228:231], v[196:199], a[40:43]\n"
    "ds_read_b64_tr_b16 v[246:247], v7 offset:6144\n"
    "v_max3_f32 v12, v12, v187, v188\n"
    "v_mfma_f32_16x16x32_bf16 a[36:39], v[228:231], v[192:195], a[36:39]\n"
    "s_waitcnt lgkmcnt(6)\n"
    "v_max3_f32 v12, v12, v189, v190\n"
    "v_max_f32 v12, v12, v191\n"
    "v_mfma_f32_16x16x32_bf16 a[48:51], v[232:235], v[192:195], a[48:51]\n"
    "ds_read_b64_tr_b16 v[216:217], v4 offset:8192\n"
    "v_cmp_lt_f32 vcc, 0x41000000, v12\n"
    "v_mfma_f32_16x16x32_bf16 a[52:55], v[232:235], v[196:199], a[52:55]\n"
    "ds_read_b64_tr_b16 v[218:219], v4 offset:10240\n"
    "v_add_f32 v9, v9, v132\n"
    "v_mfma_f32_16x16x32_bf16 a[56:59], v[232:235], v[200:203], a[56:59]\n"
    "s_waitcnt lgkmcnt(6)\n"
    "v_add_f32 v9, v9, v133\n"
    "v_add_f32 v9, v9, v134\n"
    "v_mfma_f32_16x16x32_bf16 a[68:71], v[236:239], v[200:203], a[68:71]\n"
    "ds_read_b64_tr_b16 v[220:221], v5 offset:8192\n"
    "v_add_f32 v9, v9, v135\n"
    "v_mfma_f32_16x16x32_bf16 a[64:67], v[236:239], v[196:199], a[64:67]\n"
    "ds_read_b64_tr_b16 v[222:223], v5 offset:10240\n"
    "v_add_f32 v252, v252, v124\n"
    "v_mfma_f32_16x16x32_bf16 a[60:63], v[236:239], v[192:195], a[60:63]\n"
    "s_waitcnt lgkmcnt(6)\n"
    "v_add_f32 v252, v252, v125\n"
    "v_add_f32 v252, v252, v126\n"
    "v_mfma_f32_16x16x32_bf16 a[72:75], v[240:243], v[192:195], a[72:75]\n"
    "ds_read_b64_tr_b16 v[224:225], v6 offset:8192\n"
    "v_add_f32 v252, v252, v127\n"
    "v_mfma_f32_16x16x32_bf16 a[76:79], v[240:243], v[196:199], a[76:79]\n"
    "ds_read_b64_tr_b16 v[226:227], v6 offset:10240\n"
    "v_add_f32 v252, v252, v136\n"
    "v_mfma_f32_16x16x32_bf16 a[80:83], v[240:243], v[200:203], a[80:83]\n"
    "s_waitcnt lgkmcnt(6)\n"
    "v_add_f32 v252, v252, v137\n"
    "v_add_f32 v252, v252, v138\n"
    "v_mfma_f32_16x16x32_bf16 a[92:95], v[244:247], v[200:203], a[92:95]\n"
    "ds_read_b64_tr_b16 v[228:229], v7 offset:8192\n"
    "v_add_f32 v252, v252, v139\n"
    "v_mfma_f32_16x16x32_bf16 a[88:91], v[244:247], v[196:199], a[88:91]\n"
    "ds_read_b64_tr_b16 v[230:231], v7 offset:10240\n"
    "v_add_f32 v253, v253, v128\n"
    "v_mfma_f32_16x16x32_bf16 a[84:87], v[244:247], v[192:195], a[84:87]\n"
    "s_waitcnt lgkmcnt(6)\n"
    "v_add_f32 v253, v253, v129\n"
    "v_add_f32 v253, v253, v130\n"
    "v_mfma_f32_16x16x32_bf16 a[96:99], v[216:219], v[192:195], a[96:99]\n"
    "ds_read_b64_tr_b16 v[232:233], v4 offset:12288\n"
    "v_add_f32 v253, v253, v131\n"
    "v_mfma_f32_16x16x32_bf16 a[100:103], v[216:219], v[196:199], a[100:103]\n"
    "ds_read_b64_tr_b16 v[234:235], v4 offset:14336\n"
    "v_add_f32 v253, v253, v140\n"
    "v_mfma_f32_16x16x32_bf16 a[104:107], v[216:219], v[200:203], a[104:107]\n"
    "s_waitcnt lgkmcnt(6)\n"
    "v_add_f32 v253, v253, v141\n"
    "v_add_f32 v253, v253, v142\n"
    "v_mfma_f32_16x16x32_bf16 a[116:119], v[220:223], v[200:203], a[116:119]\n"
    "ds_read_b64_tr_b16 v[236:237], v5 offset:12288\n"
    "v_add_f32 v253, v253, v143\n"
    "v_mfma_f32_16x16x32_bf16 a[112:115], v[220:223], v[196:199], a[112:115]\n"
    "ds_read_b64_tr_b16 v[238:239], v5 offset:14336\n"
    "v_add_u32 v4, 0xfffdc000, v4\n"
    "v_mfma_f32_16x16x32_bf16 a[108:111], v[220:223], v[192:195], a[108:111]\n"
    "s_waitcnt lgkmcnt(6)\n"
    "v_add_u32 v5, 0xfffdc000, v5\n"
    "v_mfma_f32_16x16x32_bf16 a[120:123], v[224:227], v[192:195], a[120:123]\n"
    "ds_read_b64_tr_b16 v[240:241], v6 offset:12288\n"
    "v_mfma_f32_16x16x32_bf16 a[124:127], v[224:227], v[196:199], a[124:127]\n"
    "ds_read_b64_tr_b16 v[242:243], v6 offset:14336\n"
    "v_add_u32 v6, 0xfffdc000, v6\n"
    "v_mfma_f32_16x16x32_bf16 a[128:131], v[224:227], v[200:203], a[128:131]\n"
    "s_waitcnt lgkmcnt(6)\n"
    "v_mfma_f32_16x16x32_bf16 a[140:143], v[228:231], v[200:203], a[140:143]\n"
    "ds_read_b64_tr_b16 v[244:245], v7 offset:12288\n"
    "v_mfma_f32_16x16x32_bf16 a[136:139], v[228:231], v[196:199], a[136:139]\n"
    "ds_read_b64_tr_b16 v[246:247], v7 offset:14336\n"
    "v_add_u32 v7, 0xfffdc000, v7\n"
    "v_mfma_f32_16x16x32_bf16 a[132:135], v[228:231], v[192:195], a[132:135]\n"
    "s_waitcnt lgkmcnt(6)\n"
    "v_mfma_f32_16x16x32_bf16 a[144:147], v[232:235], v[192:195], a[144:147]\n"
    "s_barrier\n"
    "ds_read_b64_tr_b16 v[216:217], v4 offset:0\n"
    "v_mfma_f32_16x16x32_bf16 a[148:151], v[232:235], v[196:199], a[148:151]\n"
    "ds_read_b64_tr_b16 v[218:219], v4 offset:2048\n"
    "v_mfma_f32_16x16x32_bf16 a[152:155], v[232:235], v[200:203], a[152:155]\n"
    "s_waitcnt lgkmcnt(6)\n"
    "v_mfma_f32_16x16x32_bf16 a[164:167], v[236:239], v[200:203], a[164:167]\n"
    "ds_read_b64_tr_b16 v[220:221], v5 offset:0\n"
    "v_mfma_f32_16x16x32_bf16 a[160:163], v[236:239], v[196:199], a[160:163]\n"
    "ds_read_b64_tr_b16 v[222:223], v5 offset:2048\n"
    "v_mfma_f32_16x16x32_bf16 a[156:159], v[236:239], v[192:195], a[156:159]\n"
    "s_waitcnt lgkmcnt(6)\n"
    "v_mfma_f32_16x16x32_bf16 a[168:171], v[240:243], v[192:195], a[168:171]\n"
    "ds_read_b64_tr_b16 v[224:225], v6 offset:0\n"
    "v_mfma_f32_16x16x32_bf16 a[172:175], v[240:243], v[196:199], a[172:175]\n"
    "ds_read_b64_tr_b16 v[226:227], v6 offset:2048\n"
    "v_mfma_f32_16x16x32_bf16 a[176:179], v[240:243], v[200:203], a[176:179]\n"
    "s_waitcnt lgkmcnt(6)\n"
    "v_mfma_f32_16x16x32_bf16 a[188:191], v[244:247], v[200:203], a[188:191]\n"
    "ds_read_b64_tr_b16 v[228:229], v7 offset:0\n"
    "v_mfma_f32_16x16x32_bf16 a[184:187], v[244:247], v[196:199], a[184:187]\n"
    "ds_read_b64_tr_b16 v[230:231], v7 offset:2048\n"
    "v_mfma_f32_16x16x32_bf16 a[180:183], v[244:247], v[192:195], a[180:183]\n"
    "s_waitcnt lgkmcnt(6)\n"
    "s_cbranch_vccnz TRAMP_P31_%=\n"
    "CONT_P31_%=:\n"
    "v_exp_f32 v168, v168\n"
    "v_exp_f32 v169, v169\n"
    "v_exp_f32 v170, v170\n"
    "v_exp_f32 v171, v171\n"
    "v_exp_f32 v180, v180\n"
    "v_exp_f32 v181, v181\n"
    "v_exp_f32 v182, v182\n"
    "v_exp_f32 v183, v183\n"
    "v_exp_f32 v172, v172\n"
    "v_exp_f32 v173, v173\n"
    "v_exp_f32 v174, v174\n"
    "v_exp_f32 v175, v175\n"
    "v_exp_f32 v184, v184\n"
    "v_exp_f32 v185, v185\n"
    "v_exp_f32 v186, v186\n"
    "v_exp_f32 v187, v187\n"
    "v_exp_f32 v176, v176\n"
    "v_exp_f32 v177, v177\n"
    "v_exp_f32 v178, v178\n"
    "v_exp_f32 v179, v179\n"
    "v_exp_f32 v188, v188\n"
    "v_exp_f32 v189, v189\n"
    "v_exp_f32 v190, v190\n"
    "v_exp_f32 v191, v191\n"
    "v_cvt_pk_bf16_f32 v192, v168, v169\n"
    "v_cvt_pk_bf16_f32 v193, v170, v171\n"
    "v_cvt_pk_bf16_f32 v194, v180, v181\n"
    "v_cvt_pk_bf16_f32 v195, v182, v183\n"
    "v_cvt_pk_bf16_f32 v196, v172, v173\n"
    "v_cvt_pk_bf16_f32 v197, v174, v175\n"
    "v_cvt_pk_bf16_f32 v198, v184, v185\n"
    "v_cvt_pk_bf16_f32 v199, v186, v187\n"
    "v_cvt_pk_bf16_f32 v200, v176, v177\n"
    "v_cvt_pk_bf16_f32 v201, v178, v179\n"
    "v_cvt_pk_bf16_f32 v202, v188, v189\n"
    "v_cvt_pk_bf16_f32 v203, v190, v191\n"
    "s_nop 0\n"
    "v_mfma_f32_16x16x32_bf16 a[0:3], v[216:219], v[192:195], a[0:3]\n"
    "ds_read_b64_tr_b16 v[232:233], v4 offset:4096\n"
    "v_add_f32 v9, v9, v168\n"
    "v_mfma_f32_16x16x32_bf16 a[4:7], v[216:219], v[196:199], a[4:7]\n"
    "ds_read_b64_tr_b16 v[234:235], v4 offset:6144\n"
    "v_add_f32 v9, v9, v169\n"
    "v_mfma_f32_16x16x32_bf16 a[8:11], v[216:219], v[200:203], a[8:11]\n"
    "s_waitcnt lgkmcnt(6)\n"
    "v_add_f32 v9, v9, v170\n"
    "v_add_f32 v9, v9, v171\n"
    "v_mfma_f32_16x16x32_bf16 a[20:23], v[220:223], v[200:203], a[20:23]\n"
    "ds_read_b64_tr_b16 v[236:237], v5 offset:4096\n"
    "v_add_f32 v9, v9, v180\n"
    "v_mfma_f32_16x16x32_bf16 a[16:19], v[220:223], v[196:199], a[16:19]\n"
    "ds_read_b64_tr_b16 v[238:239], v5 offset:6144\n"
    "v_add_f32 v9, v9, v181\n"
    "v_mfma_f32_16x16x32_bf16 a[12:15], v[220:223], v[192:195], a[12:15]\n"
    "s_waitcnt lgkmcnt(6)\n"
    "v_add_f32 v9, v9, v182\n"
    "v_add_f32 v9, v9, v183\n"
    "v_mfma_f32_16x16x32_bf16 a[24:27], v[224:227], v[192:195], a[24:27]\n"
    "ds_read_b64_tr_b16 v[240:241], v6 offset:4096\n"
    "v_add_f32 v252, v252, v172\n"
    "v_mfma_f32_16x16x32_bf16 a[28:31], v[224:227], v[196:199], a[28:31]\n"
    "ds_read_b64_tr_b16 v[242:243], v6 offset:6144\n"
    "v_add_f32 v252, v252, v173\n"
    "v_mfma_f32_16x16x32_bf16 a[32:35], v[224:227], v[200:203], a[32:35]\n"
    "s_waitcnt lgkmcnt(6)\n"
    "v_add_f32 v252, v252, v174\n"
    "v_add_f32 v252, v252, v175\n"
    "v_mfma_f32_16x16x32_bf16 a[44:47], v[228:231], v[200:203], a[44:47]\n"
    "ds_read_b64_tr_b16 v[244:245], v7 offset:4096\n"
    "v_add_f32 v252, v252, v184\n"
    "v_mfma_f32_16x16x32_bf16 a[40:43], v[228:231], v[196:199], a[40:43]\n"
    "ds_read_b64_tr_b16 v[246:247], v7 offset:6144\n"
    "v_add_f32 v252, v252, v185\n"
    "v_mfma_f32_16x16x32_bf16 a[36:39], v[228:231], v[192:195], a[36:39]\n"
    "s_waitcnt lgkmcnt(6)\n"
    "v_add_f32 v252, v252, v186\n"
    "v_add_f32 v252, v252, v187\n"
    "v_mfma_f32_16x16x32_bf16 a[48:51], v[232:235], v[192:195], a[48:51]\n"
    "ds_read_b64_tr_b16 v[216:217], v4 offset:8192\n"
    "v_add_f32 v253, v253, v176\n"
    "v_mfma_f32_16x16x32_bf16 a[52:55], v[232:235], v[196:199], a[52:55]\n"
    "ds_read_b64_tr_b16 v[218:219], v4 offset:10240\n"
    "v_add_f32 v253, v253, v177\n"
    "v_mfma_f32_16x16x32_bf16 a[56:59], v[232:235], v[200:203], a[56:59]\n"
    "s_waitcnt lgkmcnt(6)\n"
    "v_add_f32 v253, v253, v178\n"
    "v_add_f32 v253, v253, v179\n"
    "v_mfma_f32_16x16x32_bf16 a[68:71], v[236:239], v[200:203], a[68:71]\n"
    "ds_read_b64_tr_b16 v[220:221], v5 offset:8192\n"
    "v_add_f32 v253, v253, v188\n"
    "v_mfma_f32_16x16x32_bf16 a[64:67], v[236:239], v[196:199], a[64:67]\n"
    "ds_read_b64_tr_b16 v[222:223], v5 offset:10240\n"
    "v_add_f32 v253, v253, v189\n"
    "v_mfma_f32_16x16x32_bf16 a[60:63], v[236:239], v[192:195], a[60:63]\n"
    "s_waitcnt lgkmcnt(6)\n"
    "v_add_f32 v253, v253, v190\n"
    "v_add_f32 v253, v253, v191\n"
    "v_mfma_f32_16x16x32_bf16 a[72:75], v[240:243], v[192:195], a[72:75]\n"
    "ds_read_b64_tr_b16 v[224:225], v6 offset:8192\n"
    "v_accvgpr_read_b32 v15, a0\n"
    "v_mfma_f32_16x16x32_bf16 a[76:79], v[240:243], v[196:199], a[76:79]\n"
    "ds_read_b64_tr_b16 v[226:227], v6 offset:10240\n"
    "v_accvgpr_read_b32 v16, a1\n"
    "v_mfma_f32_16x16x32_bf16 a[80:83], v[240:243], v[200:203], a[80:83]\n"
    "s_waitcnt lgkmcnt(6)\n"
    "v_cvt_pk_bf16_f32 v24, v15, v16\n"
    "v_accvgpr_read_b32 v17, a2\n"
    "v_mfma_f32_16x16x32_bf16 a[92:95], v[244:247], v[200:203], a[92:95]\n"
    "ds_read_b64_tr_b16 v[228:229], v7 offset:8192\n"
    "v_accvgpr_read_b32 v18, a3\n"
    "v_mfma_f32_16x16x32_bf16 a[88:91], v[244:247], v[196:199], a[88:91]\n"
    "ds_read_b64_tr_b16 v[230:231], v7 offset:10240\n"
    "v_cvt_pk_bf16_f32 v25, v17, v18\n"
    "v_mfma_f32_16x16x32_bf16 a[84:87], v[244:247], v[192:195], a[84:87]\n"
    "s_waitcnt lgkmcnt(6)\n"
    "v_accvgpr_read_b32 v19, a12\n"
    "v_accvgpr_read_b32 v20, a13\n"
    "v_mfma_f32_16x16x32_bf16 a[96:99], v[216:219], v[192:195], a[96:99]\n"
    "ds_read_b64_tr_b16 v[232:233], v4 offset:12288\n"
    "v_cvt_pk_bf16_f32 v26, v19, v20\n"
    "v_mfma_f32_16x16x32_bf16 a[100:103], v[216:219], v[196:199], a[100:103]\n"
    "ds_read_b64_tr_b16 v[234:235], v4 offset:14336\n"
    "v_add_u32 v4, 0x4000, v4\n"
    "v_mfma_f32_16x16x32_bf16 a[104:107], v[216:219], v[200:203], a[104:107]\n"
    "s_waitcnt lgkmcnt(6)\n"
    "v_accvgpr_read_b32 v21, a14\n"
    "v_accvgpr_read_b32 v22, a15\n"
    "v_mfma_f32_16x16x32_bf16 a[116:119], v[220:223], v[200:203], a[116:119]\n"
    "ds_read_b64_tr_b16 v[236:237], v5 offset:12288\n"
    "v_cvt_pk_bf16_f32 v27, v21, v22\n"
    "v_mfma_f32_16x16x32_bf16 a[112:115], v[220:223], v[196:199], a[112:115]\n"
    "ds_read_b64_tr_b16 v[238:239], v5 offset:14336\n"
    "v_add_u32 v5, 0x4000, v5\n"
    "v_mfma_f32_16x16x32_bf16 a[108:111], v[220:223], v[192:195], a[108:111]\n"
    "s_waitcnt lgkmcnt(6)\n"
    "v_accvgpr_read_b32 v248, a4\n"
    "v_accvgpr_read_b32 v249, a5\n"
    "v_mfma_f32_16x16x32_bf16 a[120:123], v[224:227], v[192:195], a[120:123]\n"
    "ds_read_b64_tr_b16 v[240:241], v6 offset:12288\n"
    "v_cvt_pk_bf16_f32 v28, v248, v249\n"
    "v_mfma_f32_16x16x32_bf16 a[124:127], v[224:227], v[196:199], a[124:127]\n"
    "ds_read_b64_tr_b16 v[242:243], v6 offset:14336\n"
    "v_add_u32 v6, 0x4000, v6\n"
    "v_mfma_f32_16x16x32_bf16 a[128:131], v[224:227], v[200:203], a[128:131]\n"
    "s_waitcnt lgkmcnt(6)\n"
    "v_accvgpr_read_b32 v250, a6\n"
    "v_accvgpr_read_b32 v251, a7\n"
    "v_mfma_f32_16x16x32_bf16 a[140:143], v[228:231], v[200:203], a[140:143]\n"
    "ds_read_b64_tr_b16 v[244:245], v7 offset:12288\n"
    "v_cvt_pk_bf16_f32 v29, v250, v251\n"
    "v_mfma_f32_16x16x32_bf16 a[136:139], v[228:231], v[196:199], a[136:139]\n"
    "ds_read_b64_tr_b16 v[246:247], v7 offset:14336\n"
    "v_add_u32 v7, 0x4000, v7\n"
    "v_mfma_f32_16x16x32_bf16 a[132:135], v[228:231], v[192:195], a[132:135]\n"
    "s_waitcnt lgkmcnt(6)\n"
    "v_accvgpr_read_b32 v15, a16\n"
    "v_accvgpr_read_b32 v16, a17\n"
    "v_mfma_f32_16x16x32_bf16 a[144:147], v[232:235], v[192:195], a[144:147]\n"
    "s_barrier\n"
    "ds_read_b64_tr_b16 v[216:217], v4 offset:0\n"
    "v_cvt_pk_bf16_f32 v30, v15, v16\n"
    "v_mfma_f32_16x16x32_bf16 a[148:151], v[232:235], v[196:199], a[148:151]\n"
    "ds_read_b64_tr_b16 v[218:219], v4 offset:2048\n"
    "v_accvgpr_read_b32 v17, a18\n"
    "v_mfma_f32_16x16x32_bf16 a[152:155], v[232:235], v[200:203], a[152:155]\n"
    "s_waitcnt lgkmcnt(6)\n"
    "v_accvgpr_read_b32 v18, a19\n"
    "v_cvt_pk_bf16_f32 v31, v17, v18\n"
    "v_mfma_f32_16x16x32_bf16 a[164:167], v[236:239], v[200:203], a[164:167]\n"
    "ds_read_b64_tr_b16 v[220:221], v5 offset:0\n"
    "v_accvgpr_read_b32 v19, a8\n"
    "v_mfma_f32_16x16x32_bf16 a[160:163], v[236:239], v[196:199], a[160:163]\n"
    "ds_read_b64_tr_b16 v[222:223], v5 offset:2048\n"
    "v_accvgpr_read_b32 v20, a9\n"
    "v_mfma_f32_16x16x32_bf16 a[156:159], v[236:239], v[192:195], a[156:159]\n"
    "s_waitcnt lgkmcnt(6)\n"
    "v_cvt_pk_bf16_f32 v32, v19, v20\n"
    "v_accvgpr_read_b32 v21, a10\n"
    "v_mfma_f32_16x16x32_bf16 a[168:171], v[240:243], v[192:195], a[168:171]\n"
    "ds_read_b64_tr_b16 v[224:225], v6 offset:0\n"
    "v_accvgpr_read_b32 v22, a11\n"
    "v_mfma_f32_16x16x32_bf16 a[172:175], v[240:243], v[196:199], a[172:175]\n"
    "ds_read_b64_tr_b16 v[226:227], v6 offset:2048\n"
    "v_cvt_pk_bf16_f32 v33, v21, v22\n"
    "v_mfma_f32_16x16x32_bf16 a[176:179], v[240:243], v[200:203], a[176:179]\n"
    "s_waitcnt lgkmcnt(6)\n"
    "v_accvgpr_read_b32 v248, a20\n"
    "v_accvgpr_read_b32 v249, a21\n"
    "v_mfma_f32_16x16x32_bf16 a[188:191], v[244:247], v[200:203], a[188:191]\n"
    "ds_read_b64_tr_b16 v[228:229], v7 offset:0\n"
    "v_cvt_pk_bf16_f32 v34, v248, v249\n"
    "v_mfma_f32_16x16x32_bf16 a[184:187], v[244:247], v[196:199], a[184:187]\n"
    "ds_read_b64_tr_b16 v[230:231], v7 offset:2048\n"
    "v_accvgpr_read_b32 v250, a22\n"
    "v_mfma_f32_16x16x32_bf16 a[180:183], v[244:247], v[192:195], a[180:183]\n"
    "s_waitcnt lgkmcnt(6)\n"
    "v_accvgpr_read_b32 v251, a23\n"
    "v_cvt_pk_bf16_f32 v35, v250, v251\n"
    "v_mfma_f32_16x16x32_bf16 v[120:123], v[216:219], v[24:27], 0\n"
    "ds_read_b64_tr_b16 v[232:233], v4 offset:4096\n"
    "v_accvgpr_read_b32 v15, a24\n"
    "v_mfma_f32_16x16x32_bf16 v[124:127], v[216:219], v[28:31], 0\n"
    "ds_read_b64_tr_b16 v[234:235], v4 offset:6144\n"
    "v_accvgpr_read_b32 v16, a25\n"
    "v_mfma_f32_16x16x32_bf16 v[128:131], v[216:219], v[32:35], 0\n"
    "s_waitcnt lgkmcnt(6)\n"
    "v_cvt_pk_bf16_f32 v36, v15, v16\n"
    "v_accvgpr_read_b32 v17, a26\n"
    "v_mfma_f32_16x16x32_bf16 v[140:143], v[220:223], v[32:35], 0\n"
    "ds_read_b64_tr_b16 v[236:237], v5 offset:4096\n"
    "v_accvgpr_read_b32 v18, a27\n"
    "v_mfma_f32_16x16x32_bf16 v[136:139], v[220:223], v[28:31], 0\n"
    "ds_read_b64_tr_b16 v[238:239], v5 offset:6144\n"
    "v_cvt_pk_bf16_f32 v37, v17, v18\n"
    "v_mfma_f32_16x16x32_bf16 v[132:135], v[220:223], v[24:27], 0\n"
    "s_waitcnt lgkmcnt(6)\n"
    "v_accvgpr_read_b32 v19, a36\n"
    "v_accvgpr_read_b32 v20, a37\n"
    "v_mfma_f32_16x16x32_bf16 v[144:147], v[224:227], v[24:27], 0\n"
    "ds_read_b64_tr_b16 v[240:241], v6 offset:4096\n"
    "v_cvt_pk_bf16_f32 v38, v19, v20\n"
    "v_mfma_f32_16x16x32_bf16 v[148:151], v[224:227], v[28:31], 0\n"
    "ds_read_b64_tr_b16 v[242:243], v6 offset:6144\n"
    "v_accvgpr_read_b32 v21, a38\n"
    "v_mfma_f32_16x16x32_bf16 v[152:155], v[224:227], v[32:35], 0\n"
    "s_waitcnt lgkmcnt(6)\n"
    "v_accvgpr_read_b32 v22, a39\n"
    "v_cvt_pk_bf16_f32 v39, v21, v22\n"
    "v_mfma_f32_16x16x32_bf16 v[164:167], v[228:231], v[32:35], 0\n"
    "ds_read_b64_tr_b16 v[244:245], v7 offset:4096\n"
    "v_accvgpr_read_b32 v248, a28\n"
    "v_mfma_f32_16x16x32_bf16 v[160:163], v[228:231], v[28:31], 0\n"
    "ds_read_b64_tr_b16 v[246:247], v7 offset:6144\n"
    "v_accvgpr_read_b32 v249, a29\n"
    "v_mfma_f32_16x16x32_bf16 v[156:159], v[228:231], v[24:27], 0\n"
    "s_waitcnt lgkmcnt(6)\n"
    "v_cvt_pk_bf16_f32 v40, v248, v249\n"
    "v_accvgpr_read_b32 v250, a30\n"
    "v_accvgpr_read_b32 v251, a31\n"
    "v_cvt_pk_bf16_f32 v41, v250, v251\n"
    "v_accvgpr_read_b32 v15, a40\n"
    "v_accvgpr_read_b32 v16, a41\n"
    "v_cvt_pk_bf16_f32 v42, v15, v16\n"
    "v_accvgpr_read_b32 v17, a42\n"
    "v_accvgpr_read_b32 v18, a43\n"
    "v_cvt_pk_bf16_f32 v43, v17, v18\n"
    "v_mfma_f32_16x16x32_bf16 v[120:123], v[232:235], v[36:39], v[120:123]\n"
    "ds_read_b64_tr_b16 v[216:217], v4 offset:8192\n"
    "v_accvgpr_read_b32 v19, a32\n"
    "v_accvgpr_read_b32 v20, a33\n"
    "v_cvt_pk_bf16_f32 v44, v19, v20\n"
    "v_accvgpr_read_b32 v21, a34\n"
    "v_accvgpr_read_b32 v22, a35\n"
    "v_cvt_pk_bf16_f32 v45, v21, v22\n"
    "v_accvgpr_read_b32 v248, a44\n"
    "v_accvgpr_read_b32 v249, a45\n"
    "v_cvt_pk_bf16_f32 v46, v248, v249\n"
    "v_accvgpr_read_b32 v250, a46\n"
    "v_accvgpr_read_b32 v251, a47\n"
    "v_cvt_pk_bf16_f32 v47, v250, v251\n"
    "v_mfma_f32_16x16x32_bf16 v[124:127], v[232:235], v[40:43], v[124:127]\n"
    "ds_read_b64_tr_b16 v[218:219], v4 offset:10240\n"
    "v_accvgpr_read_b32 v15, a48\n"
    "v_mfma_f32_16x16x32_bf16 v[128:131], v[232:235], v[44:47], v[128:131]\n"
    "s_waitcnt lgkmcnt(6)\n"
    "v_accvgpr_read_b32 v16, a49\n"
    "v_cvt_pk_bf16_f32 v48, v15, v16\n"
    "v_mfma_f32_16x16x32_bf16 v[140:143], v[236:239], v[44:47], v[140:143]\n"
    "ds_read_b64_tr_b16 v[220:221], v5 offset:8192\n"
    "v_accvgpr_read_b32 v17, a50\n"
    "v_mfma_f32_16x16x32_bf16 v[136:139], v[236:239], v[40:43], v[136:139]\n"
    "ds_read_b64_tr_b16 v[222:223], v5 offset:10240\n"
    "v_accvgpr_read_b32 v18, a51\n"
    "v_mfma_f32_16x16x32_bf16 v[132:135], v[236:239], v[36:39], v[132:135]\n"
    "s_waitcnt lgkmcnt(6)\n"
    "v_cvt_pk_bf16_f32 v49, v17, v18\n"
    "v_accvgpr_read_b32 v19, a60\n"
    "v_mfma_f32_16x16x32_bf16 v[144:147], v[240:243], v[36:39], v[144:147]\n"
    "ds_read_b64_tr_b16 v[224:225], v6 offset:8192\n"
    "v_accvgpr_read_b32 v20, a61\n"
    "v_mfma_f32_16x16x32_bf16 v[148:151], v[240:243], v[40:43], v[148:151]\n"
    "ds_read_b64_tr_b16 v[226:227], v6 offset:10240\n"
    "v_cvt_pk_bf16_f32 v50, v19, v20\n"
    "v_mfma_f32_16x16x32_bf16 v[152:155], v[240:243], v[44:47], v[152:155]\n"
    "s_waitcnt lgkmcnt(6)\n"
    "v_accvgpr_read_b32 v21, a62\n"
    "v_accvgpr_read_b32 v22, a63\n"
    "v_mfma_f32_16x16x32_bf16 v[164:167], v[244:247], v[44:47], v[164:167]\n"
    "ds_read_b64_tr_b16 v[228:229], v7 offset:8192\n"
    "v_cvt_pk_bf16_f32 v51, v21, v22\n"
    "v_mfma_f32_16x16x32_bf16 v[160:163], v[244:247], v[40:43], v[160:163]\n"
    "ds_read_b64_tr_b16 v[230:231], v7 offset:10240\n"
    "v_accvgpr_read_b32 v248, a52\n"
    "v_mfma_f32_16x16x32_bf16 v[156:159], v[244:247], v[36:39], v[156:159]\n"
    "s_waitcnt lgkmcnt(6)\n"
    "v_accvgpr_read_b32 v249, a53\n"
    "v_cvt_pk_bf16_f32 v52, v248, v249\n"
    "v_accvgpr_read_b32 v250, a54\n"
    "v_accvgpr_read_b32 v251, a55\n"
    "v_cvt_pk_bf16_f32 v53, v250, v251\n"
    "v_accvgpr_read_b32 v15, a64\n"
    "v_accvgpr_read_b32 v16, a65\n"
    "v_cvt_pk_bf16_f32 v54, v15, v16\n"
    "v_accvgpr_read_b32 v17, a66\n"
    "v_accvgpr_read_b32 v18, a67\n"
    "v_cvt_pk_bf16_f32 v55, v17, v18\n"
    "v_mfma_f32_16x16x32_bf16 v[120:123], v[216:219], v[48:51], v[120:123]\n"
    "ds_read_b64_tr_b16 v[232:233], v4 offset:12288\n"
    "v_accvgpr_read_b32 v19, a56\n"
    "v_accvgpr_read_b32 v20, a57\n"
    "v_cvt_pk_bf16_f32 v56, v19, v20\n"
    "v_accvgpr_read_b32 v21, a58\n"
    "v_accvgpr_read_b32 v22, a59\n"
    "v_cvt_pk_bf16_f32 v57, v21, v22\n"
    "v_accvgpr_read_b32 v248, a68\n"
    "v_accvgpr_read_b32 v249, a69\n"
    "v_cvt_pk_bf16_f32 v58, v248, v249\n"
    "v_accvgpr_read_b32 v250, a70\n"
    "v_accvgpr_read_b32 v251, a71\n"
    "v_cvt_pk_bf16_f32 v59, v250, v251\n"
    "v_mfma_f32_16x16x32_bf16 v[124:127], v[216:219], v[52:55], v[124:127]\n"
    "ds_read_b64_tr_b16 v[234:235], v4 offset:14336\n"
    "v_add_u32 v4, 0x4000, v4\n"
    "v_mfma_f32_16x16x32_bf16 v[128:131], v[216:219], v[56:59], v[128:131]\n"
    "s_waitcnt lgkmcnt(6)\n"
    "v_accvgpr_read_b32 v15, a72\n"
    "v_accvgpr_read_b32 v16, a73\n"
    "v_mfma_f32_16x16x32_bf16 v[140:143], v[220:223], v[56:59], v[140:143]\n"
    "ds_read_b64_tr_b16 v[236:237], v5 offset:12288\n"
    "v_cvt_pk_bf16_f32 v60, v15, v16\n"
    "v_mfma_f32_16x16x32_bf16 v[136:139], v[220:223], v[52:55], v[136:139]\n"
    "ds_read_b64_tr_b16 v[238:239], v5 offset:14336\n"
    "v_add_u32 v5, 0x4000, v5\n"
    "v_mfma_f32_16x16x32_bf16 v[132:135], v[220:223], v[48:51], v[132:135]\n"
    "s_waitcnt lgkmcnt(6)\n"
    "v_accvgpr_read_b32 v17, a74\n"
    "v_accvgpr_read_b32 v18, a75\n"
    "v_mfma_f32_16x16x32_bf16 v[144:147], v[224:227], v[48:51], v[144:147]\n"
    "ds_read_b64_tr_b16 v[240:241], v6 offset:12288\n"
    "v_cvt_pk_bf16_f32 v61, v17, v18\n"
    "v_mfma_f32_16x16x32_bf16 v[148:151], v[224:227], v[52:55], v[148:151]\n"
    "ds_read_b64_tr_b16 v[242:243], v6 offset:14336\n"
    "v_add_u32 v6, 0x4000, v6\n"
    "v_mfma_f32_16x16x32_bf16 v[152:155], v[224:227], v[56:59], v[152:155]\n"
    "s_waitcnt lgkmcnt(6)\n"
    "v_accvgpr_read_b32 v19, a84\n"
    "v_accvgpr_read_b32 v20, a85\n"
    "v_mfma_f32_16x16x32_bf16 v[164:167], v[228:231], v[56:59], v[164:167]\n"
    "ds_read_b64_tr_b16 v[244:245], v7 offset:12288\n"
    "v_cvt_pk_bf16_f32 v62, v19, v20\n"
    "v_mfma_f32_16x16x32_bf16 v[160:163], v[228:231], v[52:55], v[160:163]\n"
    "ds_read_b64_tr_b16 v[246:247], v7 offset:14336\n"
    "v_add_u32 v7, 0x4000, v7\n"
    "v_accvgpr_read_b32 v21, a86\n"
    "v_accvgpr_read_b32 v22, a87\n"
    "v_cvt_pk_bf16_f32 v63, v21, v22\n"
    "v_mfma_f32_16x16x32_bf16 v[156:159], v[228:231], v[48:51], v[156:159]\n"
    "s_waitcnt lgkmcnt(6)\n"
    "v_accvgpr_read_b32 v248, a76\n"
    "v_accvgpr_read_b32 v249, a77\n"
    "v_cvt_pk_bf16_f32 v64, v248, v249\n"
    "v_accvgpr_read_b32 v250, a78\n"
    "v_accvgpr_read_b32 v251, a79\n"
    "v_cvt_pk_bf16_f32 v65, v250, v251\n"
    "v_accvgpr_read_b32 v15, a88\n"
    "v_accvgpr_read_b32 v16, a89\n"
    "v_cvt_pk_bf16_f32 v66, v15, v16\n"
    "v_accvgpr_read_b32 v17, a90\n"
    "v_accvgpr_read_b32 v18, a91\n"
    "v_cvt_pk_bf16_f32 v67, v17, v18\n"
    "v_mfma_f32_16x16x32_bf16 v[120:123], v[232:235], v[60:63], v[120:123]\n"
    "s_barrier\n"
    "ds_read_b64_tr_b16 v[216:217], v4 offset:0\n"
    "v_accvgpr_read_b32 v19, a80\n"
    "v_accvgpr_read_b32 v20, a81\n"
    "v_cvt_pk_bf16_f32 v68, v19, v20\n"
    "v_accvgpr_read_b32 v21, a82\n"
    "v_accvgpr_read_b32 v22, a83\n"
    "v_cvt_pk_bf16_f32 v69, v21, v22\n"
    "v_accvgpr_read_b32 v248, a92\n"
    "v_accvgpr_read_b32 v249, a93\n"
    "v_cvt_pk_bf16_f32 v70, v248, v249\n"
    "v_accvgpr_read_b32 v250, a94\n"
    "v_accvgpr_read_b32 v251, a95\n"
    "v_cvt_pk_bf16_f32 v71, v250, v251\n"
    "v_mfma_f32_16x16x32_bf16 v[124:127], v[232:235], v[64:67], v[124:127]\n"
    "ds_read_b64_tr_b16 v[218:219], v4 offset:2048\n"
    "v_accvgpr_read_b32 v15, a96\n"
    "v_mfma_f32_16x16x32_bf16 v[128:131], v[232:235], v[68:71], v[128:131]\n"
    "s_waitcnt lgkmcnt(6)\n"
    "v_accvgpr_read_b32 v16, a97\n"
    "v_cvt_pk_bf16_f32 v72, v15, v16\n"
    "v_mfma_f32_16x16x32_bf16 v[140:143], v[236:239], v[68:71], v[140:143]\n"
    "ds_read_b64_tr_b16 v[220:221], v5 offset:0\n"
    "v_accvgpr_read_b32 v17, a98\n"
    "v_mfma_f32_16x16x32_bf16 v[136:139], v[236:239], v[64:67], v[136:139]\n"
    "ds_read_b64_tr_b16 v[222:223], v5 offset:2048\n"
    "v_accvgpr_read_b32 v18, a99\n"
    "v_mfma_f32_16x16x32_bf16 v[132:135], v[236:239], v[60:63], v[132:135]\n"
    "s_waitcnt lgkmcnt(6)\n"
    "v_cvt_pk_bf16_f32 v73, v17, v18\n"
    "v_accvgpr_read_b32 v19, a108\n"
    "v_mfma_f32_16x16x32_bf16 v[144:147], v[240:243], v[60:63], v[144:147]\n"
    "ds_read_b64_tr_b16 v[224:225], v6 offset:0\n"
    "v_accvgpr_read_b32 v20, a109\n"
    "v_mfma_f32_16x16x32_bf16 v[148:151], v[240:243], v[64:67], v[148:151]\n"
    "ds_read_b64_tr_b16 v[226:227], v6 offset:2048\n"
    "v_cvt_pk_bf16_f32 v74, v19, v20\n"
    "v_mfma_f32_16x16x32_bf16 v[152:155], v[240:243], v[68:71], v[152:155]\n"
    "s_waitcnt lgkmcnt(6)\n"
    "v_accvgpr_read_b32 v21, a110\n"
    "v_accvgpr_read_b32 v22, a111\n"
    "v_mfma_f32_16x16x32_bf16 v[164:167], v[244:247], v[68:71], v[164:167]\n"
    "ds_read_b64_tr_b16 v[228:229], v7 offset:0\n"
    "v_cvt_pk_bf16_f32 v75, v21, v22\n"
    "v_mfma_f32_16x16x32_bf16 v[160:163], v[244:247], v[64:67], v[160:163]\n"
    "ds_read_b64_tr_b16 v[230:231], v7 offset:2048\n"
    "v_accvgpr_read_b32 v248, a100\n"
    "v_mfma_f32_16x16x32_bf16 v[156:159], v[244:247], v[60:63], v[156:159]\n"
    "s_waitcnt lgkmcnt(6)\n"
    "v_accvgpr_read_b32 v249, a101\n"
    "v_cvt_pk_bf16_f32 v76, v248, v249\n"
    "v_accvgpr_read_b32 v250, a102\n"
    "v_accvgpr_read_b32 v251, a103\n"
    "v_cvt_pk_bf16_f32 v77, v250, v251\n"
    "v_accvgpr_read_b32 v15, a112\n"
    "v_accvgpr_read_b32 v16, a113\n"
    "v_cvt_pk_bf16_f32 v78, v15, v16\n"
    "v_accvgpr_read_b32 v17, a114\n"
    "v_accvgpr_read_b32 v18, a115\n"
    "v_cvt_pk_bf16_f32 v79, v17, v18\n"
    "v_mfma_f32_16x16x32_bf16 v[120:123], v[216:219], v[72:75], v[120:123]\n"
    "ds_read_b64_tr_b16 v[232:233], v4 offset:4096\n"
    "v_accvgpr_read_b32 v19, a104\n"
    "v_accvgpr_read_b32 v20, a105\n"
    "v_cvt_pk_bf16_f32 v80, v19, v20\n"
    "v_accvgpr_read_b32 v21, a106\n"
    "v_accvgpr_read_b32 v22, a107\n"
    "v_cvt_pk_bf16_f32 v81, v21, v22\n"
    "v_accvgpr_read_b32 v248, a116\n"
    "v_accvgpr_read_b32 v249, a117\n"
    "v_cvt_pk_bf16_f32 v82, v248, v249\n"
    "v_accvgpr_read_b32 v250, a118\n"
    "v_accvgpr_read_b32 v251, a119\n"
    "v_cvt_pk_bf16_f32 v83, v250, v251\n"
    "v_mfma_f32_16x16x32_bf16 v[124:127], v[216:219], v[76:79], v[124:127]\n"
    "ds_read_b64_tr_b16 v[234:235], v4 offset:6144\n"
    "v_accvgpr_read_b32 v15, a120\n"
    "v_mfma_f32_16x16x32_bf16 v[128:131], v[216:219], v[80:83], v[128:131]\n"
    "s_waitcnt lgkmcnt(6)\n"
    "v_accvgpr_read_b32 v16, a121\n"
    "v_cvt_pk_bf16_f32 v84, v15, v16\n"
    "v_mfma_f32_16x16x32_bf16 v[140:143], v[220:223], v[80:83], v[140:143]\n"
    "ds_read_b64_tr_b16 v[236:237], v5 offset:4096\n"
    "v_accvgpr_read_b32 v17, a122\n"
    "v_mfma_f32_16x16x32_bf16 v[136:139], v[220:223], v[76:79], v[136:139]\n"
    "ds_read_b64_tr_b16 v[238:239], v5 offset:6144\n"
    "v_accvgpr_read_b32 v18, a123\n"
    "v_mfma_f32_16x16x32_bf16 v[132:135], v[220:223], v[72:75], v[132:135]\n"
    "s_waitcnt lgkmcnt(6)\n"
    "v_cvt_pk_bf16_f32 v85, v17, v18\n"
    "v_accvgpr_read_b32 v19, a132\n"
    "v_mfma_f32_16x16x32_bf16 v[144:147], v[224:227], v[72:75], v[144:147]\n"
    "ds_read_b64_tr_b16 v[240:241], v6 offset:4096\n"
    "v_accvgpr_read_b32 v20, a133\n"
    "v_mfma_f32_16x16x32_bf16 v[148:151], v[224:227], v[76:79], v[148:151]\n"
    "ds_read_b64_tr_b16 v[242:243], v6 offset:6144\n"
    "v_cvt_pk_bf16_f32 v86, v19, v20\n"
    "v_mfma_f32_16x16x32_bf16 v[152:155], v[224:227], v[80:83], v[152:155]\n"
    "s_waitcnt lgkmcnt(6)\n"
    "v_accvgpr_read_b32 v21, a134\n"
    "v_accvgpr_read_b32 v22, a135\n"
    "v_mfma_f32_16x16x32_bf16 v[164:167], v[228:231], v[80:83], v[164:167]\n"
    "ds_read_b64_tr_b16 v[244:245], v7 offset:4096\n"
    "v_cvt_pk_bf16_f32 v87, v21, v22\n"
    "v_mfma_f32_16x16x32_bf16 v[160:163], v[228:231], v[76:79], v[160:163]\n"
    "ds_read_b64_tr_b16 v[246:247], v7 offset:6144\n"
    "v_accvgpr_read_b32 v248, a124\n"
    "v_mfma_f32_16x16x32_bf16 v[156:159], v[228:231], v[72:75], v[156:159]\n"
    "s_waitcnt lgkmcnt(6)\n"
    "v_accvgpr_read_b32 v249, a125\n"
    "v_cvt_pk_bf16_f32 v88, v248, v249\n"
    "v_accvgpr_read_b32 v250, a126\n"
    "v_accvgpr_read_b32 v251, a127\n"
    "v_cvt_pk_bf16_f32 v89, v250, v251\n"
    "v_accvgpr_read_b32 v15, a136\n"
    "v_accvgpr_read_b32 v16, a137\n"
    "v_cvt_pk_bf16_f32 v90, v15, v16\n"
    "v_accvgpr_read_b32 v17, a138\n"
    "v_accvgpr_read_b32 v18, a139\n"
    "v_cvt_pk_bf16_f32 v91, v17, v18\n"
    "v_mfma_f32_16x16x32_bf16 v[120:123], v[232:235], v[84:87], v[120:123]\n"
    "ds_read_b64_tr_b16 v[216:217], v4 offset:8192\n"
    "v_mov_b32 v12, v9\n"
    "v_accvgpr_read_b32 v19, a128\n"
    "v_accvgpr_read_b32 v20, a129\n"
    "v_cvt_pk_bf16_f32 v92, v19, v20\n"
    "v_accvgpr_read_b32 v21, a130\n"
    "v_accvgpr_read_b32 v22, a131\n"
    "v_cvt_pk_bf16_f32 v93, v21, v22\n"
    "v_accvgpr_read_b32 v248, a140\n"
    "v_accvgpr_read_b32 v249, a141\n"
    "v_cvt_pk_bf16_f32 v94, v248, v249\n"
    "v_accvgpr_read_b32 v250, a142\n"
    "v_accvgpr_read_b32 v251, a143\n"
    "v_cvt_pk_bf16_f32 v95, v250, v251\n"
    "v_mfma_f32_16x16x32_bf16 v[124:127], v[232:235], v[88:91], v[124:127]\n"
    "ds_read_b64_tr_b16 v[218:219], v4 offset:10240\n"
    "v_mov_b32 v13, v252\n"
    "v_mfma_f32_16x16x32_bf16 v[128:131], v[232:235], v[92:95], v[128:131]\n"
    "s_waitcnt lgkmcnt(6)\n"
    "v_mov_b32 v14, v253\n"
    "v_accvgpr_read_b32 v15, a144\n"
    "v_mfma_f32_16x16x32_bf16 v[140:143], v[236:239], v[92:95], v[140:143]\n"
    "ds_read_b64_tr_b16 v[220:221], v5 offset:8192\n"
    "v_permlane32_swap_b32 v9, v12\n"
    "v_mfma_f32_16x16x32_bf16 v[136:139], v[236:239], v[88:91], v[136:139]\n"
    "ds_read_b64_tr_b16 v[222:223], v5 offset:10240\n"
    "v_permlane32_swap_b32 v252, v13\n"
    "v_mfma_f32_16x16x32_bf16 v[132:135], v[236:239], v[84:87], v[132:135]\n"
    "s_waitcnt lgkmcnt(6)\n"
    "v_permlane32_swap_b32 v253, v14\n"
    "v_add_f32 v9, v9, v12\n"
    "v_mfma_f32_16x16x32_bf16 v[144:147], v[240:243], v[84:87], v[144:147]\n"
    "ds_read_b64_tr_b16 v[224:225], v6 offset:8192\n"
    "v_add_f32 v252, v252, v13\n"
    "v_mfma_f32_16x16x32_bf16 v[148:151], v[240:243], v[88:91], v[148:151]\n"
    "ds_read_b64_tr_b16 v[226:227], v6 offset:10240\n"
    "v_add_f32 v253, v253, v14\n"
    "v_mfma_f32_16x16x32_bf16 v[152:155], v[240:243], v[92:95], v[152:155]\n"
    "s_waitcnt lgkmcnt(6)\n"
    "v_mov_b32 v12, v9\n"
    "v_mov_b32 v13, v252\n"
    "v_mfma_f32_16x16x32_bf16 v[164:167], v[244:247], v[92:95], v[164:167]\n"
    "ds_read_b64_tr_b16 v[228:229], v7 offset:8192\n"
    "v_mov_b32 v14, v253\n"
    "v_mfma_f32_16x16x32_bf16 v[160:163], v[244:247], v[88:91], v[160:163]\n"
    "ds_read_b64_tr_b16 v[230:231], v7 offset:10240\n"
    "v_permlane16_swap_b32 v9, v12\n"
    "v_accvgpr_read_b32 v16, a145\n"
    "v_cvt_pk_bf16_f32 v96, v15, v16\n"
    "v_accvgpr_read_b32 v17, a146\n"
    "v_accvgpr_read_b32 v18, a147\n"
    "v_cvt_pk_bf16_f32 v97, v17, v18\n"
    "v_accvgpr_read_b32 v19, a156\n"
    "v_accvgpr_read_b32 v20, a157\n"
    "v_cvt_pk_bf16_f32 v98, v19, v20\n"
    "v_accvgpr_read_b32 v21, a158\n"
    "v_accvgpr_read_b32 v22, a159\n"
    "v_cvt_pk_bf16_f32 v99, v21, v22\n"
    "v_mfma_f32_16x16x32_bf16 v[156:159], v[244:247], v[84:87], v[156:159]\n"
    "s_waitcnt lgkmcnt(6)\n"
    "v_permlane16_swap_b32 v252, v13\n"
    "v_accvgpr_read_b32 v248, a148\n"
    "v_accvgpr_read_b32 v249, a149\n"
    "v_cvt_pk_bf16_f32 v100, v248, v249\n"
    "v_accvgpr_read_b32 v250, a150\n"
    "v_accvgpr_read_b32 v251, a151\n"
    "v_cvt_pk_bf16_f32 v101, v250, v251\n"
    "v_accvgpr_read_b32 v15, a160\n"
    "v_accvgpr_read_b32 v16, a161\n"
    "v_cvt_pk_bf16_f32 v102, v15, v16\n"
    "v_accvgpr_read_b32 v17, a162\n"
    "v_accvgpr_read_b32 v18, a163\n"
    "v_cvt_pk_bf16_f32 v103, v17, v18\n"
    "v_mfma_f32_16x16x32_bf16 v[120:123], v[216:219], v[96:99], v[120:123]\n"
    "ds_read_b64_tr_b16 v[232:233], v4 offset:12288\n"
    "v_permlane16_swap_b32 v253, v14\n"
    "v_accvgpr_read_b32 v19, a152\n"
    "v_accvgpr_read_b32 v20, a153\n"
    "v_cvt_pk_bf16_f32 v104, v19, v20\n"
    "v_accvgpr_read_b32 v21, a154\n"
    "v_accvgpr_read_b32 v22, a155\n"
    "v_cvt_pk_bf16_f32 v105, v21, v22\n"
    "v_accvgpr_read_b32 v248, a164\n"
    "v_accvgpr_read_b32 v249, a165\n"
    "v_cvt_pk_bf16_f32 v106, v248, v249\n"
    "v_accvgpr_read_b32 v250, a166\n"
    "v_accvgpr_read_b32 v251, a167\n"
    "v_cvt_pk_bf16_f32 v107, v250, v251\n"
    "v_mfma_f32_16x16x32_bf16 v[124:127], v[216:219], v[100:103], v[124:127]\n"
    "ds_read_b64_tr_b16 v[234:235], v4 offset:14336\n"
    "v_add_u32 v4, 0x4000, v4\n"
    "v_mfma_f32_16x16x32_bf16 v[128:131], v[216:219], v[104:107], v[128:131]\n"
    "s_waitcnt lgkmcnt(6)\n"
    "v_add_f32 v9, v9, v12\n"
    "v_add_f32 v252, v252, v13\n"
    "v_mfma_f32_16x16x32_bf16 v[140:143], v[220:223], v[104:107], v[140:143]\n"
    "ds_read_b64_tr_b16 v[236:237], v5 offset:12288\n"
    "v_add_f32 v253, v253, v14\n"
    "v_mfma_f32_16x16x32_bf16 v[136:139], v[220:223], v[100:103], v[136:139]\n"
    "ds_read_b64_tr_b16 v[238:239], v5 offset:14336\n"
    "v_add_u32 v5, 0x4000, v5\n"
    "v_mfma_f32_16x16x32_bf16 v[132:135], v[220:223], v[96:99], v[132:135]\n"
    "s_waitcnt lgkmcnt(6)\n"
    "v_rcp_f32 v12, v9\n"
    "v_mfma_f32_16x16x32_bf16 v[144:147], v[224:227], v[96:99], v[144:147]\n"
    "ds_read_b64_tr_b16 v[240:241], v6 offset:12288\n"
    "v_accvgpr_read_b32 v15, a168\n"
    "v_mfma_f32_16x16x32_bf16 v[148:151], v[224:227], v[100:103], v[148:151]\n"
    "ds_read_b64_tr_b16 v[242:243], v6 offset:14336\n"
    "v_add_u32 v6, 0x4000, v6\n"
    "v_mfma_f32_16x16x32_bf16 v[152:155], v[224:227], v[104:107], v[152:155]\n"
    "s_waitcnt lgkmcnt(6)\n"
    "v_rcp_f32 v13, v252\n"
    "v_mfma_f32_16x16x32_bf16 v[164:167], v[228:231], v[104:107], v[164:167]\n"
    "ds_read_b64_tr_b16 v[244:245], v7 offset:12288\n"
    "v_accvgpr_read_b32 v16, a169\n"
    "v_mfma_f32_16x16x32_bf16 v[160:163], v[228:231], v[100:103], v[160:163]\n"
    "ds_read_b64_tr_b16 v[246:247], v7 offset:14336\n"
    "v_add_u32 v7, 0x4000, v7\n"
    "v_cvt_pk_bf16_f32 v108, v15, v16\n"
    "v_accvgpr_read_b32 v17, a170\n"
    "v_accvgpr_read_b32 v18, a171\n"
    "v_cvt_pk_bf16_f32 v109, v17, v18\n"
    "v_accvgpr_read_b32 v19, a180\n"
    "v_accvgpr_read_b32 v20, a181\n"
    "v_cvt_pk_bf16_f32 v110, v19, v20\n"
    "v_accvgpr_read_b32 v21, a182\n"
    "v_accvgpr_read_b32 v22, a183\n"
    "v_cvt_pk_bf16_f32 v111, v21, v22\n"
    "v_mfma_f32_16x16x32_bf16 v[156:159], v[228:231], v[96:99], v[156:159]\n"
    "s_waitcnt lgkmcnt(6)\n"
    "v_rcp_f32 v14, v253\n"
    "v_accvgpr_read_b32 v248, a172\n"
    "v_accvgpr_read_b32 v249, a173\n"
    "v_cvt_pk_bf16_f32 v112, v248, v249\n"
    "v_accvgpr_read_b32 v250, a174\n"
    "v_accvgpr_read_b32 v251, a175\n"
    "v_cvt_pk_bf16_f32 v113, v250, v251\n"
    "v_accvgpr_read_b32 v15, a184\n"
    "v_accvgpr_read_b32 v16, a185\n"
    "v_cvt_pk_bf16_f32 v114, v15, v16\n"
    "v_accvgpr_read_b32 v17, a186\n"
    "v_accvgpr_read_b32 v18, a187\n"
    "v_cvt_pk_bf16_f32 v115, v17, v18\n"
    "v_mfma_f32_16x16x32_bf16 v[120:123], v[232:235], v[108:111], v[120:123]\n"
    "s_barrier\n"
    "ds_read_b64_tr_b16 v[216:217], v4 offset:0\n"
    "v_accvgpr_read_b32 v19, a176\n"
    "v_accvgpr_read_b32 v20, a177\n"
    "v_cvt_pk_bf16_f32 v116, v19, v20\n"
    "v_accvgpr_read_b32 v21, a178\n"
    "v_accvgpr_read_b32 v22, a179\n"
    "v_cvt_pk_bf16_f32 v117, v21, v22\n"
    "v_accvgpr_read_b32 v248, a188\n"
    "v_accvgpr_read_b32 v249, a189\n"
    "v_cvt_pk_bf16_f32 v118, v248, v249\n"
    "v_accvgpr_read_b32 v250, a190\n"
    "v_accvgpr_read_b32 v251, a191\n"
    "v_cvt_pk_bf16_f32 v119, v250, v251\n"
    "v_mfma_f32_16x16x32_bf16 v[124:127], v[232:235], v[112:115], v[124:127]\n"
    "ds_read_b64_tr_b16 v[218:219], v4 offset:2048\n"
    "v_mfma_f32_16x16x32_bf16 v[128:131], v[232:235], v[116:119], v[128:131]\n"
    "s_waitcnt lgkmcnt(6)\n"
    "v_mfma_f32_16x16x32_bf16 v[140:143], v[236:239], v[116:119], v[140:143]\n"
    "ds_read_b64_tr_b16 v[220:221], v5 offset:0\n"
    "v_mfma_f32_16x16x32_bf16 v[136:139], v[236:239], v[112:115], v[136:139]\n"
    "ds_read_b64_tr_b16 v[222:223], v5 offset:2048\n"
    "v_mul_f32 v120, v12, v120\n"
    "v_mfma_f32_16x16x32_bf16 v[132:135], v[236:239], v[108:111], v[132:135]\n"
    "s_waitcnt lgkmcnt(6)\n"
    "v_mul_f32 v121, v12, v121\n"
    "v_mul_f32 v122, v12, v122\n"
    "v_mfma_f32_16x16x32_bf16 v[144:147], v[240:243], v[108:111], v[144:147]\n"
    "ds_read_b64_tr_b16 v[224:225], v4 offset:4096\n"
    "v_mul_f32 v123, v12, v123\n"
    "v_mfma_f32_16x16x32_bf16 v[148:151], v[240:243], v[112:115], v[148:151]\n"
    "ds_read_b64_tr_b16 v[226:227], v4 offset:6144\n"
    "v_cvt_pk_bf16_f32 v120, v120, v121\n"
    "v_mfma_f32_16x16x32_bf16 v[152:155], v[240:243], v[116:119], v[152:155]\n"
    "s_waitcnt lgkmcnt(6)\n"
    "v_cvt_pk_bf16_f32 v121, v122, v123\n"
    "v_mul_f32 v124, v13, v124\n"
    "v_mfma_f32_16x16x32_bf16 v[164:167], v[244:247], v[116:119], v[164:167]\n"
    "ds_read_b64_tr_b16 v[228:229], v5 offset:4096\n"
    "v_mul_f32 v132, v12, v132\n"
    "v_mfma_f32_16x16x32_bf16 v[160:163], v[244:247], v[112:115], v[160:163]\n"
    "s_mov_b32 s64, 0\n"
    "ds_read_b64_tr_b16 v[230:231], v5 offset:6144\n"
    "v_mul_f32 v133, v12, v133\n"
    "v_mfma_f32_16x16x32_bf16 v[156:159], v[244:247], v[108:111], v[156:159]\n"
    "s_waitcnt lgkmcnt(6)\n"
    "v_mul_f32 v134, v12, v134\n"
    "v_mul_f32 v135, v12, v135\n"
    "v_mfma_f32_16x16x32_bf16 v[168:171], v[216:219], v[24:27], 0\n"
    "ds_read_b64_tr_b16 v[232:233], v4 offset:8192\n"
    "v_cvt_pk_bf16_f32 v122, v132, v133\n"
    "v_mfma_f32_16x16x32_bf16 v[172:175], v[216:219], v[28:31], 0\n"
    "ds_read_b64_tr_b16 v[234:235], v4 offset:10240\n"
    "v_cvt_pk_bf16_f32 v123, v134, v135\n"
    "v_mfma_f32_16x16x32_bf16 v[176:179], v[216:219], v[32:35], 0\n"
    "s_waitcnt lgkmcnt(6)\n"
    "buffer_store_dwordx4 v[120:123], v11, s[96:99], s64 offen sc1\n"
    "v_mul_f32 v144, v12, v144\n"
    "v_mfma_f32_16x16x32_bf16 v[188:191], v[220:223], v[32:35], 0\n"
    "ds_read_b64_tr_b16 v[236:237], v5 offset:8192\n"
    "v_mul_f32 v145, v12, v145\n"
    "v_mfma_f32_16x16x32_bf16 v[184:187], v[220:223], v[28:31], 0\n"
    "ds_read_b64_tr_b16 v[238:239], v5 offset:10240\n"
    "v_mul_f32 v146, v12, v146\n"
    "v_mfma_f32_16x16x32_bf16 v[180:183], v[220:223], v[24:27], 0\n"
    "s_waitcnt lgkmcnt(6)\n"
    "v_mul_f32 v147, v12, v147\n"
    "v_cvt_pk_bf16_f32 v144, v144, v145\n"
    "v_mfma_f32_16x16x32_bf16 v[168:171], v[224:227], v[36:39], v[168:171]\n"
    "ds_read_b64_tr_b16 v[240:241], v4 offset:12288\n"
    "v_cvt_pk_bf16_f32 v145, v146, v147\n"
    "v_mfma_f32_16x16x32_bf16 v[172:175], v[224:227], v[40:43], v[172:175]\n"
    "ds_read_b64_tr_b16 v[242:243], v4 offset:14336\n"
    "v_add_u32 v4, 0x4000, v4\n"
    "v_mfma_f32_16x16x32_bf16 v[176:179], v[224:227], v[44:47], v[176:179]\n"
    "s_waitcnt lgkmcnt(6)\n"
    "v_mul_f32 v156, v12, v156\n"
    "v_mul_f32 v157, v12, v157\n"
    "v_mfma_f32_16x16x32_bf16 v[188:191], v[228:231], v[44:47], v[188:191]\n"
    "ds_read_b64_tr_b16 v[244:245], v5 offset:12288\n"
    "v_mul_f32 v158, v12, v158\n"
    "v_mfma_f32_16x16x32_bf16 v[184:187], v[228:231], v[40:43], v[184:187]\n"
    "s_mov_b32 s65, 1024\n"
    "ds_read_b64_tr_b16 v[246:247], v5 offset:14336\n"
    "v_add_u32 v5, 0x4000, v5\n"
    "v_mfma_f32_16x16x32_bf16 v[180:183], v[228:231], v[36:39], v[180:183]\n"
    "s_waitcnt lgkmcnt(6)\n"
    "v_mul_f32 v159, v12, v159\n"
    "v_cvt_pk_bf16_f32 v146, v156, v157\n"
    "v_mfma_f32_16x16x32_bf16 v[168:171], v[232:235], v[48:51], v[168:171]\n"
    "s_barrier\n"
    "ds_read_b64_tr_b16 v[216:217], v4 offset:0\n"
    "v_cvt_pk_bf16_f32 v147, v158, v159\n"
    "v_mfma_f32_16x16x32_bf16 v[172:175], v[232:235], v[52:55], v[172:175]\n"
    "ds_read_b64_tr_b16 v[218:219], v4 offset:2048\n"
    "v_mul_f32 v125, v13, v125\n"
    "v_mfma_f32_16x16x32_bf16 v[176:179], v[232:235], v[56:59], v[176:179]\n"
    "s_waitcnt lgkmcnt(6)\n"
    "buffer_store_dwordx4 v[144:147], v11, s[96:99], s65 offen sc1\n"
    "v_mul_f32 v126, v13, v126\n"
    "v_mfma_f32_16x16x32_bf16 v[188:191], v[236:239], v[56:59], v[188:191]\n"
    "ds_read_b64_tr_b16 v[220:221], v5 offset:0\n"
    "v_mul_f32 v127, v13, v127\n"
    "v_mfma_f32_16x16x32_bf16 v[184:187], v[236:239], v[52:55], v[184:187]\n"
    "ds_read_b64_tr_b16 v[222:223], v5 offset:2048\n"
    "v_cvt_pk_bf16_f32 v124, v124, v125\n"
    "v_mfma_f32_16x16x32_bf16 v[180:183], v[236:239], v[48:51], v[180:183]\n"
    "s_waitcnt lgkmcnt(6)\n"
    "v_cvt_pk_bf16_f32 v125, v126, v127\n"
    "v_mul_f32 v136, v13, v136\n"
    "v_mfma_f32_16x16x32_bf16 v[168:171], v[240:243], v[60:63], v[168:171]\n"
    "ds_read_b64_tr_b16 v[224:225], v4 offset:4096\n"
    "v_mul_f32 v137, v13, v137\n"
    "v_mfma_f32_16x16x32_bf16 v[172:175], v[240:243], v[64:67], v[172:175]\n"
    "ds_read_b64_tr_b16 v[226:227], v4 offset:6144\n"
    "v_mul_f32 v138, v13, v138\n"
    "v_mfma_f32_16x16x32_bf16 v[176:179], v[240:243], v[68:71], v[176:179]\n"
    "s_waitcnt lgkmcnt(6)\n"
    "v_mul_f32 v139, v13, v139\n"
    "v_cvt_pk_bf16_f32 v126, v136, v137\n"
    "v_mfma_f32_16x16x32_bf16 v[188:191], v[244:247], v[68:71], v[188:191]\n"
    "s_mov_b32 s64, 2048\n"
    "ds_read_b64_tr_b16 v[228:229], v5 offset:4096\n"
    "v_cvt_pk_bf16_f32 v127, v138, v139\n"
    "v_mfma_f32_16x16x32_bf16 v[184:187], v[244:247], v[64:67], v[184:187]\n"
    "ds_read_b64_tr_b16 v[230:231], v5 offset:6144\n"
    "v_mul_f32 v148, v13, v148\n"
    "v_mfma_f32_16x16x32_bf16 v[180:183], v[244:247], v[60:63], v[180:183]\n"
    "s_waitcnt lgkmcnt(6)\n"
    "v_mul_f32 v149, v13, v149\n"
    "v_mul_f32 v150, v13, v150\n"
    "v_mfma_f32_16x16x32_bf16 v[168:171], v[216:219], v[72:75], v[168:171]\n"
    "ds_read_b64_tr_b16 v[232:233], v4 offset:8192\n"
    "v_mul_f32 v151, v13, v151\n"
    "v_mfma_f32_16x16x32_bf16 v[172:175], v[216:219], v[76:79], v[172:175]\n"
    "ds_read_b64_tr_b16 v[234:235], v4 offset:10240\n"
    "v_cvt_pk_bf16_f32 v148, v148, v149\n"
    "v_mfma_f32_16x16x32_bf16 v[176:179], v[216:219], v[80:83], v[176:179]\n"
    "s_waitcnt lgkmcnt(6)\n"
    "buffer_store_dwordx4 v[124:127], v11, s[96:99], s64 offen sc1\n"
    "v_cvt_pk_bf16_f32 v149, v150, v151\n"
    "v_mfma_f32_16x16x32_bf16 v[188:191], v[220:223], v[80:83], v[188:191]\n"
    "ds_read_b64_tr_b16 v[236:237], v5 offset:8192\n"
    "v_mul_f32 v160, v13, v160\n"
    "v_mfma_f32_16x16x32_bf16 v[184:187], v[220:223], v[76:79], v[184:187]\n"
    "ds_read_b64_tr_b16 v[238:239], v5 offset:10240\n"
    "v_mul_f32 v161, v13, v161\n"
    "v_mfma_f32_16x16x32_bf16 v[180:183], v[220:223], v[72:75], v[180:183]\n"
    "s_waitcnt lgkmcnt(6)\n"
    "v_mul_f32 v162, v13, v162\n"
    "v_mul_f32 v163, v13, v163\n"
    "v_mfma_f32_16x16x32_bf16 v[168:171], v[224:227], v[84:87], v[168:171]\n"
    "ds_read_b64_tr_b16 v[240:241], v4 offset:12288\n"
    "v_cvt_pk_bf16_f32 v150, v160, v161\n"
    "v_mfma_f32_16x16x32_bf16 v[172:175], v[224:227], v[88:91], v[172:175]\n"
    "ds_read_b64_tr_b16 v[242:243], v4 offset:14336\n"
    "v_add_u32 v4, 0x4000, v4\n"
    "v_mfma_f32_16x16x32_bf16 v[176:179], v[224:227], v[92:95], v[176:179]\n"
    "s_waitcnt lgkmcnt(6)\n"
    "v_cvt_pk_bf16_f32 v151, v162, v163\n"
    "v_mul_f32 v128, v14, v128\n"
    "v_mfma_f32_16x16x32_bf16 v[188:191], v[228:231], v[92:95], v[188:191]\n"
    "s_mov_b32 s65, 3072\n"
    "ds_read_b64_tr_b16 v[244:245], v5 offset:12288\n"
    "v_mul_f32 v129, v14, v129\n"
    "v_mfma_f32_16x16x32_bf16 v[184:187], v[228:231], v[88:91], v[184:187]\n"
    "ds_read_b64_tr_b16 v[246:247], v5 offset:14336\n"
    "v_add_u32 v5, 0x4000, v5\n"
    "v_mfma_f32_16x16x32_bf16 v[180:183], v[228:231], v[84:87], v[180:183]\n"
    "s_waitcnt lgkmcnt(6)\n"
    "v_mul_f32 v130, v14, v130\n"
    "v_mul_f32 v131, v14, v131\n"
    "v_mfma_f32_16x16x32_bf16 v[168:171], v[232:235], v[96:99], v[168:171]\n"
    "ds_read_b64_tr_b16 v[216:217], v6 offset:0\n"
    "v_cvt_pk_bf16_f32 v128, v128, v129\n"
    "v_mfma_f32_16x16x32_bf16 v[172:175], v[232:235], v[100:103], v[172:175]\n"
    "ds_read_b64_tr_b16 v[218:219], v6 offset:2048\n"
    "v_cvt_pk_bf16_f32 v129, v130, v131\n"
    "v_mfma_f32_16x16x32_bf16 v[176:179], v[232:235], v[104:107], v[176:179]\n"
    "s_waitcnt lgkmcnt(6)\n"
    "buffer_store_dwordx4 v[148:151], v11, s[96:99], s65 offen sc1\n"
    "v_mul_f32 v140, v14, v140\n"
    "v_mfma_f32_16x16x32_bf16 v[188:191], v[236:239], v[104:107], v[188:191]\n"
    "ds_read_b64_tr_b16 v[220:221], v7 offset:0\n"
    "v_mul_f32 v141, v14, v141\n"
    "v_mfma_f32_16x16x32_bf16 v[184:187], v[236:239], v[100:103], v[184:187]\n"
    "ds_read_b64_tr_b16 v[222:223], v7 offset:2048\n"
    "v_mul_f32 v142, v14, v142\n"
    "v_mfma_f32_16x16x32_bf16 v[180:183], v[236:239], v[96:99], v[180:183]\n"
    "s_waitcnt lgkmcnt(6)\n"
    "v_mul_f32 v143, v14, v143\n"
    "v_cvt_pk_bf16_f32 v130, v140, v141\n"
    "v_mfma_f32_16x16x32_bf16 v[168:171], v[240:243], v[108:111], v[168:171]\n"
    "ds_read_b64_tr_b16 v[224:225], v6 offset:4096\n"
    "v_cvt_pk_bf16_f32 v131, v142, v143\n"
    "v_mfma_f32_16x16x32_bf16 v[172:175], v[240:243], v[112:115], v[172:175]\n"
    "ds_read_b64_tr_b16 v[226:227], v6 offset:6144\n"
    "v_mul_f32 v152, v14, v152\n"
    "v_mfma_f32_16x16x32_bf16 v[176:179], v[240:243], v[116:119], v[176:179]\n"
    "s_waitcnt lgkmcnt(6)\n"
    "v_mul_f32 v153, v14, v153\n"
    "v_mul_f32 v154, v14, v154\n"
    "v_mfma_f32_16x16x32_bf16 v[188:191], v[244:247], v[116:119], v[188:191]\n"
    "s_mov_b32 s64, 4096\n"
    "ds_read_b64_tr_b16 v[228:229], v7 offset:4096\n"
    "v_mul_f32 v155, v14, v155\n"
    "v_mfma_f32_16x16x32_bf16 v[184:187], v[244:247], v[112:115], v[184:187]\n"
    "ds_read_b64_tr_b16 v[230:231], v7 offset:6144\n"
    "v_cvt_pk_bf16_f32 v152, v152, v153\n"
    "v_mfma_f32_16x16x32_bf16 v[180:183], v[244:247], v[108:111], v[180:183]\n"
    "s_waitcnt lgkmcnt(6)\n"
    "v_cvt_pk_bf16_f32 v153, v154, v155\n"
    "v_mul_f32 v164, v14, v164\n"
    "v_mfma_f32_16x16x32_bf16 v[192:195], v[216:219], v[24:27], 0\n"
    "ds_read_b64_tr_b16 v[232:233], v6 offset:8192\n"
    "v_mul_f32 v165, v14, v165\n"
    "v_mfma_f32_16x16x32_bf16 v[196:199], v[216:219], v[28:31], 0\n"
    "ds_read_b64_tr_b16 v[234:235], v6 offset:10240\n"
    "v_mul_f32 v166, v14, v166\n"
    "v_mfma_f32_16x16x32_bf16 v[200:203], v[216:219], v[32:35], 0\n"
    "s_waitcnt lgkmcnt(6)\n"
    "buffer_store_dwordx4 v[128:131], v11, s[96:99], s64 offen sc1\n"
    "v_mul_f32 v167, v14, v167\n"
    "v_mfma_f32_16x16x32_bf16 v[212:215], v[220:223], v[32:35], 0\n"
    "ds_read_b64_tr_b16 v[236:237], v7 offset:8192\n"
    "v_cvt_pk_bf16_f32 v154, v164, v165\n"
    "v_mfma_f32_16x16x32_bf16 v[208:211], v[220:223], v[28:31], 0\n"
    "ds_read_b64_tr_b16 v[238:239], v7 offset:10240\n"
    "v_cvt_pk_bf16_f32 v155, v166, v167\n"
    "v_mfma_f32_16x16x32_bf16 v[204:207], v[220:223], v[24:27], 0\n"
    "s_waitcnt lgkmcnt(6)\n"
    "v_mul_f32 v168, v12, v168\n"
    "v_mul_f32 v169, v12, v169\n"
    "v_mfma_f32_16x16x32_bf16 v[192:195], v[224:227], v[36:39], v[192:195]\n"
    "ds_read_b64_tr_b16 v[240:241], v6 offset:12288\n"
    "v_mul_f32 v170, v12, v170\n"
    "v_mfma_f32_16x16x32_bf16 v[196:199], v[224:227], v[40:43], v[196:199]\n"
    "ds_read_b64_tr_b16 v[242:243], v6 offset:14336\n"
    "v_add_u32 v6, 0x4000, v6\n"
    "v_mfma_f32_16x16x32_bf16 v[200:203], v[224:227], v[44:47], v[200:203]\n"
    "s_waitcnt lgkmcnt(6)\n"
    "v_mul_f32 v171, v12, v171\n"
    "v_cvt_pk_bf16_f32 v168, v168, v169\n"
    "v_mfma_f32_16x16x32_bf16 v[212:215], v[228:231], v[44:47], v[212:215]\n"
    "s_mov_b32 s65, 5120\n"
    "ds_read_b64_tr_b16 v[244:245], v7 offset:12288\n"
    "v_cvt_pk_bf16_f32 v169, v170, v171\n"
    "v_mfma_f32_16x16x32_bf16 v[208:211], v[228:231], v[40:43], v[208:211]\n"
    "ds_read_b64_tr_b16 v[246:247], v7 offset:14336\n"
    "v_add_u32 v7, 0x4000, v7\n"
    "v_mfma_f32_16x16x32_bf16 v[204:207], v[228:231], v[36:39], v[204:207]\n"
    "s_waitcnt lgkmcnt(6)\n"
    "v_mul_f32 v180, v12, v180\n"
    "v_mul_f32 v181, v12, v181\n"
    "v_mfma_f32_16x16x32_bf16 v[192:195], v[232:235], v[48:51], v[192:195]\n"
    "ds_read_b64_tr_b16 v[216:217], v6 offset:0\n"
    "v_mul_f32 v182, v12, v182\n"
    "v_mfma_f32_16x16x32_bf16 v[196:199], v[232:235], v[52:55], v[196:199]\n"
    "ds_read_b64_tr_b16 v[218:219], v6 offset:2048\n"
    "v_mul_f32 v183, v12, v183\n"
    "v_mfma_f32_16x16x32_bf16 v[200:203], v[232:235], v[56:59], v[200:203]\n"
    "s_waitcnt lgkmcnt(6)\n"
    "buffer_store_dwordx4 v[152:155], v11, s[96:99], s65 offen sc1\n"
    "v_cvt_pk_bf16_f32 v170, v180, v181\n"
    "v_mfma_f32_16x16x32_bf16 v[212:215], v[236:239], v[56:59], v[212:215]\n"
    "ds_read_b64_tr_b16 v[220:221], v7 offset:0\n"
    "v_cvt_pk_bf16_f32 v171, v182, v183\n"
    "v_mfma_f32_16x16x32_bf16 v[208:211], v[236:239], v[52:55], v[208:211]\n"
    "ds_read_b64_tr_b16 v[222:223], v7 offset:2048\n"
    "v_mul_f32 v172, v13, v172\n"
    "v_mfma_f32_16x16x32_bf16 v[204:207], v[236:239], v[48:51], v[204:207]\n"
    "s_waitcnt lgkmcnt(6)\n"
    "v_mul_f32 v173, v13, v173\n"
    "v_mul_f32 v174, v13, v174\n"
    "v_mfma_f32_16x16x32_bf16 v[192:195], v[240:243], v[60:63], v[192:195]\n"
    "ds_read_b64_tr_b16 v[224:225], v6 offset:4096\n"
    "v_mul_f32 v175, v13, v175\n"
    "v_mfma_f32_16x16x32_bf16 v[196:199], v[240:243], v[64:67], v[196:199]\n"
    "ds_read_b64_tr_b16 v[226:227], v6 offset:6144\n"
    "v_cvt_pk_bf16_f32 v172, v172, v173\n"
    "v_mfma_f32_16x16x32_bf16 v[200:203], v[240:243], v[68:71], v[200:203]\n"
    "s_waitcnt lgkmcnt(6)\n"
    "v_cvt_pk_bf16_f32 v173, v174, v175\n"
    "v_mul_f32 v184, v13, v184\n"
    "v_mfma_f32_16x16x32_bf16 v[212:215], v[244:247], v[68:71], v[212:215]\n"
    "s_mov_b32 s64, 18432\n"
    "ds_read_b64_tr_b16 v[228:229], v7 offset:4096\n"
    "v_mul_f32 v185, v13, v185\n"
    "v_mfma_f32_16x16x32_bf16 v[208:211], v[244:247], v[64:67], v[208:211]\n"
    "ds_read_b64_tr_b16 v[230:231], v7 offset:6144\n"
    "v_mul_f32 v186, v13, v186\n"
    "v_mfma_f32_16x16x32_bf16 v[204:207], v[244:247], v[60:63], v[204:207]\n"
    "s_waitcnt lgkmcnt(6)\n"
    "v_mul_f32 v187, v13, v187\n"
    "v_cvt_pk_bf16_f32 v174, v184, v185\n"
    "v_mfma_f32_16x16x32_bf16 v[192:195], v[216:219], v[72:75], v[192:195]\n"
    "ds_read_b64_tr_b16 v[232:233], v6 offset:8192\n"
    "v_cvt_pk_bf16_f32 v175, v186, v187\n"
    "v_mfma_f32_16x16x32_bf16 v[196:199], v[216:219], v[76:79], v[196:199]\n"
    "ds_read_b64_tr_b16 v[234:235], v6 offset:10240\n"
    "v_mul_f32 v176, v14, v176\n"
    "v_mfma_f32_16x16x32_bf16 v[200:203], v[216:219], v[80:83], v[200:203]\n"
    "s_waitcnt lgkmcnt(6)\n"
    "buffer_store_dwordx4 v[168:171], v11, s[96:99], s64 offen sc1\n"
    "v_mul_f32 v177, v14, v177\n"
    "v_mfma_f32_16x16x32_bf16 v[212:215], v[220:223], v[80:83], v[212:215]\n"
    "ds_read_b64_tr_b16 v[236:237], v7 offset:8192\n"
    "v_mul_f32 v178, v14, v178\n"
    "v_mfma_f32_16x16x32_bf16 v[208:211], v[220:223], v[76:79], v[208:211]\n"
    "ds_read_b64_tr_b16 v[238:239], v7 offset:10240\n"
    "v_mul_f32 v179, v14, v179\n"
    "v_mfma_f32_16x16x32_bf16 v[204:207], v[220:223], v[72:75], v[204:207]\n"
    "s_waitcnt lgkmcnt(6)\n"
    "v_cvt_pk_bf16_f32 v176, v176, v177\n"
    "v_cvt_pk_bf16_f32 v177, v178, v179\n"
    "v_mfma_f32_16x16x32_bf16 v[192:195], v[224:227], v[84:87], v[192:195]\n"
    "ds_read_b64_tr_b16 v[240:241], v6 offset:12288\n"
    "v_mul_f32 v188, v14, v188\n"
    "v_mfma_f32_16x16x32_bf16 v[196:199], v[224:227], v[88:91], v[196:199]\n"
    "ds_read_b64_tr_b16 v[242:243], v6 offset:14336\n"
    "v_add_u32 v6, 0x4000, v6\n"
    "v_mfma_f32_16x16x32_bf16 v[200:203], v[224:227], v[92:95], v[200:203]\n"
    "s_waitcnt lgkmcnt(6)\n"
    "v_mul_f32 v189, v14, v189\n"
    "v_mul_f32 v190, v14, v190\n"
    "v_mfma_f32_16x16x32_bf16 v[212:215], v[228:231], v[92:95], v[212:215]\n"
    "s_mov_b32 s65, 20480\n"
    "ds_read_b64_tr_b16 v[244:245], v7 offset:12288\n"
    "v_mul_f32 v191, v14, v191\n"
    "v_mfma_f32_16x16x32_bf16 v[208:211], v[228:231], v[88:91], v[208:211]\n"
    "ds_read_b64_tr_b16 v[246:247], v7 offset:14336\n"
    "v_add_u32 v7, 0x4000, v7\n"
    "v_mfma_f32_16x16x32_bf16 v[204:207], v[228:231], v[84:87], v[204:207]\n"
    "s_waitcnt lgkmcnt(6)\n"
    "v_cvt_pk_bf16_f32 v178, v188, v189\n"
    "v_cvt_pk_bf16_f32 v179, v190, v191\n"
    "v_mfma_f32_16x16x32_bf16 v[192:195], v[232:235], v[96:99], v[192:195]\n"
    "s_barrier\n"
    "ds_read_b64_tr_b16 v[216:217], v4 offset:0\n"
    "v_mfma_f32_16x16x32_bf16 v[196:199], v[232:235], v[100:103], v[196:199]\n"
    "ds_read_b64_tr_b16 v[218:219], v4 offset:2048\n"
    "v_mfma_f32_16x16x32_bf16 v[200:203], v[232:235], v[104:107], v[200:203]\n"
    "s_waitcnt lgkmcnt(6)\n"
    "buffer_store_dwordx4 v[172:175], v11, s[96:99], s65 offen sc1\n"
    "v_mfma_f32_16x16x32_bf16 v[212:215], v[236:239], v[104:107], v[212:215]\n"
    "ds_read_b64_tr_b16 v[220:221], v5 offset:0\n"
    "v_mfma_f32_16x16x32_bf16 v[208:211], v[236:239], v[100:103], v[208:211]\n"
    "ds_read_b64_tr_b16 v[222:223], v5 offset:2048\n"
    "v_mfma_f32_16x16x32_bf16 v[204:207], v[236:239], v[96:99], v[204:207]\n"
    "s_waitcnt lgkmcnt(6)\n"
    "v_mfma_f32_16x16x32_bf16 v[192:195], v[240:243], v[108:111], v[192:195]\n"
    "ds_read_b64_tr_b16 v[224:225], v4 offset:4096\n"
    "v_mfma_f32_16x16x32_bf16 v[196:199], v[240:243], v[112:115], v[196:199]\n"
    "ds_read_b64_tr_b16 v[226:227], v4 offset:6144\n"
    "v_mfma_f32_16x16x32_bf16 v[200:203], v[240:243], v[116:119], v[200:203]\n"
    "s_waitcnt lgkmcnt(6)\n"
    "v_mfma_f32_16x16x32_bf16 v[212:215], v[244:247], v[116:119], v[212:215]\n"
    "s_mov_b32 s64, 22528\n"
    "ds_read_b64_tr_b16 v[228:229], v5 offset:4096\n"
    "v_mfma_f32_16x16x32_bf16 v[208:211], v[244:247], v[112:115], v[208:211]\n"
    "ds_read_b64_tr_b16 v[230:231], v5 offset:6144\n"
    "v_mul_f32 v192, v12, v192\n"
    "v_mfma_f32_16x16x32_bf16 v[204:207], v[244:247], v[108:111], v[204:207]\n"
    "s_waitcnt lgkmcnt(6)\n"
    "v_mul_f32 v193, v12, v193\n"
    "v_mul_f32 v194, v12, v194\n"
    "v_mfma_f32_16x16x32_bf16 v[120:123], v[216:219], v[24:27], 0\n"
    "ds_read_b64_tr_b16 v[232:233], v4 offset:8192\n"
    "v_mul_f32 v195, v12, v195\n"
    "v_mfma_f32_16x16x32_bf16 v[124:127], v[216:219], v[28:31], 0\n"
    "ds_read_b64_tr_b16 v[234:235], v4 offset:10240\n"
    "v_cvt_pk_bf16_f32 v192, v192, v193\n"
    "v_mfma_f32_16x16x32_bf16 v[128:131], v[216:219], v[32:35], 0\n"
    "s_waitcnt lgkmcnt(6)\n"
    "buffer_store_dwordx4 v[176:179], v11, s[96:99], s64 offen sc1\n"
    "v_cvt_pk_bf16_f32 v193, v194, v195\n"
    "v_mfma_f32_16x16x32_bf16 v[140:143], v[220:223], v[32:35], 0\n"
    "ds_read_b64_tr_b16 v[236:237], v5 offset:8192\n"
    "v_mul_f32 v204, v12, v204\n"
    "v_mfma_f32_16x16x32_bf16 v[136:139], v[220:223], v[28:31], 0\n"
    "ds_read_b64_tr_b16 v[238:239], v5 offset:10240\n"
    "v_mul_f32 v205, v12, v205\n"
    "v_mfma_f32_16x16x32_bf16 v[132:135], v[220:223], v[24:27], 0\n"
    "s_waitcnt lgkmcnt(6)\n"
    "v_mul_f32 v206, v12, v206\n"
    "v_mul_f32 v207, v12, v207\n"
    "v_mfma_f32_16x16x32_bf16 v[120:123], v[224:227], v[36:39], v[120:123]\n"
    "ds_read_b64_tr_b16 v[240:241], v4 offset:12288\n"
    "v_cvt_pk_bf16_f32 v194, v204, v205\n"
    "v_mfma_f32_16x16x32_bf16 v[124:127], v[224:227], v[40:43], v[124:127]\n"
    "ds_read_b64_tr_b16 v[242:243], v4 offset:14336\n"
    "v_add_u32 v4, 0x4000, v4\n"
    "v_mfma_f32_16x16x32_bf16 v[128:131], v[224:227], v[44:47], v[128:131]\n"
    "s_waitcnt lgkmcnt(6)\n"
    "v_cvt_pk_bf16_f32 v195, v206, v207\n"
    "v_mul_f32 v196, v13, v196\n"
    "v_mfma_f32_16x16x32_bf16 v[140:143], v[228:231], v[44:47], v[140:143]\n"
    "s_mov_b32 s65, 19456\n"
    "ds_read_b64_tr_b16 v[244:245], v5 offset:12288\n"
    "v_mul_f32 v197, v13, v197\n"
    "v_mfma_f32_16x16x32_bf16 v[136:139], v[228:231], v[40:43], v[136:139]\n"
    "ds_read_b64_tr_b16 v[246:247], v5 offset:14336\n"
    "v_add_u32 v5, 0x4000, v5\n"
    "v_mfma_f32_16x16x32_bf16 v[132:135], v[228:231], v[36:39], v[132:135]\n"
    "s_waitcnt lgkmcnt(6)\n"
    "v_mul_f32 v198, v13, v198\n"
    "v_mul_f32 v199, v13, v199\n"
    "v_mfma_f32_16x16x32_bf16 v[120:123], v[232:235], v[48:51], v[120:123]\n"
    "s_barrier\n"
    "ds_read_b64_tr_b16 v[216:217], v4 offset:0\n"
    "v_cvt_pk_bf16_f32 v196, v196, v197\n"
    "v_mfma_f32_16x16x32_bf16 v[124:127], v[232:235], v[52:55], v[124:127]\n"
    "ds_read_b64_tr_b16 v[218:219], v4 offset:2048\n"
    "v_cvt_pk_bf16_f32 v197, v198, v199\n"
    "v_mfma_f32_16x16x32_bf16 v[128:131], v[232:235], v[56:59], v[128:131]\n"
    "s_waitcnt lgkmcnt(6)\n"
    "buffer_store_dwordx4 v[192:195], v11, s[96:99], s65 offen sc1\n"
    "v_mul_f32 v208, v13, v208\n"
    "v_mfma_f32_16x16x32_bf16 v[140:143], v[236:239], v[56:59], v[140:143]\n"
    "ds_read_b64_tr_b16 v[220:221], v5 offset:0\n"
    "v_mul_f32 v209, v13, v209\n"
    "v_mfma_f32_16x16x32_bf16 v[136:139], v[236:239], v[52:55], v[136:139]\n"
    "ds_read_b64_tr_b16 v[222:223], v5 offset:2048\n"
    "v_mul_f32 v210, v13, v210\n"
    "v_mfma_f32_16x16x32_bf16 v[132:135], v[236:239], v[48:51], v[132:135]\n"
    "s_waitcnt lgkmcnt(6)\n"
    "v_mul_f32 v211, v13, v211\n"
    "v_cvt_pk_bf16_f32 v198, v208, v209\n"
    "v_mfma_f32_16x16x32_bf16 v[120:123], v[240:243], v[60:63], v[120:123]\n"
    "ds_read_b64_tr_b16 v[224:225], v4 offset:4096\n"
    "v_cvt_pk_bf16_f32 v199, v210, v211\n"
    "v_mfma_f32_16x16x32_bf16 v[124:127], v[240:243], v[64:67], v[124:127]\n"
    "ds_read_b64_tr_b16 v[226:227], v4 offset:6144\n"
    "v_mul_f32 v200, v14, v200\n"
    "v_mfma_f32_16x16x32_bf16 v[128:131], v[240:243], v[68:71], v[128:131]\n"
    "s_waitcnt lgkmcnt(6)\n"
    "v_mul_f32 v201, v14, v201\n"
    "v_mul_f32 v202, v14, v202\n"
    "v_mfma_f32_16x16x32_bf16 v[140:143], v[244:247], v[68:71], v[140:143]\n"
    "s_mov_b32 s64, 21504\n"
    "ds_read_b64_tr_b16 v[228:229], v5 offset:4096\n"
    "v_mul_f32 v203, v14, v203\n"
    "v_mfma_f32_16x16x32_bf16 v[136:139], v[244:247], v[64:67], v[136:139]\n"
    "ds_read_b64_tr_b16 v[230:231], v5 offset:6144\n"
    "v_cvt_pk_bf16_f32 v200, v200, v201\n"
    "v_mfma_f32_16x16x32_bf16 v[132:135], v[244:247], v[60:63], v[132:135]\n"
    "s_waitcnt lgkmcnt(6)\n"
    "v_cvt_pk_bf16_f32 v201, v202, v203\n"
    "v_mul_f32 v212, v14, v212\n"
    "v_mfma_f32_16x16x32_bf16 v[120:123], v[216:219], v[72:75], v[120:123]\n"
    "ds_read_b64_tr_b16 v[232:233], v4 offset:8192\n"
    "v_mul_f32 v213, v14, v213\n"
    "v_mfma_f32_16x16x32_bf16 v[124:127], v[216:219], v[76:79], v[124:127]\n"
    "ds_read_b64_tr_b16 v[234:235], v4 offset:10240\n"
    "v_mul_f32 v214, v14, v214\n"
    "v_mfma_f32_16x16x32_bf16 v[128:131], v[216:219], v[80:83], v[128:131]\n"
    "s_waitcnt lgkmcnt(6)\n"
    "buffer_store_dwordx4 v[196:199], v11, s[96:99], s64 offen sc1\n"
    "v_mul_f32 v215, v14, v215\n"
    "v_mfma_f32_16x16x32_bf16 v[140:143], v[220:223], v[80:83], v[140:143]\n"
    "ds_read_b64_tr_b16 v[236:237], v5 offset:8192\n"
    "v_cvt_pk_bf16_f32 v202, v212, v213\n"
    "v_mfma_f32_16x16x32_bf16 v[136:139], v[220:223], v[76:79], v[136:139]\n"
    "ds_read_b64_tr_b16 v[238:239], v5 offset:10240\n"
    "v_cvt_pk_bf16_f32 v203, v214, v215\n"
    "v_mfma_f32_16x16x32_bf16 v[132:135], v[220:223], v[72:75], v[132:135]\n"
    "s_waitcnt lgkmcnt(6)\n"
    "v_mfma_f32_16x16x32_bf16 v[120:123], v[224:227], v[84:87], v[120:123]\n"
    "ds_read_b64_tr_b16 v[240:241], v4 offset:12288\n"
    "v_mfma_f32_16x16x32_bf16 v[124:127], v[224:227], v[88:91], v[124:127]\n"
    "ds_read_b64_tr_b16 v[242:243], v4 offset:14336\n"
    "v_add_u32 v4, 0x4000, v4\n"
    "v_mfma_f32_16x16x32_bf16 v[128:131], v[224:227], v[92:95], v[128:131]\n"
    "s_waitcnt lgkmcnt(6)\n"
    "v_mfma_f32_16x16x32_bf16 v[140:143], v[228:231], v[92:95], v[140:143]\n"
    "s_mov_b32 s65, 23552\n"
    "ds_read_b64_tr_b16 v[244:245], v5 offset:12288\n"
    "v_mfma_f32_16x16x32_bf16 v[136:139], v[228:231], v[88:91], v[136:139]\n"
    "ds_read_b64_tr_b16 v[246:247], v5 offset:14336\n"
    "v_add_u32 v5, 0x4000, v5\n"
    "v_mfma_f32_16x16x32_bf16 v[132:135], v[228:231], v[84:87], v[132:135]\n"
    "s_waitcnt lgkmcnt(6)\n"
    "v_add_u32 v254, 1, v254\n"
    "v_mfma_f32_16x16x32_bf16 v[120:123], v[232:235], v[96:99], v[120:123]\n"
    "ds_read_b64_tr_b16 v[216:217], v6 offset:0\n"
    "v_mfma_f32_16x16x32_bf16 v[124:127], v[232:235], v[100:103], v[124:127]\n"
    "ds_read_b64_tr_b16 v[218:219], v6 offset:2048\n"
    "v_mfma_f32_16x16x32_bf16 v[128:131], v[232:235], v[104:107], v[128:131]\n"
    "s_waitcnt lgkmcnt(6)\n"
    "buffer_store_dwordx4 v[200:203], v11, s[96:99], s65 offen sc1\n"
    "v_mfma_f32_16x16x32_bf16 v[140:143], v[236:239], v[104:107], v[140:143]\n"
    "ds_read_b64_tr_b16 v[220:221], v7 offset:0\n"
    "v_mfma_f32_16x16x32_bf16 v[136:139], v[236:239], v[100:103], v[136:139]\n"
    "ds_read_b64_tr_b16 v[222:223], v7 offset:2048\n"
    "v_mfma_f32_16x16x32_bf16 v[132:135], v[236:239], v[96:99], v[132:135]\n"
    "s_waitcnt lgkmcnt(6)\n"
    "s_waitcnt vmcnt(6)\n"
    "buffer_store_dwordx2 v[254:255], v10, s[28:31], 0 offen sc1\n"
    "v_mfma_f32_16x16x32_bf16 v[120:123], v[240:243], v[108:111], v[120:123]\n"
    "ds_read_b64_tr_b16 v[224:225], v6 offset:4096\n"
    "v_mfma_f32_16x16x32_bf16 v[124:127], v[240:243], v[112:115], v[124:127]\n"
    "ds_read_b64_tr_b16 v[226:227], v6 offset:6144\n"
    "v_add_u32 v254, 1, v254\n"
    "v_mfma_f32_16x16x32_bf16 v[128:131], v[240:243], v[116:119], v[128:131]\n"
    "s_waitcnt lgkmcnt(6)\n"
    "v_mfma_f32_16x16x32_bf16 v[140:143], v[244:247], v[116:119], v[140:143]\n"
    "ds_read_b64_tr_b16 v[228:229], v7 offset:4096\n"
    "v_mfma_f32_16x16x32_bf16 v[136:139], v[244:247], v[112:115], v[136:139]\n"
    "ds_read_b64_tr_b16 v[230:231], v7 offset:6144\n"
    "v_mul_f32 v120, v12, v120\n"
    "v_mfma_f32_16x16x32_bf16 v[132:135], v[244:247], v[108:111], v[132:135]\n"
    "s_waitcnt lgkmcnt(6)\n"
    "v_mul_f32 v121, v12, v121\n"
    "v_mul_f32 v122, v12, v122\n"
    "v_mfma_f32_16x16x32_bf16 v[144:147], v[216:219], v[24:27], 0\n"
    "ds_read_b64_tr_b16 v[232:233], v6 offset:8192\n"
    "v_mul_f32 v123, v12, v123\n"
    "v_mfma_f32_16x16x32_bf16 v[148:151], v[216:219], v[28:31], 0\n"
    "ds_read_b64_tr_b16 v[234:235], v6 offset:10240\n"
    "v_cvt_pk_bf16_f32 v120, v120, v121\n"
    "v_mfma_f32_16x16x32_bf16 v[152:155], v[216:219], v[32:35], 0\n"
    "s_waitcnt lgkmcnt(6)\n"
    "v_cvt_pk_bf16_f32 v121, v122, v123\n"
    "v_mul_f32 v124, v13, v124\n"
    "v_mfma_f32_16x16x32_bf16 v[164:167], v[220:223], v[32:35], 0\n"
    "ds_read_b64_tr_b16 v[236:237], v7 offset:8192\n"
    "v_mul_f32 v132, v12, v132\n"
    "v_mfma_f32_16x16x32_bf16 v[160:163], v[220:223], v[28:31], 0\n"
    "ds_read_b64_tr_b16 v[238:239], v7 offset:10240\n"
    "v_mul_f32 v133, v12, v133\n"
    "v_mfma_f32_16x16x32_bf16 v[156:159], v[220:223], v[24:27], 0\n"
    "s_waitcnt lgkmcnt(6)\n"
    "v_mul_f32 v134, v12, v134\n"
    "v_mul_f32 v135, v12, v135\n"
    "v_mfma_f32_16x16x32_bf16 v[144:147], v[224:227], v[36:39], v[144:147]\n"
    "s_mov_b32 s64, 36864\n"
    "ds_read_b64_tr_b16 v[240:241], v6 offset:12288\n"
    "v_cvt_pk_bf16_f32 v122, v132, v133\n"
    "v_mfma_f32_16x16x32_bf16 v[148:151], v[224:227], v[40:43], v[148:151]\n"
    "ds_read_b64_tr_b16 v[242:243], v6 offset:14336\n"
    "v_add_u32 v6, 0x4000, v6\n"
    "v_mfma_f32_16x16x32_bf16 v[152:155], v[224:227], v[44:47], v[152:155]\n"
    "s_waitcnt lgkmcnt(6)\n"
    "v_cvt_pk_bf16_f32 v123, v134, v135\n"
    "v_mul_f32 v125, v13, v125\n"
    "v_mfma_f32_16x16x32_bf16 v[164:167], v[228:231], v[44:47], v[164:167]\n"
    "ds_read_b64_tr_b16 v[244:245], v7 offset:12288\n"
    "v_mul_f32 v126, v13, v126\n"
    "v_mfma_f32_16x16x32_bf16 v[160:163], v[228:231], v[40:43], v[160:163]\n"
    "ds_read_b64_tr_b16 v[246:247], v7 offset:14336\n"
    "v_add_u32 v7, 0x4000, v7\n"
    "v_mfma_f32_16x16x32_bf16 v[156:159], v[228:231], v[36:39], v[156:159]\n"
    "s_waitcnt lgkmcnt(6)\n"
    "buffer_store_dwordx4 v[120:123], v11, s[96:99], s64 offen sc1\n"
    "v_mul_f32 v127, v13, v127\n"
    "v_mfma_f32_16x16x32_bf16 v[144:147], v[232:235], v[48:51], v[144:147]\n"
    "ds_read_b64_tr_b16 v[216:217], v6 offset:0\n"
    "v_cvt_pk_bf16_f32 v124, v124, v125\n"
    "v_mfma_f32_16x16x32_bf16 v[148:151], v[232:235], v[52:55], v[148:151]\n"
    "ds_read_b64_tr_b16 v[218:219], v6 offset:2048\n"
    "v_cvt_pk_bf16_f32 v125, v126, v127\n"
    "v_mfma_f32_16x16x32_bf16 v[152:155], v[232:235], v[56:59], v[152:155]\n"
    "s_waitcnt lgkmcnt(6)\n"
    "v_mul_f32 v136, v13, v136\n"
    "v_mul_f32 v137, v13, v137\n"
    "v_mfma_f32_16x16x32_bf16 v[164:167], v[236:239], v[56:59], v[164:167]\n"
    "ds_read_b64_tr_b16 v[220:221], v7 offset:0\n"
    "v_mul_f32 v138, v13, v138\n"
    "v_mfma_f32_16x16x32_bf16 v[160:163], v[236:239], v[52:55], v[160:163]\n"
    "ds_read_b64_tr_b16 v[222:223], v7 offset:2048\n"
    "v_mul_f32 v139, v13, v139\n"
    "v_mfma_f32_16x16x32_bf16 v[156:159], v[236:239], v[48:51], v[156:159]\n"
    "s_waitcnt lgkmcnt(6)\n"
    "v_cvt_pk_bf16_f32 v126, v136, v137\n"
    "v_cvt_pk_bf16_f32 v127, v138, v139\n"
    "v_mfma_f32_16x16x32_bf16 v[144:147], v[240:243], v[60:63], v[144:147]\n"
    "s_mov_b32 s65, 38912\n"
    "ds_read_b64_tr_b16 v[224:225], v6 offset:4096\n"
    "v_mul_f32 v128, v14, v128\n"
    "v_mfma_f32_16x16x32_bf16 v[148:151], v[240:243], v[64:67], v[148:151]\n"
    "ds_read_b64_tr_b16 v[226:227], v6 offset:6144\n"
    "v_mul_f32 v129, v14, v129\n"
    "v_mfma_f32_16x16x32_bf16 v[152:155], v[240:243], v[68:71], v[152:155]\n"
    "s_waitcnt lgkmcnt(6)\n"
    "v_mul_f32 v130, v14, v130\n"
    "v_mul_f32 v131, v14, v131\n"
    "v_mfma_f32_16x16x32_bf16 v[164:167], v[244:247], v[68:71], v[164:167]\n"
    "ds_read_b64_tr_b16 v[228:229], v7 offset:4096\n"
    "v_cvt_pk_bf16_f32 v128, v128, v129\n"
    "v_mfma_f32_16x16x32_bf16 v[160:163], v[244:247], v[64:67], v[160:163]\n"
    "ds_read_b64_tr_b16 v[230:231], v7 offset:6144\n"
    "v_cvt_pk_bf16_f32 v129, v130, v131\n"
    "v_mfma_f32_16x16x32_bf16 v[156:159], v[244:247], v[60:63], v[156:159]\n"
    "s_waitcnt lgkmcnt(6)\n"
    "buffer_store_dwordx4 v[124:127], v11, s[96:99], s65 offen sc1\n"
    "v_mul_f32 v140, v14, v140\n"
    "v_mfma_f32_16x16x32_bf16 v[144:147], v[216:219], v[72:75], v[144:147]\n"
    "ds_read_b64_tr_b16 v[232:233], v6 offset:8192\n"
    "v_mul_f32 v141, v14, v141\n"
    "v_mfma_f32_16x16x32_bf16 v[148:151], v[216:219], v[76:79], v[148:151]\n"
    "ds_read_b64_tr_b16 v[234:235], v6 offset:10240\n"
    "v_mul_f32 v142, v14, v142\n"
    "v_mfma_f32_16x16x32_bf16 v[152:155], v[216:219], v[80:83], v[152:155]\n"
    "s_waitcnt lgkmcnt(6)\n"
    "v_mul_f32 v143, v14, v143\n"
    "v_cvt_pk_bf16_f32 v130, v140, v141\n"
    "v_mfma_f32_16x16x32_bf16 v[164:167], v[220:223], v[80:83], v[164:167]\n"
    "ds_read_b64_tr_b16 v[236:237], v7 offset:8192\n"
    "v_cvt_pk_bf16_f32 v131, v142, v143\n"
    "v_mfma_f32_16x16x32_bf16 v[160:163], v[220:223], v[76:79], v[160:163]\n"
    "ds_read_b64_tr_b16 v[238:239], v7 offset:10240\n"
    "v_mfma_f32_16x16x32_bf16 v[156:159], v[220:223], v[72:75], v[156:159]\n"
    "s_waitcnt lgkmcnt(6)\n"
    "v_mfma_f32_16x16x32_bf16 v[144:147], v[224:227], v[84:87], v[144:147]\n"
    "s_mov_b32 s64, 40960\n"
    "ds_read_b64_tr_b16 v[240:241], v6 offset:12288\n"
    "v_mfma_f32_16x16x32_bf16 v[148:151], v[224:227], v[88:91], v[148:151]\n"
    "ds_read_b64_tr_b16 v[242:243], v6 offset:14336\n"
    "v_add_u32 v6, 0x4000, v6\n"
    "v_mfma_f32_16x16x32_bf16 v[152:155], v[224:227], v[92:95], v[152:155]\n"
    "s_waitcnt lgkmcnt(6)\n"
    "v_mfma_f32_16x16x32_bf16 v[164:167], v[228:231], v[92:95], v[164:167]\n"
    "ds_read_b64_tr_b16 v[244:245], v7 offset:12288\n"
    "v_mfma_f32_16x16x32_bf16 v[160:163], v[228:231], v[88:91], v[160:163]\n"
    "ds_read_b64_tr_b16 v[246:247], v7 offset:14336\n"
    "v_add_u32 v7, 0x4000, v7\n"
    "v_mfma_f32_16x16x32_bf16 v[156:159], v[228:231], v[84:87], v[156:159]\n"
    "s_waitcnt lgkmcnt(6)\n"
    "buffer_store_dwordx4 v[128:131], v11, s[96:99], s64 offen sc1\n"
    "v_mfma_f32_16x16x32_bf16 v[144:147], v[232:235], v[96:99], v[144:147]\n"
    "s_barrier\n"
    "ds_read_b64_tr_b16 v[216:217], v4 offset:0\n"
    "v_mfma_f32_16x16x32_bf16 v[148:151], v[232:235], v[100:103], v[148:151]\n"
    "ds_read_b64_tr_b16 v[218:219], v4 offset:2048\n"
    "v_mfma_f32_16x16x32_bf16 v[152:155], v[232:235], v[104:107], v[152:155]\n"
    "s_waitcnt lgkmcnt(6)\n"
    "v_mfma_f32_16x16x32_bf16 v[164:167], v[236:239], v[104:107], v[164:167]\n"
    "ds_read_b64_tr_b16 v[220:221], v5 offset:0\n"
    "v_mfma_f32_16x16x32_bf16 v[160:163], v[236:239], v[100:103], v[160:163]\n"
    "ds_read_b64_tr_b16 v[222:223], v5 offset:2048\n"
    "v_mfma_f32_16x16x32_bf16 v[156:159], v[236:239], v[96:99], v[156:159]\n"
    "s_waitcnt lgkmcnt(6)\n"
    "v_mfma_f32_16x16x32_bf16 v[144:147], v[240:243], v[108:111], v[144:147]\n"
    "ds_read_b64_tr_b16 v[224:225], v4 offset:4096\n"
    "v_mfma_f32_16x16x32_bf16 v[148:151], v[240:243], v[112:115], v[148:151]\n"
    "ds_read_b64_tr_b16 v[226:227], v4 offset:6144\n"
    "v_mfma_f32_16x16x32_bf16 v[152:155], v[240:243], v[116:119], v[152:155]\n"
    "s_waitcnt lgkmcnt(6)\n"
    "v_mfma_f32_16x16x32_bf16 v[164:167], v[244:247], v[116:119], v[164:167]\n"
    "ds_read_b64_tr_b16 v[228:229], v5 offset:4096\n"
    "v_mfma_f32_16x16x32_bf16 v[160:163], v[244:247], v[112:115], v[160:163]\n"
    "ds_read_b64_tr_b16 v[230:231], v5 offset:6144\n"
    "v_mul_f32 v144, v12, v144\n"
    "v_mfma_f32_16x16x32_bf16 v[156:159], v[244:247], v[108:111], v[156:159]\n"
    "s_waitcnt lgkmcnt(6)\n"
    "v_mul_f32 v145, v12, v145\n"
    "v_mul_f32 v146, v12, v146\n"
    "v_mfma_f32_16x16x32_bf16 v[168:171], v[216:219], v[24:27], 0\n"
    "ds_read_b64_tr_b16 v[232:233], v4 offset:8192\n"
    "v_mul_f32 v147, v12, v147\n"
    "v_mfma_f32_16x16x32_bf16 v[172:175], v[216:219], v[28:31], 0\n"
    "ds_read_b64_tr_b16 v[234:235], v4 offset:10240\n"
    "v_cvt_pk_bf16_f32 v144, v144, v145\n"
    "v_mfma_f32_16x16x32_bf16 v[176:179], v[216:219], v[32:35], 0\n"
    "s_waitcnt lgkmcnt(6)\n"
    "v_cvt_pk_bf16_f32 v145, v146, v147\n"
    "v_mul_f32 v148, v13, v148\n"
    "v_mfma_f32_16x16x32_bf16 v[188:191], v[220:223], v[32:35], 0\n"
    "ds_read_b64_tr_b16 v[236:237], v5 offset:8192\n"
    "v_mul_f32 v156, v12, v156\n"
    "v_mfma_f32_16x16x32_bf16 v[184:187], v[220:223], v[28:31], 0\n"
    "ds_read_b64_tr_b16 v[238:239], v5 offset:10240\n"
    "v_mul_f32 v157, v12, v157\n"
    "v_mfma_f32_16x16x32_bf16 v[180:183], v[220:223], v[24:27], 0\n"
    "s_waitcnt lgkmcnt(6)\n"
    "v_mul_f32 v158, v12, v158\n"
    "v_mul_f32 v159, v12, v159\n"
    "v_mfma_f32_16x16x32_bf16 v[168:171], v[224:227], v[36:39], v[168:171]\n"
    "s_mov_b32 s65, 37888\n"
    "ds_read_b64_tr_b16 v[240:241], v4 offset:12288\n"
    "v_cvt_pk_bf16_f32 v146, v156, v157\n"
    "v_mfma_f32_16x16x32_bf16 v[172:175], v[224:227], v[40:43], v[172:175]\n"
    "ds_read_b64_tr_b16 v[242:243], v4 offset:14336\n"
    "v_add_u32 v4, 0x4000, v4\n"
    "v_mfma_f32_16x16x32_bf16 v[176:179], v[224:227], v[44:47], v[176:179]\n"
    "s_waitcnt lgkmcnt(6)\n"
    "v_cvt_pk_bf16_f32 v147, v158, v159\n"
    "v_mul_f32 v149, v13, v149\n"
    "v_mfma_f32_16x16x32_bf16 v[188:191], v[228:231], v[44:47], v[188:191]\n"
    "ds_read_b64_tr_b16 v[244:245], v5 offset:12288\n"
    "v_mul_f32 v150, v13, v150\n"
    "v_mfma_f32_16x16x32_bf16 v[184:187], v[228:231], v[40:43], v[184:187]\n"
    "ds_read_b64_tr_b16 v[246:247], v5 offset:14336\n"
    "v_add_u32 v5, 0x4000, v5\n"
    "v_mfma_f32_16x16x32_bf16 v[180:183], v[228:231], v[36:39], v[180:183]\n"
    "s_waitcnt lgkmcnt(6)\n"
    "buffer_store_dwordx4 v[144:147], v11, s[96:99], s65 offen sc1\n"
    "v_mul_f32 v151, v13, v151\n"
    "v_mfma_f32_16x16x32_bf16 v[168:171], v[232:235], v[48:51], v[168:171]\n"
    "s_barrier\n"
    "ds_read_b64_tr_b16 v[216:217], v4 offset:0\n"
    "v_cvt_pk_bf16_f32 v148, v148, v149\n"
    "v_mfma_f32_16x16x32_bf16 v[172:175], v[232:235], v[52:55], v[172:175]\n"
    "ds_read_b64_tr_b16 v[218:219], v4 offset:2048\n"
    "v_cvt_pk_bf16_f32 v149, v150, v151\n"
    "v_mfma_f32_16x16x32_bf16 v[176:179], v[232:235], v[56:59], v[176:179]\n"
    "s_waitcnt lgkmcnt(6)\n"
    "v_mul_f32 v160, v13, v160\n"
    "v_mul_f32 v161, v13, v161\n"
    "v_mfma_f32_16x16x32_bf16 v[188:191], v[236:239], v[56:59], v[188:191]\n"
    "ds_read_b64_tr_b16 v[220:221], v5 offset:0\n"
    "v_mul_f32 v162, v13, v162\n"
    "v_mfma_f32_16x16x32_bf16 v[184:187], v[236:239], v[52:55], v[184:187]\n"
    "ds_read_b64_tr_b16 v[222:223], v5 offset:2048\n"
    "v_mul_f32 v163, v13, v163\n"
    "v_mfma_f32_16x16x32_bf16 v[180:183], v[236:239], v[48:51], v[180:183]\n"
    "s_waitcnt lgkmcnt(6)\n"
    "v_cvt_pk_bf16_f32 v150, v160, v161\n"
    "v_cvt_pk_bf16_f32 v151, v162, v163\n"
    "v_mfma_f32_16x16x32_bf16 v[168:171], v[240:243], v[60:63], v[168:171]\n"
    "s_mov_b32 s64, 39936\n"
    "ds_read_b64_tr_b16 v[224:225], v4 offset:4096\n"
    "v_mul_f32 v152, v14, v152\n"
    "v_mfma_f32_16x16x32_bf16 v[172:175], v[240:243], v[64:67], v[172:175]\n"
    "ds_read_b64_tr_b16 v[226:227], v4 offset:6144\n"
    "v_mul_f32 v153, v14, v153\n"
    "v_mfma_f32_16x16x32_bf16 v[176:179], v[240:243], v[68:71], v[176:179]\n"
    "s_waitcnt lgkmcnt(6)\n"
    "v_mul_f32 v154, v14, v154\n"
    "v_mul_f32 v155, v14, v155\n"
    "v_mfma_f32_16x16x32_bf16 v[188:191], v[244:247], v[68:71], v[188:191]\n"
    "ds_read_b64_tr_b16 v[228:229], v5 offset:4096\n"
    "v_cvt_pk_bf16_f32 v152, v152, v153\n"
    "v_mfma_f32_16x16x32_bf16 v[184:187], v[244:247], v[64:67], v[184:187]\n"
    "ds_read_b64_tr_b16 v[230:231], v5 offset:6144\n"
    "v_cvt_pk_bf16_f32 v153, v154, v155\n"
    "v_mfma_f32_16x16x32_bf16 v[180:183], v[244:247], v[60:63], v[180:183]\n"
    "s_waitcnt lgkmcnt(6)\n"
    "buffer_store_dwordx4 v[148:151], v11, s[96:99], s64 offen sc1\n"
    "v_mul_f32 v164, v14, v164\n"
    "v_mfma_f32_16x16x32_bf16 v[168:171], v[216:219], v[72:75], v[168:171]\n"
    "ds_read_b64_tr_b16 v[232:233], v4 offset:8192\n"
    "v_mul_f32 v165, v14, v165\n"
    "v_mfma_f32_16x16x32_bf16 v[172:175], v[216:219], v[76:79], v[172:175]\n"
    "ds_read_b64_tr_b16 v[234:235], v4 offset:10240\n"
    "v_mul_f32 v166, v14, v166\n"
    "v_mfma_f32_16x16x32_bf16 v[176:179], v[216:219], v[80:83], v[176:179]\n"
    "s_waitcnt lgkmcnt(6)\n"
    "v_mul_f32 v167, v14, v167\n"
    "v_cvt_pk_bf16_f32 v154, v164, v165\n"
    "v_mfma_f32_16x16x32_bf16 v[188:191], v[220:223], v[80:83], v[188:191]\n"
    "ds_read_b64_tr_b16 v[236:237], v5 offset:8192\n"
    "v_cvt_pk_bf16_f32 v155, v166, v167\n"
    "v_mfma_f32_16x16x32_bf16 v[184:187], v[220:223], v[76:79], v[184:187]\n"
    "ds_read_b64_tr_b16 v[238:239], v5 offset:10240\n"
    "v_mfma_f32_16x16x32_bf16 v[180:183], v[220:223], v[72:75], v[180:183]\n"
    "s_waitcnt lgkmcnt(6)\n"
    "v_mfma_f32_16x16x32_bf16 v[168:171], v[224:227], v[84:87], v[168:171]\n"
    "s_mov_b32 s65, 41984\n"
    "ds_read_b64_tr_b16 v[240:241], v4 offset:12288\n"
    "v_mfma_f32_16x16x32_bf16 v[172:175], v[224:227], v[88:91], v[172:175]\n"
    "ds_read_b64_tr_b16 v[242:243], v4 offset:14336\n"
    "v_add_u32 v4, 0x4000, v4\n"
    "v_mfma_f32_16x16x32_bf16 v[176:179], v[224:227], v[92:95], v[176:179]\n"
    "s_waitcnt lgkmcnt(6)\n"
    "v_mfma_f32_16x16x32_bf16 v[188:191], v[228:231], v[92:95], v[188:191]\n"
    "ds_read_b64_tr_b16 v[244:245], v5 offset:12288\n"
    "v_mfma_f32_16x16x32_bf16 v[184:187], v[228:231], v[88:91], v[184:187]\n"
    "ds_read_b64_tr_b16 v[246:247], v5 offset:14336\n"
    "v_add_u32 v5, 0x4000, v5\n"
    "v_mfma_f32_16x16x32_bf16 v[180:183], v[228:231], v[84:87], v[180:183]\n"
    "s_waitcnt lgkmcnt(6)\n"
    "buffer_store_dwordx4 v[152:155], v11, s[96:99], s65 offen sc1\n"
    "v_mfma_f32_16x16x32_bf16 v[168:171], v[232:235], v[96:99], v[168:171]\n"
    "ds_read_b64_tr_b16 v[216:217], v6 offset:0\n"
    "v_mfma_f32_16x16x32_bf16 v[172:175], v[232:235], v[100:103], v[172:175]\n"
    "ds_read_b64_tr_b16 v[218:219], v6 offset:2048\n"
    "v_mfma_f32_16x16x32_bf16 v[176:179], v[232:235], v[104:107], v[176:179]\n"
    "s_waitcnt lgkmcnt(6)\n"
    "s_waitcnt vmcnt(7)\n"
    "buffer_store_dwordx2 v[254:255], v10, s[28:31], 0 offen sc1\n"
    "v_mfma_f32_16x16x32_bf16 v[188:191], v[236:239], v[104:107], v[188:191]\n"
    "ds_read_b64_tr_b16 v[220:221], v7 offset:0\n"
    "v_mfma_f32_16x16x32_bf16 v[184:187], v[236:239], v[100:103], v[184:187]\n"
    "ds_read_b64_tr_b16 v[222:223], v7 offset:2048\n"
    "v_add_u32 v254, 1, v254\n"
    "v_mfma_f32_16x16x32_bf16 v[180:183], v[236:239], v[96:99], v[180:183]\n"
    "s_waitcnt lgkmcnt(6)\n"
    "v_mfma_f32_16x16x32_bf16 v[168:171], v[240:243], v[108:111], v[168:171]\n"
    "ds_read_b64_tr_b16 v[224:225], v6 offset:4096\n"
    "v_mfma_f32_16x16x32_bf16 v[172:175], v[240:243], v[112:115], v[172:175]\n"
    "ds_read_b64_tr_b16 v[226:227], v6 offset:6144\n"
    "v_mfma_f32_16x16x32_bf16 v[176:179], v[240:243], v[116:119], v[176:179]\n"
    "s_waitcnt lgkmcnt(6)\n"
    "v_mfma_f32_16x16x32_bf16 v[188:191], v[244:247], v[116:119], v[188:191]\n"
    "ds_read_b64_tr_b16 v[228:229], v7 offset:4096\n"
    "v_mfma_f32_16x16x32_bf16 v[184:187], v[244:247], v[112:115], v[184:187]\n"
    "ds_read_b64_tr_b16 v[230:231], v7 offset:6144\n"
    "v_mul_f32 v168, v12, v168\n"
    "v_mfma_f32_16x16x32_bf16 v[180:183], v[244:247], v[108:111], v[180:183]\n"
    "s_waitcnt lgkmcnt(6)\n"
    "v_mul_f32 v169, v12, v169\n"
    "v_mul_f32 v170, v12, v170\n"
    "v_mfma_f32_16x16x32_bf16 v[192:195], v[216:219], v[24:27], 0\n"
    "ds_read_b64_tr_b16 v[232:233], v6 offset:8192\n"
    "v_mul_f32 v171, v12, v171\n"
    "v_mfma_f32_16x16x32_bf16 v[196:199], v[216:219], v[28:31], 0\n"
    "ds_read_b64_tr_b16 v[234:235], v6 offset:10240\n"
    "v_cvt_pk_bf16_f32 v168, v168, v169\n"
    "v_mfma_f32_16x16x32_bf16 v[200:203], v[216:219], v[32:35], 0\n"
    "s_waitcnt lgkmcnt(6)\n"
    "v_cvt_pk_bf16_f32 v169, v170, v171\n"
    "v_mul_f32 v172, v13, v172\n"
    "v_mfma_f32_16x16x32_bf16 v[212:215], v[220:223], v[32:35], 0\n"
    "ds_read_b64_tr_b16 v[236:237], v7 offset:8192\n"
    "v_mul_f32 v180, v12, v180\n"
    "v_mfma_f32_16x16x32_bf16 v[208:211], v[220:223], v[28:31], 0\n"
    "ds_read_b64_tr_b16 v[238:239], v7 offset:10240\n"
    "v_mul_f32 v181, v12, v181\n"
    "v_mfma_f32_16x16x32_bf16 v[204:207], v[220:223], v[24:27], 0\n"
    "s_waitcnt lgkmcnt(6)\n"
    "v_mul_f32 v182, v12, v182\n"
    "v_mul_f32 v183, v12, v183\n"
    "v_mfma_f32_16x16x32_bf16 v[192:195], v[224:227], v[36:39], v[192:195]\n"
    "s_mov_b32 s64, 55296\n"
    "ds_read_b64_tr_b16 v[240:241], v6 offset:12288\n"
    "v_cvt_pk_bf16_f32 v170, v180, v181\n"
    "v_mfma_f32_16x16x32_bf16 v[196:199], v[224:227], v[40:43], v[196:199]\n"
    "ds_read_b64_tr_b16 v[242:243], v6 offset:14336\n"
    "v_add_u32 v6, 0x4000, v6\n"
    "v_mfma_f32_16x16x32_bf16 v[200:203], v[224:227], v[44:47], v[200:203]\n"
    "s_waitcnt lgkmcnt(6)\n"
    "v_cvt_pk_bf16_f32 v171, v182, v183\n"
    "v_mul_f32 v173, v13, v173\n"
    "v_mfma_f32_16x16x32_bf16 v[212:215], v[228:231], v[44:47], v[212:215]\n"
    "ds_read_b64_tr_b16 v[244:245], v7 offset:12288\n"
    "v_mul_f32 v174, v13, v174\n"
    "v_mfma_f32_16x16x32_bf16 v[208:211], v[228:231], v[40:43], v[208:211]\n"
    "ds_read_b64_tr_b16 v[246:247], v7 offset:14336\n"
    "v_add_u32 v7, 0x4000, v7\n"
    "v_mfma_f32_16x16x32_bf16 v[204:207], v[228:231], v[36:39], v[204:207]\n"
    "s_waitcnt lgkmcnt(6)\n"
    "buffer_store_dwordx4 v[168:171], v11, s[96:99], s64 offen sc1\n"
    "v_mul_f32 v175, v13, v175\n"
    "v_mfma_f32_16x16x32_bf16 v[192:195], v[232:235], v[48:51], v[192:195]\n"
    "ds_read_b64_tr_b16 v[216:217], v6 offset:0\n"
    "v_cvt_pk_bf16_f32 v172, v172, v173\n"
    "v_mfma_f32_16x16x32_bf16 v[196:199], v[232:235], v[52:55], v[196:199]\n"
    "ds_read_b64_tr_b16 v[218:219], v6 offset:2048\n"
    "v_cvt_pk_bf16_f32 v173, v174, v175\n"
    "v_mfma_f32_16x16x32_bf16 v[200:203], v[232:235], v[56:59], v[200:203]\n"
    "s_waitcnt lgkmcnt(6)\n"
    "v_mul_f32 v184, v13, v184\n"
    "v_mul_f32 v185, v13, v185\n"
    "v_mfma_f32_16x16x32_bf16 v[212:215], v[236:239], v[56:59], v[212:215]\n"
    "ds_read_b64_tr_b16 v[220:221], v7 offset:0\n"
    "v_mul_f32 v186, v13, v186\n"
    "v_mfma_f32_16x16x32_bf16 v[208:211], v[236:239], v[52:55], v[208:211]\n"
    "ds_read_b64_tr_b16 v[222:223], v7 offset:2048\n"
    "v_mul_f32 v187, v13, v187\n"
    "v_mfma_f32_16x16x32_bf16 v[204:207], v[236:239], v[48:51], v[204:207]\n"
    "s_waitcnt lgkmcnt(6)\n"
    "v_cvt_pk_bf16_f32 v174, v184, v185\n"
    "v_cvt_pk_bf16_f32 v175, v186, v187\n"
    "v_mfma_f32_16x16x32_bf16 v[192:195], v[240:243], v[60:63], v[192:195]\n"
    "s_mov_b32 s65, 57344\n"
    "ds_read_b64_tr_b16 v[224:225], v6 offset:4096\n"
    "v_mul_f32 v176, v14, v176\n"
    "v_mfma_f32_16x16x32_bf16 v[196:199], v[240:243], v[64:67], v[196:199]\n"
    "ds_read_b64_tr_b16 v[226:227], v6 offset:6144\n"
    "v_mul_f32 v177, v14, v177\n"
    "v_mfma_f32_16x16x32_bf16 v[200:203], v[240:243], v[68:71], v[200:203]\n"
    "s_waitcnt lgkmcnt(6)\n"
    "v_mul_f32 v178, v14, v178\n"
    "v_mul_f32 v179, v14, v179\n"
    "v_mfma_f32_16x16x32_bf16 v[212:215], v[244:247], v[68:71], v[212:215]\n"
    "ds_read_b64_tr_b16 v[228:229], v7 offset:4096\n"
    "v_cvt_pk_bf16_f32 v176, v176, v177\n"
    "v_mfma_f32_16x16x32_bf16 v[208:211], v[244:247], v[64:67], v[208:211]\n"
    "ds_read_b64_tr_b16 v[230:231], v7 offset:6144\n"
    "v_cvt_pk_bf16_f32 v177, v178, v179\n"
    "v_mfma_f32_16x16x32_bf16 v[204:207], v[244:247], v[60:63], v[204:207]\n"
    "s_waitcnt lgkmcnt(6)\n"
    "buffer_store_dwordx4 v[172:175], v11, s[96:99], s65 offen sc1\n"
    "v_mul_f32 v188, v14, v188\n"
    "v_mfma_f32_16x16x32_bf16 v[192:195], v[216:219], v[72:75], v[192:195]\n"
    "ds_read_b64_tr_b16 v[232:233], v6 offset:8192\n"
    "v_mul_f32 v189, v14, v189\n"
    "v_mfma_f32_16x16x32_bf16 v[196:199], v[216:219], v[76:79], v[196:199]\n"
    "ds_read_b64_tr_b16 v[234:235], v6 offset:10240\n"
    "v_mul_f32 v190, v14, v190\n"
    "v_mfma_f32_16x16x32_bf16 v[200:203], v[216:219], v[80:83], v[200:203]\n"
    "s_waitcnt lgkmcnt(6)\n"
    "v_mul_f32 v191, v14, v191\n"
    "v_cvt_pk_bf16_f32 v178, v188, v189\n"
    "v_mfma_f32_16x16x32_bf16 v[212:215], v[220:223], v[80:83], v[212:215]\n"
    "ds_read_b64_tr_b16 v[236:237], v7 offset:8192\n"
    "v_cvt_pk_bf16_f32 v179, v190, v191\n"
    "v_mfma_f32_16x16x32_bf16 v[208:211], v[220:223], v[76:79], v[208:211]\n"
    "ds_read_b64_tr_b16 v[238:239], v7 offset:10240\n"
    "v_mfma_f32_16x16x32_bf16 v[204:207], v[220:223], v[72:75], v[204:207]\n"
    "s_waitcnt lgkmcnt(6)\n"
    "v_mfma_f32_16x16x32_bf16 v[192:195], v[224:227], v[84:87], v[192:195]\n"
    "s_mov_b32 s64, 59392\n"
    "ds_read_b64_tr_b16 v[240:241], v6 offset:12288\n"
    "v_mfma_f32_16x16x32_bf16 v[196:199], v[224:227], v[88:91], v[196:199]\n"
    "ds_read_b64_tr_b16 v[242:243], v6 offset:14336\n"
    "v_add_u32 v6, 0x4000, v6\n"
    "v_mfma_f32_16x16x32_bf16 v[200:203], v[224:227], v[92:95], v[200:203]\n"
    "s_waitcnt lgkmcnt(6)\n"
    "v_mfma_f32_16x16x32_bf16 v[212:215], v[228:231], v[92:95], v[212:215]\n"
    "ds_read_b64_tr_b16 v[244:245], v7 offset:12288\n"
    "v_mfma_f32_16x16x32_bf16 v[208:211], v[228:231], v[88:91], v[208:211]\n"
    "ds_read_b64_tr_b16 v[246:247], v7 offset:14336\n"
    "v_add_u32 v7, 0x4000, v7\n"
    "v_mfma_f32_16x16x32_bf16 v[204:207], v[228:231], v[84:87], v[204:207]\n"
    "s_waitcnt lgkmcnt(6)\n"
    "buffer_store_dwordx4 v[176:179], v11, s[96:99], s64 offen sc1\n"
    "v_mfma_f32_16x16x32_bf16 v[192:195], v[232:235], v[96:99], v[192:195]\n"
    "ds_read_b64_tr_b16 v[216:217], v4 offset:0\n"
    "v_mfma_f32_16x16x32_bf16 v[196:199], v[232:235], v[100:103], v[196:199]\n"
    "ds_read_b64_tr_b16 v[218:219], v4 offset:2048\n"
    "v_mfma_f32_16x16x32_bf16 v[200:203], v[232:235], v[104:107], v[200:203]\n"
    "s_waitcnt lgkmcnt(6)\n"
    "v_mfma_f32_16x16x32_bf16 v[212:215], v[236:239], v[104:107], v[212:215]\n"
    "ds_read_b64_tr_b16 v[220:221], v5 offset:0\n"
    "v_mfma_f32_16x16x32_bf16 v[208:211], v[236:239], v[100:103], v[208:211]\n"
    "ds_read_b64_tr_b16 v[222:223], v5 offset:2048\n"
    "v_mfma_f32_16x16x32_bf16 v[204:207], v[236:239], v[96:99], v[204:207]\n"
    "s_waitcnt lgkmcnt(6)\n"
    "v_mfma_f32_16x16x32_bf16 v[192:195], v[240:243], v[108:111], v[192:195]\n"
    "ds_read_b64_tr_b16 v[224:225], v4 offset:4096\n"
    "v_mfma_f32_16x16x32_bf16 v[196:199], v[240:243], v[112:115], v[196:199]\n"
    "ds_read_b64_tr_b16 v[226:227], v4 offset:6144\n"
    "v_mfma_f32_16x16x32_bf16 v[200:203], v[240:243], v[116:119], v[200:203]\n"
    "s_waitcnt lgkmcnt(6)\n"
    "v_mfma_f32_16x16x32_bf16 v[212:215], v[244:247], v[116:119], v[212:215]\n"
    "ds_read_b64_tr_b16 v[228:229], v5 offset:4096\n"
    "v_mfma_f32_16x16x32_bf16 v[208:211], v[244:247], v[112:115], v[208:211]\n"
    "ds_read_b64_tr_b16 v[230:231], v5 offset:6144\n"
    "v_mul_f32 v192, v12, v192\n"
    "v_mfma_f32_16x16x32_bf16 v[204:207], v[244:247], v[108:111], v[204:207]\n"
    "s_waitcnt lgkmcnt(6)\n"
    "v_mul_f32 v193, v12, v193\n"
    "v_mul_f32 v194, v12, v194\n"
    "v_mfma_f32_16x16x32_bf16 v[120:123], v[216:219], v[24:27], 0\n"
    "ds_read_b64_tr_b16 v[232:233], v4 offset:8192\n"
    "v_mul_f32 v195, v12, v195\n"
    "v_mfma_f32_16x16x32_bf16 v[124:127], v[216:219], v[28:31], 0\n"
    "ds_read_b64_tr_b16 v[234:235], v4 offset:10240\n"
    "v_cvt_pk_bf16_f32 v192, v192, v193\n"
    "v_mfma_f32_16x16x32_bf16 v[128:131], v[216:219], v[32:35], 0\n"
    "s_waitcnt lgkmcnt(6)\n"
    "v_cvt_pk_bf16_f32 v193, v194, v195\n"
    "v_mul_f32 v196, v13, v196\n"
    "v_mfma_f32_16x16x32_bf16 v[140:143], v[220:223], v[32:35], 0\n"
    "ds_read_b64_tr_b16 v[236:237], v5 offset:8192\n"
    "v_mul_f32 v204, v12, v204\n"
    "v_mfma_f32_16x16x32_bf16 v[136:139], v[220:223], v[28:31], 0\n"
    "ds_read_b64_tr_b16 v[238:239], v5 offset:10240\n"
    "v_mul_f32 v205, v12, v205\n"
    "v_mfma_f32_16x16x32_bf16 v[132:135], v[220:223], v[24:27], 0\n"
    "s_waitcnt lgkmcnt(6)\n"
    "v_mul_f32 v206, v12, v206\n"
    "v_mul_f32 v207, v12, v207\n"
    "v_mfma_f32_16x16x32_bf16 v[120:123], v[224:227], v[36:39], v[120:123]\n"
    "s_mov_b32 s65, 56320\n"
    "ds_read_b64_tr_b16 v[240:241], v4 offset:12288\n"
    "v_cvt_pk_bf16_f32 v194, v204, v205\n"
    "v_mfma_f32_16x16x32_bf16 v[124:127], v[224:227], v[40:43], v[124:127]\n"
    "ds_read_b64_tr_b16 v[242:243], v4 offset:14336\n"
    "v_add_u32 v4, 0xfffdc000, v4\n"
    "v_mfma_f32_16x16x32_bf16 v[128:131], v[224:227], v[44:47], v[128:131]\n"
    "s_waitcnt lgkmcnt(6)\n"
    "v_cvt_pk_bf16_f32 v195, v206, v207\n"
    "v_mul_f32 v197, v13, v197\n"
    "v_mfma_f32_16x16x32_bf16 v[140:143], v[228:231], v[44:47], v[140:143]\n"
    "ds_read_b64_tr_b16 v[244:245], v5 offset:12288\n"
    "v_mul_f32 v198, v13, v198\n"
    "v_mfma_f32_16x16x32_bf16 v[136:139], v[228:231], v[40:43], v[136:139]\n"
    "ds_read_b64_tr_b16 v[246:247], v5 offset:14336\n"
    "v_add_u32 v5, 0xfffdc000, v5\n"
    "v_mfma_f32_16x16x32_bf16 v[132:135], v[228:231], v[36:39], v[132:135]\n"
    "s_waitcnt lgkmcnt(6)\n"
    "buffer_store_dwordx4 v[192:195], v11, s[96:99], s65 offen sc1\n"
    "v_mul_f32 v199, v13, v199\n"
    "v_mfma_f32_16x16x32_bf16 v[120:123], v[232:235], v[48:51], v[120:123]\n"
    "ds_read_b64_tr_b16 v[216:217], v4 offset:0\n"
    "v_cvt_pk_bf16_f32 v196, v196, v197\n"
    "v_mfma_f32_16x16x32_bf16 v[124:127], v[232:235], v[52:55], v[124:127]\n"
    "ds_read_b64_tr_b16 v[218:219], v4 offset:2048\n"
    "v_cvt_pk_bf16_f32 v197, v198, v199\n"
    "v_mfma_f32_16x16x32_bf16 v[128:131], v[232:235], v[56:59], v[128:131]\n"
    "s_waitcnt lgkmcnt(6)\n"
    "v_mul_f32 v208, v13, v208\n"
    "v_mul_f32 v209, v13, v209\n"
    "v_mfma_f32_16x16x32_bf16 v[140:143], v[236:239], v[56:59], v[140:143]\n"
    "ds_read_b64_tr_b16 v[220:221], v5 offset:0\n"
    "v_mul_f32 v210, v13, v210\n"
    "v_mfma_f32_16x16x32_bf16 v[136:139], v[236:239], v[52:55], v[136:139]\n"
    "ds_read_b64_tr_b16 v[222:223], v5 offset:2048\n"
    "v_mul_f32 v211, v13, v211\n"
    "v_mfma_f32_16x16x32_bf16 v[132:135], v[236:239], v[48:51], v[132:135]\n"
    "s_waitcnt lgkmcnt(6)\n"
    "v_cvt_pk_bf16_f32 v198, v208, v209\n"
    "v_cvt_pk_bf16_f32 v199, v210, v211\n"
    "v_mfma_f32_16x16x32_bf16 v[120:123], v[240:243], v[60:63], v[120:123]\n"
    "s_mov_b32 s64, 58368\n"
    "ds_read_b64_tr_b16 v[224:225], v4 offset:4096\n"
    "v_mul_f32 v200, v14, v200\n"
    "v_mfma_f32_16x16x32_bf16 v[124:127], v[240:243], v[64:67], v[124:127]\n"
    "ds_read_b64_tr_b16 v[226:227], v4 offset:6144\n"
    "v_mul_f32 v201, v14, v201\n"
    "v_mfma_f32_16x16x32_bf16 v[128:131], v[240:243], v[68:71], v[128:131]\n"
    "s_waitcnt lgkmcnt(6)\n"
    "v_mul_f32 v202, v14, v202\n"
    "v_mul_f32 v203, v14, v203\n"
    "v_mfma_f32_16x16x32_bf16 v[140:143], v[244:247], v[68:71], v[140:143]\n"
    "ds_read_b64_tr_b16 v[228:229], v5 offset:4096\n"
    "v_cvt_pk_bf16_f32 v200, v200, v201\n"
    "v_mfma_f32_16x16x32_bf16 v[136:139], v[244:247], v[64:67], v[136:139]\n"
    "ds_read_b64_tr_b16 v[230:231], v5 offset:6144\n"
    "v_cvt_pk_bf16_f32 v201, v202, v203\n"
    "v_mfma_f32_16x16x32_bf16 v[132:135], v[244:247], v[60:63], v[132:135]\n"
    "s_waitcnt lgkmcnt(6)\n"
    "buffer_store_dwordx4 v[196:199], v11, s[96:99], s64 offen sc1\n"
    "v_mul_f32 v212, v14, v212\n"
    "v_mfma_f32_16x16x32_bf16 v[120:123], v[216:219], v[72:75], v[120:123]\n"
    "ds_read_b64_tr_b16 v[232:233], v4 offset:8192\n"
    "v_mul_f32 v213, v14, v213\n"
    "v_mfma_f32_16x16x32_bf16 v[124:127], v[216:219], v[76:79], v[124:127]\n"
    "ds_read_b64_tr_b16 v[234:235], v4 offset:10240\n"
    "v_mul_f32 v214, v14, v214\n"
    "v_mfma_f32_16x16x32_bf16 v[128:131], v[216:219], v[80:83], v[128:131]\n"
    "s_waitcnt lgkmcnt(6)\n"
    "v_mul_f32 v215, v14, v215\n"
    "v_cvt_pk_bf16_f32 v202, v212, v213\n"
    "v_mfma_f32_16x16x32_bf16 v[140:143], v[220:223], v[80:83], v[140:143]\n"
    "ds_read_b64_tr_b16 v[236:237], v5 offset:8192\n"
    "v_cvt_pk_bf16_f32 v203, v214, v215\n"
    "v_mfma_f32_16x16x32_bf16 v[136:139], v[220:223], v[76:79], v[136:139]\n"
    "ds_read_b64_tr_b16 v[238:239], v5 offset:10240\n"
    "v_mfma_f32_16x16x32_bf16 v[132:135], v[220:223], v[72:75], v[132:135]\n"
    "s_waitcnt lgkmcnt(6)\n"
    "v_mfma_f32_16x16x32_bf16 v[120:123], v[224:227], v[84:87], v[120:123]\n"
    "s_mov_b32 s65, 60416\n"
    "ds_read_b64_tr_b16 v[240:241], v4 offset:12288\n"
    "v_mfma_f32_16x16x32_bf16 v[124:127], v[224:227], v[88:91], v[124:127]\n"
    "ds_read_b64_tr_b16 v[242:243], v4 offset:14336\n"
    "v_add_u32 v4, 0x4000, v4\n"
    "v_mfma_f32_16x16x32_bf16 v[128:131], v[224:227], v[92:95], v[128:131]\n"
    "s_waitcnt lgkmcnt(6)\n"
    "v_mfma_f32_16x16x32_bf16 v[140:143], v[228:231], v[92:95], v[140:143]\n"
    "ds_read_b64_tr_b16 v[244:245], v5 offset:12288\n"
    "v_mfma_f32_16x16x32_bf16 v[136:139], v[228:231], v[88:91], v[136:139]\n"
    "ds_read_b64_tr_b16 v[246:247], v5 offset:14336\n"
    "v_add_u32 v5, 0x4000, v5\n"
    "v_mfma_f32_16x16x32_bf16 v[132:135], v[228:231], v[84:87], v[132:135]\n"
    "s_waitcnt lgkmcnt(6)\n"
    "buffer_store_dwordx4 v[200:203], v11, s[96:99], s65 offen sc1\n"
    "v_mfma_f32_16x16x32_bf16 v[120:123], v[232:235], v[96:99], v[120:123]\n"
    "ds_read_b64_tr_b16 v[216:217], v6 offset:0\n"
    "v_mfma_f32_16x16x32_bf16 v[124:127], v[232:235], v[100:103], v[124:127]\n"
    "ds_read_b64_tr_b16 v[218:219], v6 offset:2048\n"
    "v_mfma_f32_16x16x32_bf16 v[128:131], v[232:235], v[104:107], v[128:131]\n"
    "s_waitcnt lgkmcnt(6)\n"
    "s_waitcnt vmcnt(7)\n"
    "buffer_store_dwordx2 v[254:255], v10, s[28:31], 0 offen sc1\n"
    "v_mfma_f32_16x16x32_bf16 v[140:143], v[236:239], v[104:107], v[140:143]\n"
    "ds_read_b64_tr_b16 v[220:221], v7 offset:0\n"
    "v_mfma_f32_16x16x32_bf16 v[136:139], v[236:239], v[100:103], v[136:139]\n"
    "ds_read_b64_tr_b16 v[222:223], v7 offset:2048\n"
    "v_add_u32 v254, 1, v254\n"
    "v_mfma_f32_16x16x32_bf16 v[132:135], v[236:239], v[96:99], v[132:135]\n"
    "s_waitcnt lgkmcnt(6)\n"
    "v_mfma_f32_16x16x32_bf16 v[120:123], v[240:243], v[108:111], v[120:123]\n"
    "ds_read_b64_tr_b16 v[224:225], v6 offset:4096\n"
    "v_mfma_f32_16x16x32_bf16 v[124:127], v[240:243], v[112:115], v[124:127]\n"
    "ds_read_b64_tr_b16 v[226:227], v6 offset:6144\n"
    "v_mfma_f32_16x16x32_bf16 v[128:131], v[240:243], v[116:119], v[128:131]\n"
    "s_waitcnt lgkmcnt(6)\n"
    "v_mfma_f32_16x16x32_bf16 v[140:143], v[244:247], v[116:119], v[140:143]\n"
    "ds_read_b64_tr_b16 v[228:229], v7 offset:4096\n"
    "v_mfma_f32_16x16x32_bf16 v[136:139], v[244:247], v[112:115], v[136:139]\n"
    "ds_read_b64_tr_b16 v[230:231], v7 offset:6144\n"
    "v_mul_f32 v120, v12, v120\n"
    "v_mfma_f32_16x16x32_bf16 v[132:135], v[244:247], v[108:111], v[132:135]\n"
    "s_waitcnt lgkmcnt(6)\n"
    "v_mul_f32 v121, v12, v121\n"
    "v_mul_f32 v122, v12, v122\n"
    "v_mfma_f32_16x16x32_bf16 v[144:147], v[216:219], v[24:27], 0\n"
    "ds_read_b64_tr_b16 v[232:233], v6 offset:8192\n"
    "v_mul_f32 v123, v12, v123\n"
    "v_mfma_f32_16x16x32_bf16 v[148:151], v[216:219], v[28:31], 0\n"
    "ds_read_b64_tr_b16 v[234:235], v6 offset:10240\n"
    "v_cvt_pk_bf16_f32 v120, v120, v121\n"
    "v_mfma_f32_16x16x32_bf16 v[152:155], v[216:219], v[32:35], 0\n"
    "s_waitcnt lgkmcnt(6)\n"
    "v_cvt_pk_bf16_f32 v121, v122, v123\n"
    "v_mul_f32 v124, v13, v124\n"
    "v_mfma_f32_16x16x32_bf16 v[164:167], v[220:223], v[32:35], 0\n"
    "ds_read_b64_tr_b16 v[236:237], v7 offset:8192\n"
    "v_mul_f32 v132, v12, v132\n"
    "v_mfma_f32_16x16x32_bf16 v[160:163], v[220:223], v[28:31], 0\n"
    "ds_read_b64_tr_b16 v[238:239], v7 offset:10240\n"
    "v_mul_f32 v133, v12, v133\n"
    "v_mfma_f32_16x16x32_bf16 v[156:159], v[220:223], v[24:27], 0\n"
    "s_waitcnt lgkmcnt(6)\n"
    "v_mul_f32 v134, v12, v134\n"
    "v_mul_f32 v135, v12, v135\n"
    "v_mfma_f32_16x16x32_bf16 v[144:147], v[224:227], v[36:39], v[144:147]\n"
    "s_mov_b32 s64, 73728\n"
    "ds_read_b64_tr_b16 v[240:241], v6 offset:12288\n"
    "v_cvt_pk_bf16_f32 v122, v132, v133\n"
    "v_mfma_f32_16x16x32_bf16 v[148:151], v[224:227], v[40:43], v[148:151]\n"
    "ds_read_b64_tr_b16 v[242:243], v6 offset:14336\n"
    "v_add_u32 v6, 0xfffdc000, v6\n"
    "v_mfma_f32_16x16x32_bf16 v[152:155], v[224:227], v[44:47], v[152:155]\n"
    "s_waitcnt lgkmcnt(6)\n"
    "v_cvt_pk_bf16_f32 v123, v134, v135\n"
    "v_mul_f32 v125, v13, v125\n"
    "v_mfma_f32_16x16x32_bf16 v[164:167], v[228:231], v[44:47], v[164:167]\n"
    "ds_read_b64_tr_b16 v[244:245], v7 offset:12288\n"
    "v_mul_f32 v126, v13, v126\n"
    "v_mfma_f32_16x16x32_bf16 v[160:163], v[228:231], v[40:43], v[160:163]\n"
    "ds_read_b64_tr_b16 v[246:247], v7 offset:14336\n"
    "v_add_u32 v7, 0xfffdc000, v7\n"
    "v_mfma_f32_16x16x32_bf16 v[156:159], v[228:231], v[36:39], v[156:159]\n"
    "s_waitcnt lgkmcnt(6)\n"
    "buffer_store_dwordx4 v[120:123], v11, s[96:99], s64 offen sc1\n"
    "v_mul_f32 v127, v13, v127\n"
    "v_mfma_f32_16x16x32_bf16 v[144:147], v[232:235], v[48:51], v[144:147]\n"
    "ds_read_b64_tr_b16 v[216:217], v6 offset:0\n"
    "v_cvt_pk_bf16_f32 v124, v124, v125\n"
    "v_mfma_f32_16x16x32_bf16 v[148:151], v[232:235], v[52:55], v[148:151]\n"
    "ds_read_b64_tr_b16 v[218:219], v6 offset:2048\n"
    "v_cvt_pk_bf16_f32 v125, v126, v127\n"
    "v_mfma_f32_16x16x32_bf16 v[152:155], v[232:235], v[56:59], v[152:155]\n"
    "s_waitcnt lgkmcnt(6)\n"
    "v_mul_f32 v136, v13, v136\n"
    "v_mul_f32 v137, v13, v137\n"
    "v_mfma_f32_16x16x32_bf16 v[164:167], v[236:239], v[56:59], v[164:167]\n"
    "ds_read_b64_tr_b16 v[220:221], v7 offset:0\n"
    "v_mul_f32 v138, v13, v138\n"
    "v_mfma_f32_16x16x32_bf16 v[160:163], v[236:239], v[52:55], v[160:163]\n"
    "ds_read_b64_tr_b16 v[222:223], v7 offset:2048\n"
    "v_mul_f32 v139, v13, v139\n"
    "v_mfma_f32_16x16x32_bf16 v[156:159], v[236:239], v[48:51], v[156:159]\n"
    "s_waitcnt lgkmcnt(6)\n"
    "v_cvt_pk_bf16_f32 v126, v136, v137\n"
    "v_cvt_pk_bf16_f32 v127, v138, v139\n"
    "v_mfma_f32_16x16x32_bf16 v[144:147], v[240:243], v[60:63], v[144:147]\n"
    "s_mov_b32 s65, 75776\n"
    "ds_read_b64_tr_b16 v[224:225], v6 offset:4096\n"
    "v_mul_f32 v128, v14, v128\n"
    "v_mfma_f32_16x16x32_bf16 v[148:151], v[240:243], v[64:67], v[148:151]\n"
    "ds_read_b64_tr_b16 v[226:227], v6 offset:6144\n"
    "v_mul_f32 v129, v14, v129\n"
    "v_mfma_f32_16x16x32_bf16 v[152:155], v[240:243], v[68:71], v[152:155]\n"
    "s_waitcnt lgkmcnt(6)\n"
    "v_mul_f32 v130, v14, v130\n"
    "v_mul_f32 v131, v14, v131\n"
    "v_mfma_f32_16x16x32_bf16 v[164:167], v[244:247], v[68:71], v[164:167]\n"
    "ds_read_b64_tr_b16 v[228:229], v7 offset:4096\n"
    "v_cvt_pk_bf16_f32 v128, v128, v129\n"
    "v_mfma_f32_16x16x32_bf16 v[160:163], v[244:247], v[64:67], v[160:163]\n"
    "ds_read_b64_tr_b16 v[230:231], v7 offset:6144\n"
    "v_cvt_pk_bf16_f32 v129, v130, v131\n"
    "v_mfma_f32_16x16x32_bf16 v[156:159], v[244:247], v[60:63], v[156:159]\n"
    "s_waitcnt lgkmcnt(6)\n"
    "buffer_store_dwordx4 v[124:127], v11, s[96:99], s65 offen sc1\n"
    "v_mul_f32 v140, v14, v140\n"
    "v_mfma_f32_16x16x32_bf16 v[144:147], v[216:219], v[72:75], v[144:147]\n"
    "ds_read_b64_tr_b16 v[232:233], v6 offset:8192\n"
    "v_mul_f32 v141, v14, v141\n"
    "v_mfma_f32_16x16x32_bf16 v[148:151], v[216:219], v[76:79], v[148:151]\n"
    "ds_read_b64_tr_b16 v[234:235], v6 offset:10240\n"
    "v_mul_f32 v142, v14, v142\n"
    "v_mfma_f32_16x16x32_bf16 v[152:155], v[216:219], v[80:83], v[152:155]\n"
    "s_waitcnt lgkmcnt(6)\n"
    "v_mul_f32 v143, v14, v143\n"
    "v_cvt_pk_bf16_f32 v130, v140, v141\n"
    "v_mfma_f32_16x16x32_bf16 v[164:167], v[220:223], v[80:83], v[164:167]\n"
    "ds_read_b64_tr_b16 v[236:237], v7 offset:8192\n"
    "v_cvt_pk_bf16_f32 v131, v142, v143\n"
    "v_mfma_f32_16x16x32_bf16 v[160:163], v[220:223], v[76:79], v[160:163]\n"
    "ds_read_b64_tr_b16 v[238:239], v7 offset:10240\n"
    "v_mfma_f32_16x16x32_bf16 v[156:159], v[220:223], v[72:75], v[156:159]\n"
    "s_waitcnt lgkmcnt(6)\n"
    "v_mfma_f32_16x16x32_bf16 v[144:147], v[224:227], v[84:87], v[144:147]\n"
    "s_mov_b32 s64, 77824\n"
    "ds_read_b64_tr_b16 v[240:241], v6 offset:12288\n"
    "v_mfma_f32_16x16x32_bf16 v[148:151], v[224:227], v[88:91], v[148:151]\n"
    "ds_read_b64_tr_b16 v[242:243], v6 offset:14336\n"
    "v_add_u32 v6, 0x4000, v6\n"
    "v_mfma_f32_16x16x32_bf16 v[152:155], v[224:227], v[92:95], v[152:155]\n"
    "s_waitcnt lgkmcnt(6)\n"
    "v_mfma_f32_16x16x32_bf16 v[164:167], v[228:231], v[92:95], v[164:167]\n"
    "ds_read_b64_tr_b16 v[244:245], v7 offset:12288\n"
    "v_mfma_f32_16x16x32_bf16 v[160:163], v[228:231], v[88:91], v[160:163]\n"
    "ds_read_b64_tr_b16 v[246:247], v7 offset:14336\n"
    "v_add_u32 v7, 0x4000, v7\n"
    "v_mfma_f32_16x16x32_bf16 v[156:159], v[228:231], v[84:87], v[156:159]\n"
    "s_waitcnt lgkmcnt(6)\n"
    "buffer_store_dwordx4 v[128:131], v11, s[96:99], s64 offen sc1\n"
    "v_mfma_f32_16x16x32_bf16 v[144:147], v[232:235], v[96:99], v[144:147]\n"
    "ds_read_b64_tr_b16 v[216:217], v4 offset:0\n"
    "v_mfma_f32_16x16x32_bf16 v[148:151], v[232:235], v[100:103], v[148:151]\n"
    "ds_read_b64_tr_b16 v[218:219], v4 offset:2048\n"
    "v_mfma_f32_16x16x32_bf16 v[152:155], v[232:235], v[104:107], v[152:155]\n"
    "s_waitcnt lgkmcnt(6)\n"
    "v_mfma_f32_16x16x32_bf16 v[164:167], v[236:239], v[104:107], v[164:167]\n"
    "ds_read_b64_tr_b16 v[220:221], v5 offset:0\n"
    "v_mfma_f32_16x16x32_bf16 v[160:163], v[236:239], v[100:103], v[160:163]\n"
    "ds_read_b64_tr_b16 v[222:223], v5 offset:2048\n"
    "v_mfma_f32_16x16x32_bf16 v[156:159], v[236:239], v[96:99], v[156:159]\n"
    "s_waitcnt lgkmcnt(6)\n"
    "v_mfma_f32_16x16x32_bf16 v[144:147], v[240:243], v[108:111], v[144:147]\n"
    "ds_read_b64_tr_b16 v[224:225], v4 offset:4096\n"
    "v_mfma_f32_16x16x32_bf16 v[148:151], v[240:243], v[112:115], v[148:151]\n"
    "ds_read_b64_tr_b16 v[226:227], v4 offset:6144\n"
    "v_mfma_f32_16x16x32_bf16 v[152:155], v[240:243], v[116:119], v[152:155]\n"
    "s_waitcnt lgkmcnt(6)\n"
    "v_mfma_f32_16x16x32_bf16 v[164:167], v[244:247], v[116:119], v[164:167]\n"
    "ds_read_b64_tr_b16 v[228:229], v5 offset:4096\n"
    "v_mfma_f32_16x16x32_bf16 v[160:163], v[244:247], v[112:115], v[160:163]\n"
    "ds_read_b64_tr_b16 v[230:231], v5 offset:6144\n"
    "v_mul_f32 v144, v12, v144\n"
    "v_mfma_f32_16x16x32_bf16 v[156:159], v[244:247], v[108:111], v[156:159]\n"
    "s_waitcnt lgkmcnt(6)\n"
    "v_mul_f32 v145, v12, v145\n"
    "v_mul_f32 v146, v12, v146\n"
    "v_mfma_f32_16x16x32_bf16 v[168:171], v[216:219], v[24:27], 0\n"
    "ds_read_b64_tr_b16 v[232:233], v4 offset:8192\n"
    "v_mul_f32 v147, v12, v147\n"
    "v_mfma_f32_16x16x32_bf16 v[172:175], v[216:219], v[28:31], 0\n"
    "ds_read_b64_tr_b16 v[234:235], v4 offset:10240\n"
    "v_cvt_pk_bf16_f32 v144, v144, v145\n"
    "v_mfma_f32_16x16x32_bf16 v[176:179], v[216:219], v[32:35], 0\n"
    "s_waitcnt lgkmcnt(6)\n"
    "v_cvt_pk_bf16_f32 v145, v146, v147\n"
    "v_mul_f32 v148, v13, v148\n"
    "v_mfma_f32_16x16x32_bf16 v[188:191], v[220:223], v[32:35], 0\n"
    "ds_read_b64_tr_b16 v[236:237], v5 offset:8192\n"
    "v_mul_f32 v156, v12, v156\n"
    "v_mfma_f32_16x16x32_bf16 v[184:187], v[220:223], v[28:31], 0\n"
    "ds_read_b64_tr_b16 v[238:239], v5 offset:10240\n"
    "v_mul_f32 v157, v12, v157\n"
    "v_mfma_f32_16x16x32_bf16 v[180:183], v[220:223], v[24:27], 0\n"
    "s_waitcnt lgkmcnt(6)\n"
    "v_mul_f32 v158, v12, v158\n"
    "v_mul_f32 v159, v12, v159\n"
    "v_mfma_f32_16x16x32_bf16 v[168:171], v[224:227], v[36:39], v[168:171]\n"
    "s_mov_b32 s65, 74752\n"
    "ds_read_b64_tr_b16 v[240:241], v4 offset:12288\n"
    "v_cvt_pk_bf16_f32 v146, v156, v157\n"
    "v_mfma_f32_16x16x32_bf16 v[172:175], v[224:227], v[40:43], v[172:175]\n"
    "ds_read_b64_tr_b16 v[242:243], v4 offset:14336\n"
    "v_add_u32 v4, 0x4000, v4\n"
    "v_mfma_f32_16x16x32_bf16 v[176:179], v[224:227], v[44:47], v[176:179]\n"
    "s_waitcnt lgkmcnt(6)\n"
    "v_cvt_pk_bf16_f32 v147, v158, v159\n"
    "v_mul_f32 v149, v13, v149\n"
    "v_mfma_f32_16x16x32_bf16 v[188:191], v[228:231], v[44:47], v[188:191]\n"
    "ds_read_b64_tr_b16 v[244:245], v5 offset:12288\n"
    "v_mul_f32 v150, v13, v150\n"
    "v_mfma_f32_16x16x32_bf16 v[184:187], v[228:231], v[40:43], v[184:187]\n"
    "ds_read_b64_tr_b16 v[246:247], v5 offset:14336\n"
    "v_add_u32 v5, 0x4000, v5\n"
    "v_mfma_f32_16x16x32_bf16 v[180:183], v[228:231], v[36:39], v[180:183]\n"
    "s_waitcnt lgkmcnt(6)\n"
    "buffer_store_dwordx4 v[144:147], v11, s[96:99], s65 offen sc1\n"
    "v_mul_f32 v151, v13, v151\n"
    "v_mfma_f32_16x16x32_bf16 v[168:171], v[232:235], v[48:51], v[168:171]\n"
    "ds_read_b64_tr_b16 v[216:217], v4 offset:0\n"
    "v_cvt_pk_bf16_f32 v148, v148, v149\n"
    "v_mfma_f32_16x16x32_bf16 v[172:175], v[232:235], v[52:55], v[172:175]\n"
    "ds_read_b64_tr_b16 v[218:219], v4 offset:2048\n"
    "v_cvt_pk_bf16_f32 v149, v150, v151\n"
    "v_mfma_f32_16x16x32_bf16 v[176:179], v[232:235], v[56:59], v[176:179]\n"
    "s_waitcnt lgkmcnt(6)\n"
    "v_mul_f32 v160, v13, v160\n"
    "v_mul_f32 v161, v13, v161\n"
    "v_mfma_f32_16x16x32_bf16 v[188:191], v[236:239], v[56:59], v[188:191]\n"
    "ds_read_b64_tr_b16 v[220:221], v5 offset:0\n"
    "v_mul_f32 v162, v13, v162\n"
    "v_mfma_f32_16x16x32_bf16 v[184:187], v[236:239], v[52:55], v[184:187]\n"
    "ds_read_b64_tr_b16 v[222:223], v5 offset:2048\n"
    "v_mul_f32 v163, v13, v163\n"
    "v_mfma_f32_16x16x32_bf16 v[180:183], v[236:239], v[48:51], v[180:183]\n"
    "s_waitcnt lgkmcnt(6)\n"
    "v_cvt_pk_bf16_f32 v150, v160, v161\n"
    "v_cvt_pk_bf16_f32 v151, v162, v163\n"
    "v_mfma_f32_16x16x32_bf16 v[168:171], v[240:243], v[60:63], v[168:171]\n"
    "s_mov_b32 s64, 76800\n"
    "ds_read_b64_tr_b16 v[224:225], v4 offset:4096\n"
    "v_mul_f32 v152, v14, v152\n"
    "v_mfma_f32_16x16x32_bf16 v[172:175], v[240:243], v[64:67], v[172:175]\n"
    "ds_read_b64_tr_b16 v[226:227], v4 offset:6144\n"
    "v_mul_f32 v153, v14, v153\n"
    "v_mfma_f32_16x16x32_bf16 v[176:179], v[240:243], v[68:71], v[176:179]\n"
    "s_waitcnt lgkmcnt(6)\n"
    "v_mul_f32 v154, v14, v154\n"
    "v_mul_f32 v155, v14, v155\n"
    "v_mfma_f32_16x16x32_bf16 v[188:191], v[244:247], v[68:71], v[188:191]\n"
    "ds_read_b64_tr_b16 v[228:229], v5 offset:4096\n"
    "v_cvt_pk_bf16_f32 v152, v152, v153\n"
    "v_mfma_f32_16x16x32_bf16 v[184:187], v[244:247], v[64:67], v[184:187]\n"
    "ds_read_b64_tr_b16 v[230:231], v5 offset:6144\n"
    "v_cvt_pk_bf16_f32 v153, v154, v155\n"
    "v_mfma_f32_16x16x32_bf16 v[180:183], v[244:247], v[60:63], v[180:183]\n"
    "s_waitcnt lgkmcnt(6)\n"
    "buffer_store_dwordx4 v[148:151], v11, s[96:99], s64 offen sc1\n"
    "v_mul_f32 v164, v14, v164\n"
    "v_mfma_f32_16x16x32_bf16 v[168:171], v[216:219], v[72:75], v[168:171]\n"
    "ds_read_b64_tr_b16 v[232:233], v4 offset:8192\n"
    "v_mul_f32 v165, v14, v165\n"
    "v_mfma_f32_16x16x32_bf16 v[172:175], v[216:219], v[76:79], v[172:175]\n"
    "ds_read_b64_tr_b16 v[234:235], v4 offset:10240\n"
    "v_mul_f32 v166, v14, v166\n"
    "v_mfma_f32_16x16x32_bf16 v[176:179], v[216:219], v[80:83], v[176:179]\n"
    "s_waitcnt lgkmcnt(6)\n"
    "v_mul_f32 v167, v14, v167\n"
    "v_cvt_pk_bf16_f32 v154, v164, v165\n"
    "v_mfma_f32_16x16x32_bf16 v[188:191], v[220:223], v[80:83], v[188:191]\n"
    "ds_read_b64_tr_b16 v[236:237], v5 offset:8192\n"
    "v_cvt_pk_bf16_f32 v155, v166, v167\n"
    "v_mfma_f32_16x16x32_bf16 v[184:187], v[220:223], v[76:79], v[184:187]\n"
    "ds_read_b64_tr_b16 v[238:239], v5 offset:10240\n"
    "v_mfma_f32_16x16x32_bf16 v[180:183], v[220:223], v[72:75], v[180:183]\n"
    "s_waitcnt lgkmcnt(6)\n"
    "v_mfma_f32_16x16x32_bf16 v[168:171], v[224:227], v[84:87], v[168:171]\n"
    "s_mov_b32 s65, 78848\n"
    "ds_read_b64_tr_b16 v[240:241], v4 offset:12288\n"
    "v_mfma_f32_16x16x32_bf16 v[172:175], v[224:227], v[88:91], v[172:175]\n"
    "ds_read_b64_tr_b16 v[242:243], v4 offset:14336\n"
    "v_add_u32 v4, 0x4000, v4\n"
    "v_mfma_f32_16x16x32_bf16 v[176:179], v[224:227], v[92:95], v[176:179]\n"
    "s_waitcnt lgkmcnt(6)\n"
    "v_mfma_f32_16x16x32_bf16 v[188:191], v[228:231], v[92:95], v[188:191]\n"
    "ds_read_b64_tr_b16 v[244:245], v5 offset:12288\n"
    "v_mfma_f32_16x16x32_bf16 v[184:187], v[228:231], v[88:91], v[184:187]\n"
    "ds_read_b64_tr_b16 v[246:247], v5 offset:14336\n"
    "v_add_u32 v5, 0x4000, v5\n"
    "v_mfma_f32_16x16x32_bf16 v[180:183], v[228:231], v[84:87], v[180:183]\n"
    "s_waitcnt lgkmcnt(6)\n"
    "buffer_store_dwordx4 v[152:155], v11, s[96:99], s65 offen sc1\n"
    "v_mfma_f32_16x16x32_bf16 v[168:171], v[232:235], v[96:99], v[168:171]\n"
    "ds_read_b64_tr_b16 v[216:217], v6 offset:0\n"
    "v_mfma_f32_16x16x32_bf16 v[172:175], v[232:235], v[100:103], v[172:175]\n"
    "ds_read_b64_tr_b16 v[218:219], v6 offset:2048\n"
    "v_mfma_f32_16x16x32_bf16 v[176:179], v[232:235], v[104:107], v[176:179]\n"
    "s_waitcnt lgkmcnt(6)\n"
    "s_waitcnt vmcnt(7)\n"
    "buffer_store_dwordx2 v[254:255], v10, s[28:31], 0 offen sc1\n"
    "v_mfma_f32_16x16x32_bf16 v[188:191], v[236:239], v[104:107], v[188:191]\n"
    "ds_read_b64_tr_b16 v[220:221], v7 offset:0\n"
    "v_mfma_f32_16x16x32_bf16 v[184:187], v[236:239], v[100:103], v[184:187]\n"
    "ds_read_b64_tr_b16 v[222:223], v7 offset:2048\n"
    "v_add_u32 v254, 1, v254\n"
    "v_mfma_f32_16x16x32_bf16 v[180:183], v[236:239], v[96:99], v[180:183]\n"
    "s_waitcnt lgkmcnt(6)\n"
    "v_mfma_f32_16x16x32_bf16 v[168:171], v[240:243], v[108:111], v[168:171]\n"
    "ds_read_b64_tr_b16 v[224:225], v6 offset:4096\n"
    "v_mfma_f32_16x16x32_bf16 v[172:175], v[240:243], v[112:115], v[172:175]\n"
    "ds_read_b64_tr_b16 v[226:227], v6 offset:6144\n"
    "v_mfma_f32_16x16x32_bf16 v[176:179], v[240:243], v[116:119], v[176:179]\n"
    "s_waitcnt lgkmcnt(6)\n"
    "v_mfma_f32_16x16x32_bf16 v[188:191], v[244:247], v[116:119], v[188:191]\n"
    "ds_read_b64_tr_b16 v[228:229], v7 offset:4096\n"
    "v_mfma_f32_16x16x32_bf16 v[184:187], v[244:247], v[112:115], v[184:187]\n"
    "ds_read_b64_tr_b16 v[230:231], v7 offset:6144\n"
    "v_mul_f32 v168, v12, v168\n"
    "v_mfma_f32_16x16x32_bf16 v[180:183], v[244:247], v[108:111], v[180:183]\n"
    "s_waitcnt lgkmcnt(6)\n"
    "v_mul_f32 v169, v12, v169\n"
    "v_mul_f32 v170, v12, v170\n"
    "v_mfma_f32_16x16x32_bf16 v[192:195], v[216:219], v[24:27], 0\n"
    "ds_read_b64_tr_b16 v[232:233], v6 offset:8192\n"
    "v_mul_f32 v171, v12, v171\n"
    "v_mfma_f32_16x16x32_bf16 v[196:199], v[216:219], v[28:31], 0\n"
    "ds_read_b64_tr_b16 v[234:235], v6 offset:10240\n"
    "v_cvt_pk_bf16_f32 v168, v168, v169\n"
    "v_mfma_f32_16x16x32_bf16 v[200:203], v[216:219], v[32:35], 0\n"
    "s_waitcnt lgkmcnt(6)\n"
    "v_cvt_pk_bf16_f32 v169, v170, v171\n"
    "v_mul_f32 v172, v13, v172\n"
    "v_mfma_f32_16x16x32_bf16 v[212:215], v[220:223], v[32:35], 0\n"
    "ds_read_b64_tr_b16 v[236:237], v7 offset:8192\n"
    "v_mul_f32 v180, v12, v180\n"
    "v_mfma_f32_16x16x32_bf16 v[208:211], v[220:223], v[28:31], 0\n"
    "ds_read_b64_tr_b16 v[238:239], v7 offset:10240\n"
    "v_mul_f32 v181, v12, v181\n"
    "v_mfma_f32_16x16x32_bf16 v[204:207], v[220:223], v[24:27], 0\n"
    "s_waitcnt lgkmcnt(6)\n"
    "v_mul_f32 v182, v12, v182\n"
    "v_mul_f32 v183, v12, v183\n"
    "v_mfma_f32_16x16x32_bf16 v[192:195], v[224:227], v[36:39], v[192:195]\n"
    "s_mov_b32 s64, 92160\n"
    "ds_read_b64_tr_b16 v[240:241], v6 offset:12288\n"
    "v_cvt_pk_bf16_f32 v170, v180, v181\n"
    "v_mfma_f32_16x16x32_bf16 v[196:199], v[224:227], v[40:43], v[196:199]\n"
    "ds_read_b64_tr_b16 v[242:243], v6 offset:14336\n"
    "v_add_u32 v6, 0x4000, v6\n"
    "v_mfma_f32_16x16x32_bf16 v[200:203], v[224:227], v[44:47], v[200:203]\n"
    "s_waitcnt lgkmcnt(6)\n"
    "v_cvt_pk_bf16_f32 v171, v182, v183\n"
    "v_mul_f32 v173, v13, v173\n"
    "v_mfma_f32_16x16x32_bf16 v[212:215], v[228:231], v[44:47], v[212:215]\n"
    "ds_read_b64_tr_b16 v[244:245], v7 offset:12288\n"
    "v_mul_f32 v174, v13, v174\n"
    "v_mfma_f32_16x16x32_bf16 v[208:211], v[228:231], v[40:43], v[208:211]\n"
    "ds_read_b64_tr_b16 v[246:247], v7 offset:14336\n"
    "v_add_u32 v7, 0x4000, v7\n"
    "v_mfma_f32_16x16x32_bf16 v[204:207], v[228:231], v[36:39], v[204:207]\n"
    "s_waitcnt lgkmcnt(6)\n"
    "buffer_store_dwordx4 v[168:171], v11, s[96:99], s64 offen sc1\n"
    "v_mul_f32 v175, v13, v175\n"
    "v_mfma_f32_16x16x32_bf16 v[192:195], v[232:235], v[48:51], v[192:195]\n"
    "ds_read_b64_tr_b16 v[216:217], v6 offset:0\n"
    "v_cvt_pk_bf16_f32 v172, v172, v173\n"
    "v_mfma_f32_16x16x32_bf16 v[196:199], v[232:235], v[52:55], v[196:199]\n"
    "ds_read_b64_tr_b16 v[218:219], v6 offset:2048\n"
    "v_cvt_pk_bf16_f32 v173, v174, v175\n"
    "v_mfma_f32_16x16x32_bf16 v[200:203], v[232:235], v[56:59], v[200:203]\n"
    "s_waitcnt lgkmcnt(6)\n"
    "v_mul_f32 v184, v13, v184\n"
    "v_mul_f32 v185, v13, v185\n"
    "v_mfma_f32_16x16x32_bf16 v[212:215], v[236:239], v[56:59], v[212:215]\n"
    "ds_read_b64_tr_b16 v[220:221], v7 offset:0\n"
    "v_mul_f32 v186, v13, v186\n"
    "v_mfma_f32_16x16x32_bf16 v[208:211], v[236:239], v[52:55], v[208:211]\n"
    "ds_read_b64_tr_b16 v[222:223], v7 offset:2048\n"
    "v_mul_f32 v187, v13, v187\n"
    "v_mfma_f32_16x16x32_bf16 v[204:207], v[236:239], v[48:51], v[204:207]\n"
    "s_waitcnt lgkmcnt(6)\n"
    "v_cvt_pk_bf16_f32 v174, v184, v185\n"
    "v_cvt_pk_bf16_f32 v175, v186, v187\n"
    "v_mfma_f32_16x16x32_bf16 v[192:195], v[240:243], v[60:63], v[192:195]\n"
    "s_mov_b32 s65, 94208\n"
    "ds_read_b64_tr_b16 v[224:225], v6 offset:4096\n"
    "v_mul_f32 v176, v14, v176\n"
    "v_mfma_f32_16x16x32_bf16 v[196:199], v[240:243], v[64:67], v[196:199]\n"
    "ds_read_b64_tr_b16 v[226:227], v6 offset:6144\n"
    "v_mul_f32 v177, v14, v177\n"
    "v_mfma_f32_16x16x32_bf16 v[200:203], v[240:243], v[68:71], v[200:203]\n"
    "s_waitcnt lgkmcnt(6)\n"
    "v_mul_f32 v178, v14, v178\n"
    "v_mul_f32 v179, v14, v179\n"
    "v_mfma_f32_16x16x32_bf16 v[212:215], v[244:247], v[68:71], v[212:215]\n"
    "ds_read_b64_tr_b16 v[228:229], v7 offset:4096\n"
    "v_cvt_pk_bf16_f32 v176, v176, v177\n"
    "v_mfma_f32_16x16x32_bf16 v[208:211], v[244:247], v[64:67], v[208:211]\n"
    "ds_read_b64_tr_b16 v[230:231], v7 offset:6144\n"
    "v_cvt_pk_bf16_f32 v177, v178, v179\n"
    "v_mfma_f32_16x16x32_bf16 v[204:207], v[244:247], v[60:63], v[204:207]\n"
    "s_waitcnt lgkmcnt(6)\n"
    "buffer_store_dwordx4 v[172:175], v11, s[96:99], s65 offen sc1\n"
    "v_mul_f32 v188, v14, v188\n"
    "v_mfma_f32_16x16x32_bf16 v[192:195], v[216:219], v[72:75], v[192:195]\n"
    "ds_read_b64_tr_b16 v[232:233], v6 offset:8192\n"
    "v_mul_f32 v189, v14, v189\n"
    "v_mfma_f32_16x16x32_bf16 v[196:199], v[216:219], v[76:79], v[196:199]\n"
    "ds_read_b64_tr_b16 v[234:235], v6 offset:10240\n"
    "v_mul_f32 v190, v14, v190\n"
    "v_mfma_f32_16x16x32_bf16 v[200:203], v[216:219], v[80:83], v[200:203]\n"
    "s_waitcnt lgkmcnt(6)\n"
    "v_mul_f32 v191, v14, v191\n"
    "v_cvt_pk_bf16_f32 v178, v188, v189\n"
    "v_mfma_f32_16x16x32_bf16 v[212:215], v[220:223], v[80:83], v[212:215]\n"
    "ds_read_b64_tr_b16 v[236:237], v7 offset:8192\n"
    "v_cvt_pk_bf16_f32 v179, v190, v191\n"
    "v_mfma_f32_16x16x32_bf16 v[208:211], v[220:223], v[76:79], v[208:211]\n"
    "ds_read_b64_tr_b16 v[238:239], v7 offset:10240\n"
    "v_mfma_f32_16x16x32_bf16 v[204:207], v[220:223], v[72:75], v[204:207]\n"
    "s_waitcnt lgkmcnt(6)\n"
    "v_mfma_f32_16x16x32_bf16 v[192:195], v[224:227], v[84:87], v[192:195]\n"
    "s_mov_b32 s64, 96256\n"
    "ds_read_b64_tr_b16 v[240:241], v6 offset:12288\n"
    "v_mfma_f32_16x16x32_bf16 v[196:199], v[224:227], v[88:91], v[196:199]\n"
    "ds_read_b64_tr_b16 v[242:243], v6 offset:14336\n"
    "v_add_u32 v6, 0x4000, v6\n"
    "v_mfma_f32_16x16x32_bf16 v[200:203], v[224:227], v[92:95], v[200:203]\n"
    "s_waitcnt lgkmcnt(6)\n"
    "v_mfma_f32_16x16x32_bf16 v[212:215], v[228:231], v[92:95], v[212:215]\n"
    "ds_read_b64_tr_b16 v[244:245], v7 offset:12288\n"
    "v_mfma_f32_16x16x32_bf16 v[208:211], v[228:231], v[88:91], v[208:211]\n"
    "ds_read_b64_tr_b16 v[246:247], v7 offset:14336\n"
    "v_add_u32 v7, 0x4000, v7\n"
    "v_mfma_f32_16x16x32_bf16 v[204:207], v[228:231], v[84:87], v[204:207]\n"
    "s_waitcnt lgkmcnt(6)\n"
    "buffer_store_dwordx4 v[176:179], v11, s[96:99], s64 offen sc1\n"
    "v_mfma_f32_16x16x32_bf16 v[192:195], v[232:235], v[96:99], v[192:195]\n"
    "ds_read_b64_tr_b16 v[216:217], v4 offset:0\n"
    "v_mfma_f32_16x16x32_bf16 v[196:199], v[232:235], v[100:103], v[196:199]\n"
    "ds_read_b64_tr_b16 v[218:219], v4 offset:2048\n"
    "v_mfma_f32_16x16x32_bf16 v[200:203], v[232:235], v[104:107], v[200:203]\n"
    "s_waitcnt lgkmcnt(6)\n"
    "v_mfma_f32_16x16x32_bf16 v[212:215], v[236:239], v[104:107], v[212:215]\n"
    "ds_read_b64_tr_b16 v[220:221], v5 offset:0\n"
    "v_mfma_f32_16x16x32_bf16 v[208:211], v[236:239], v[100:103], v[208:211]\n"
    "ds_read_b64_tr_b16 v[222:223], v5 offset:2048\n"
    "v_mfma_f32_16x16x32_bf16 v[204:207], v[236:239], v[96:99], v[204:207]\n"
    "s_waitcnt lgkmcnt(6)\n"
    "v_mfma_f32_16x16x32_bf16 v[192:195], v[240:243], v[108:111], v[192:195]\n"
    "ds_read_b64_tr_b16 v[224:225], v4 offset:4096\n"
    "v_mfma_f32_16x16x32_bf16 v[196:199], v[240:243], v[112:115], v[196:199]\n"
    "ds_read_b64_tr_b16 v[226:227], v4 offset:6144\n"
    "v_mfma_f32_16x16x32_bf16 v[200:203], v[240:243], v[116:119], v[200:203]\n"
    "s_waitcnt lgkmcnt(6)\n"
    "v_mfma_f32_16x16x32_bf16 v[212:215], v[244:247], v[116:119], v[212:215]\n"
    "ds_read_b64_tr_b16 v[228:229], v5 offset:4096\n"
    "v_mfma_f32_16x16x32_bf16 v[208:211], v[244:247], v[112:115], v[208:211]\n"
    "ds_read_b64_tr_b16 v[230:231], v5 offset:6144\n"
    "v_mul_f32 v192, v12, v192\n"
    "v_mfma_f32_16x16x32_bf16 v[204:207], v[244:247], v[108:111], v[204:207]\n"
    "s_waitcnt lgkmcnt(6)\n"
    "v_mul_f32 v193, v12, v193\n"
    "v_mul_f32 v194, v12, v194\n"
    "v_mfma_f32_16x16x32_bf16 v[120:123], v[216:219], v[24:27], 0\n"
    "ds_read_b64_tr_b16 v[232:233], v4 offset:8192\n"
    "v_mul_f32 v195, v12, v195\n"
    "v_mfma_f32_16x16x32_bf16 v[124:127], v[216:219], v[28:31], 0\n"
    "ds_read_b64_tr_b16 v[234:235], v4 offset:10240\n"
    "v_cvt_pk_bf16_f32 v192, v192, v193\n"
    "v_mfma_f32_16x16x32_bf16 v[128:131], v[216:219], v[32:35], 0\n"
    "s_waitcnt lgkmcnt(6)\n"
    "v_cvt_pk_bf16_f32 v193, v194, v195\n"
    "v_mul_f32 v196, v13, v196\n"
    "v_mfma_f32_16x16x32_bf16 v[140:143], v[220:223], v[32:35], 0\n"
    "ds_read_b64_tr_b16 v[236:237], v5 offset:8192\n"
    "v_mul_f32 v204, v12, v204\n"
    "v_mfma_f32_16x16x32_bf16 v[136:139], v[220:223], v[28:31], 0\n"
    "ds_read_b64_tr_b16 v[238:239], v5 offset:10240\n"
    "v_mul_f32 v205, v12, v205\n"
    "v_mfma_f32_16x16x32_bf16 v[132:135], v[220:223], v[24:27], 0\n"
    "s_waitcnt lgkmcnt(6)\n"
    "v_mul_f32 v206, v12, v206\n"
    "v_mul_f32 v207, v12, v207\n"
    "v_mfma_f32_16x16x32_bf16 v[120:123], v[224:227], v[36:39], v[120:123]\n"
    "s_mov_b32 s65, 93184\n"
    "ds_read_b64_tr_b16 v[240:241], v4 offset:12288\n"
    "v_cvt_pk_bf16_f32 v194, v204, v205\n"
    "v_mfma_f32_16x16x32_bf16 v[124:127], v[224:227], v[40:43], v[124:127]\n"
    "ds_read_b64_tr_b16 v[242:243], v4 offset:14336\n"
    "v_add_u32 v4, 0x4000, v4\n"
    "v_mfma_f32_16x16x32_bf16 v[128:131], v[224:227], v[44:47], v[128:131]\n"
    "s_waitcnt lgkmcnt(6)\n"
    "v_cvt_pk_bf16_f32 v195, v206, v207\n"
    "v_mul_f32 v197, v13, v197\n"
    "v_mfma_f32_16x16x32_bf16 v[140:143], v[228:231], v[44:47], v[140:143]\n"
    "ds_read_b64_tr_b16 v[244:245], v5 offset:12288\n"
    "v_mul_f32 v198, v13, v198\n"
    "v_mfma_f32_16x16x32_bf16 v[136:139], v[228:231], v[40:43], v[136:139]\n"
    "ds_read_b64_tr_b16 v[246:247], v5 offset:14336\n"
    "v_add_u32 v5, 0x4000, v5\n"
    "v_mfma_f32_16x16x32_bf16 v[132:135], v[228:231], v[36:39], v[132:135]\n"
    "s_waitcnt lgkmcnt(6)\n"
    "buffer_store_dwordx4 v[192:195], v11, s[96:99], s65 offen sc1\n"
    "v_mul_f32 v199, v13, v199\n"
    "v_mfma_f32_16x16x32_bf16 v[120:123], v[232:235], v[48:51], v[120:123]\n"
    "ds_read_b64_tr_b16 v[216:217], v4 offset:0\n"
    "v_cvt_pk_bf16_f32 v196, v196, v197\n"
    "v_mfma_f32_16x16x32_bf16 v[124:127], v[232:235], v[52:55], v[124:127]\n"
    "ds_read_b64_tr_b16 v[218:219], v4 offset:2048\n"
    "v_cvt_pk_bf16_f32 v197, v198, v199\n"
    "v_mfma_f32_16x16x32_bf16 v[128:131], v[232:235], v[56:59], v[128:131]\n"
    "s_waitcnt lgkmcnt(6)\n"
    "v_mul_f32 v208, v13, v208\n"
    "v_mul_f32 v209, v13, v209\n"
    "v_mfma_f32_16x16x32_bf16 v[140:143], v[236:239], v[56:59], v[140:143]\n"
    "ds_read_b64_tr_b16 v[220:221], v5 offset:0\n"
    "v_mul_f32 v210, v13, v210\n"
    "v_mfma_f32_16x16x32_bf16 v[136:139], v[236:239], v[52:55], v[136:139]\n"
    "ds_read_b64_tr_b16 v[222:223], v5 offset:2048\n"
    "v_mul_f32 v211, v13, v211\n"
    "v_mfma_f32_16x16x32_bf16 v[132:135], v[236:239], v[48:51], v[132:135]\n"
    "s_waitcnt lgkmcnt(6)\n"
    "v_cvt_pk_bf16_f32 v198, v208, v209\n"
    "v_cvt_pk_bf16_f32 v199, v210, v211\n"
    "v_mfma_f32_16x16x32_bf16 v[120:123], v[240:243], v[60:63], v[120:123]\n"
    "s_mov_b32 s64, 95232\n"
    "ds_read_b64_tr_b16 v[224:225], v4 offset:4096\n"
    "v_mul_f32 v200, v14, v200\n"
    "v_mfma_f32_16x16x32_bf16 v[124:127], v[240:243], v[64:67], v[124:127]\n"
    "ds_read_b64_tr_b16 v[226:227], v4 offset:6144\n"
    "v_mul_f32 v201, v14, v201\n"
    "v_mfma_f32_16x16x32_bf16 v[128:131], v[240:243], v[68:71], v[128:131]\n"
    "s_waitcnt lgkmcnt(6)\n"
    "v_mul_f32 v202, v14, v202\n"
    "v_mul_f32 v203, v14, v203\n"
    "v_mfma_f32_16x16x32_bf16 v[140:143], v[244:247], v[68:71], v[140:143]\n"
    "ds_read_b64_tr_b16 v[228:229], v5 offset:4096\n"
    "v_cvt_pk_bf16_f32 v200, v200, v201\n"
    "v_mfma_f32_16x16x32_bf16 v[136:139], v[244:247], v[64:67], v[136:139]\n"
    "ds_read_b64_tr_b16 v[230:231], v5 offset:6144\n"
    "v_cvt_pk_bf16_f32 v201, v202, v203\n"
    "v_mfma_f32_16x16x32_bf16 v[132:135], v[244:247], v[60:63], v[132:135]\n"
    "s_waitcnt lgkmcnt(6)\n"
    "buffer_store_dwordx4 v[196:199], v11, s[96:99], s64 offen sc1\n"
    "v_mul_f32 v212, v14, v212\n"
    "v_mfma_f32_16x16x32_bf16 v[120:123], v[216:219], v[72:75], v[120:123]\n"
    "ds_read_b64_tr_b16 v[232:233], v4 offset:8192\n"
    "v_mul_f32 v213, v14, v213\n"
    "v_mfma_f32_16x16x32_bf16 v[124:127], v[216:219], v[76:79], v[124:127]\n"
    "ds_read_b64_tr_b16 v[234:235], v4 offset:10240\n"
    "v_mul_f32 v214, v14, v214\n"
    "v_mfma_f32_16x16x32_bf16 v[128:131], v[216:219], v[80:83], v[128:131]\n"
    "s_waitcnt lgkmcnt(6)\n"
    "v_mul_f32 v215, v14, v215\n"
    "v_cvt_pk_bf16_f32 v202, v212, v213\n"
    "v_mfma_f32_16x16x32_bf16 v[140:143], v[220:223], v[80:83], v[140:143]\n"
    "ds_read_b64_tr_b16 v[236:237], v5 offset:8192\n"
    "v_cvt_pk_bf16_f32 v203, v214, v215\n"
    "v_mfma_f32_16x16x32_bf16 v[136:139], v[220:223], v[76:79], v[136:139]\n"
    "ds_read_b64_tr_b16 v[238:239], v5 offset:10240\n"
    "v_mfma_f32_16x16x32_bf16 v[132:135], v[220:223], v[72:75], v[132:135]\n"
    "s_waitcnt lgkmcnt(6)\n"
    "v_mfma_f32_16x16x32_bf16 v[120:123], v[224:227], v[84:87], v[120:123]\n"
    "s_mov_b32 s65, 97280\n"
    "ds_read_b64_tr_b16 v[240:241], v4 offset:12288\n"
    "v_mfma_f32_16x16x32_bf16 v[124:127], v[224:227], v[88:91], v[124:127]\n"
    "ds_read_b64_tr_b16 v[242:243], v4 offset:14336\n"
    "v_mfma_f32_16x16x32_bf16 v[128:131], v[224:227], v[92:95], v[128:131]\n"
    "s_waitcnt lgkmcnt(6)\n"
    "v_mfma_f32_16x16x32_bf16 v[140:143], v[228:231], v[92:95], v[140:143]\n"
    "ds_read_b64_tr_b16 v[244:245], v5 offset:12288\n"
    "v_mfma_f32_16x16x32_bf16 v[136:139], v[228:231], v[88:91], v[136:139]\n"
    "ds_read_b64_tr_b16 v[246:247], v5 offset:14336\n"
    "v_mfma_f32_16x16x32_bf16 v[132:135], v[228:231], v[84:87], v[132:135]\n"
    "s_waitcnt lgkmcnt(6)\n"
    "buffer_store_dwordx4 v[200:203], v11, s[96:99], s65 offen sc1\n"
    "v_mfma_f32_16x16x32_bf16 v[120:123], v[232:235], v[96:99], v[120:123]\n"
    "ds_read_b64_tr_b16 v[216:217], v6 offset:0\n"
    "v_mfma_f32_16x16x32_bf16 v[124:127], v[232:235], v[100:103], v[124:127]\n"
    "ds_read_b64_tr_b16 v[218:219], v6 offset:2048\n"
    "v_mfma_f32_16x16x32_bf16 v[128:131], v[232:235], v[104:107], v[128:131]\n"
    "s_waitcnt lgkmcnt(6)\n"
    "s_waitcnt vmcnt(7)\n"
    "buffer_store_dwordx2 v[254:255], v10, s[28:31], 0 offen sc1\n"
    "v_mfma_f32_16x16x32_bf16 v[140:143], v[236:239], v[104:107], v[140:143]\n"
    "ds_read_b64_tr_b16 v[220:221], v7 offset:0\n"
    "v_mfma_f32_16x16x32_bf16 v[136:139], v[236:239], v[100:103], v[136:139]\n"
    "ds_read_b64_tr_b16 v[222:223], v7 offset:2048\n"
    "v_mfma_f32_16x16x32_bf16 v[132:135], v[236:239], v[96:99], v[132:135]\n"
    "s_waitcnt lgkmcnt(6)\n"
    "v_mfma_f32_16x16x32_bf16 v[120:123], v[240:243], v[108:111], v[120:123]\n"
    "ds_read_b64_tr_b16 v[224:225], v6 offset:4096\n"
    "v_mfma_f32_16x16x32_bf16 v[124:127], v[240:243], v[112:115], v[124:127]\n"
    "ds_read_b64_tr_b16 v[226:227], v6 offset:6144\n"
    "v_mfma_f32_16x16x32_bf16 v[128:131], v[240:243], v[116:119], v[128:131]\n"
    "s_waitcnt lgkmcnt(6)\n"
    "v_mfma_f32_16x16x32_bf16 v[140:143], v[244:247], v[116:119], v[140:143]\n"
    "ds_read_b64_tr_b16 v[228:229], v7 offset:4096\n"
    "v_mfma_f32_16x16x32_bf16 v[136:139], v[244:247], v[112:115], v[136:139]\n"
    "ds_read_b64_tr_b16 v[230:231], v7 offset:6144\n"
    "v_mul_f32 v120, v12, v120\n"
    "v_mfma_f32_16x16x32_bf16 v[132:135], v[244:247], v[108:111], v[132:135]\n"
    "s_waitcnt lgkmcnt(6)\n"
    "v_mul_f32 v121, v12, v121\n"
    "v_mul_f32 v122, v12, v122\n"
    "v_mfma_f32_16x16x32_bf16 v[144:147], v[216:219], v[24:27], 0\n"
    "ds_read_b64_tr_b16 v[232:233], v6 offset:8192\n"
    "v_mul_f32 v123, v12, v123\n"
    "v_mfma_f32_16x16x32_bf16 v[148:151], v[216:219], v[28:31], 0\n"
    "ds_read_b64_tr_b16 v[234:235], v6 offset:10240\n"
    "v_cvt_pk_bf16_f32 v120, v120, v121\n"
    "v_mfma_f32_16x16x32_bf16 v[152:155], v[216:219], v[32:35], 0\n"
    "s_waitcnt lgkmcnt(6)\n"
    "v_cvt_pk_bf16_f32 v121, v122, v123\n"
    "v_mul_f32 v124, v13, v124\n"
    "v_mfma_f32_16x16x32_bf16 v[164:167], v[220:223], v[32:35], 0\n"
    "ds_read_b64_tr_b16 v[236:237], v7 offset:8192\n"
    "v_mul_f32 v132, v12, v132\n"
    "v_mfma_f32_16x16x32_bf16 v[160:163], v[220:223], v[28:31], 0\n"
    "ds_read_b64_tr_b16 v[238:239], v7 offset:10240\n"
    "v_mul_f32 v133, v12, v133\n"
    "v_mfma_f32_16x16x32_bf16 v[156:159], v[220:223], v[24:27], 0\n"
    "s_waitcnt lgkmcnt(6)\n"
    "v_mul_f32 v134, v12, v134\n"
    "v_mul_f32 v135, v12, v135\n"
    "v_mfma_f32_16x16x32_bf16 v[144:147], v[224:227], v[36:39], v[144:147]\n"
    "s_mov_b32 s64, 110592\n"
    "ds_read_b64_tr_b16 v[240:241], v6 offset:12288\n"
    "v_cvt_pk_bf16_f32 v122, v132, v133\n"
    "v_mfma_f32_16x16x32_bf16 v[148:151], v[224:227], v[40:43], v[148:151]\n"
    "ds_read_b64_tr_b16 v[242:243], v6 offset:14336\n"
    "v_add_u32 v6, 0x4000, v6\n"
    "v_mfma_f32_16x16x32_bf16 v[152:155], v[224:227], v[44:47], v[152:155]\n"
    "s_waitcnt lgkmcnt(6)\n"
    "v_cvt_pk_bf16_f32 v123, v134, v135\n"
    "v_mul_f32 v125, v13, v125\n"
    "v_mfma_f32_16x16x32_bf16 v[164:167], v[228:231], v[44:47], v[164:167]\n"
    "ds_read_b64_tr_b16 v[244:245], v7 offset:12288\n"
    "v_mul_f32 v126, v13, v126\n"
    "v_mfma_f32_16x16x32_bf16 v[160:163], v[228:231], v[40:43], v[160:163]\n"
    "ds_read_b64_tr_b16 v[246:247], v7 offset:14336\n"
    "v_add_u32 v7, 0x4000, v7\n"
    "v_mfma_f32_16x16x32_bf16 v[156:159], v[228:231], v[36:39], v[156:159]\n"
    "s_waitcnt lgkmcnt(6)\n"
    "buffer_store_dwordx4 v[120:123], v11, s[96:99], s64 offen sc1\n"
    "v_mul_f32 v127, v13, v127\n"
    "v_mfma_f32_16x16x32_bf16 v[144:147], v[232:235], v[48:51], v[144:147]\n"
    "ds_read_b64_tr_b16 v[216:217], v6 offset:0\n"
    "v_cvt_pk_bf16_f32 v124, v124, v125\n"
    "v_mfma_f32_16x16x32_bf16 v[148:151], v[232:235], v[52:55], v[148:151]\n"
    "ds_read_b64_tr_b16 v[218:219], v6 offset:2048\n"
    "v_cvt_pk_bf16_f32 v125, v126, v127\n"
    "v_mfma_f32_16x16x32_bf16 v[152:155], v[232:235], v[56:59], v[152:155]\n"
    "s_waitcnt lgkmcnt(6)\n"
    "v_mul_f32 v136, v13, v136\n"
    "v_mul_f32 v137, v13, v137\n"
    "v_mfma_f32_16x16x32_bf16 v[164:167], v[236:239], v[56:59], v[164:167]\n"
    "ds_read_b64_tr_b16 v[220:221], v7 offset:0\n"
    "v_mul_f32 v138, v13, v138\n"
    "v_mfma_f32_16x16x32_bf16 v[160:163], v[236:239], v[52:55], v[160:163]\n"
    "ds_read_b64_tr_b16 v[222:223], v7 offset:2048\n"
    "v_mul_f32 v139, v13, v139\n"
    "v_mfma_f32_16x16x32_bf16 v[156:159], v[236:239], v[48:51], v[156:159]\n"
    "s_waitcnt lgkmcnt(6)\n"
    "v_cvt_pk_bf16_f32 v126, v136, v137\n"
    "v_cvt_pk_bf16_f32 v127, v138, v139\n"
    "v_mfma_f32_16x16x32_bf16 v[144:147], v[240:243], v[60:63], v[144:147]\n"
    "s_mov_b32 s65, 112640\n"
    "ds_read_b64_tr_b16 v[224:225], v6 offset:4096\n"
    "v_mul_f32 v128, v14, v128\n"
    "v_mfma_f32_16x16x32_bf16 v[148:151], v[240:243], v[64:67], v[148:151]\n"
    "ds_read_b64_tr_b16 v[226:227], v6 offset:6144\n"
    "v_mul_f32 v129, v14, v129\n"
    "v_mfma_f32_16x16x32_bf16 v[152:155], v[240:243], v[68:71], v[152:155]\n"
    "s_waitcnt lgkmcnt(6)\n"
    "v_mul_f32 v130, v14, v130\n"
    "v_mul_f32 v131, v14, v131\n"
    "v_mfma_f32_16x16x32_bf16 v[164:167], v[244:247], v[68:71], v[164:167]\n"
    "ds_read_b64_tr_b16 v[228:229], v7 offset:4096\n"
    "v_cvt_pk_bf16_f32 v128, v128, v129\n"
    "v_mfma_f32_16x16x32_bf16 v[160:163], v[244:247], v[64:67], v[160:163]\n"
    "ds_read_b64_tr_b16 v[230:231], v7 offset:6144\n"
    "v_cvt_pk_bf16_f32 v129, v130, v131\n"
    "v_mfma_f32_16x16x32_bf16 v[156:159], v[244:247], v[60:63], v[156:159]\n"
    "s_waitcnt lgkmcnt(6)\n"
    "buffer_store_dwordx4 v[124:127], v11, s[96:99], s65 offen sc1\n"
    "v_mul_f32 v140, v14, v140\n"
    "v_mfma_f32_16x16x32_bf16 v[144:147], v[216:219], v[72:75], v[144:147]\n"
    "ds_read_b64_tr_b16 v[232:233], v6 offset:8192\n"
    "v_mul_f32 v141, v14, v141\n"
    "v_mfma_f32_16x16x32_bf16 v[148:151], v[216:219], v[76:79], v[148:151]\n"
    "ds_read_b64_tr_b16 v[234:235], v6 offset:10240\n"
    "v_mul_f32 v142, v14, v142\n"
    "v_mfma_f32_16x16x32_bf16 v[152:155], v[216:219], v[80:83], v[152:155]\n"
    "s_waitcnt lgkmcnt(6)\n"
    "v_mul_f32 v143, v14, v143\n"
    "v_cvt_pk_bf16_f32 v130, v140, v141\n"
    "v_mfma_f32_16x16x32_bf16 v[164:167], v[220:223], v[80:83], v[164:167]\n"
    "ds_read_b64_tr_b16 v[236:237], v7 offset:8192\n"
    "v_cvt_pk_bf16_f32 v131, v142, v143\n"
    "v_mfma_f32_16x16x32_bf16 v[160:163], v[220:223], v[76:79], v[160:163]\n"
    "ds_read_b64_tr_b16 v[238:239], v7 offset:10240\n"
    "v_mfma_f32_16x16x32_bf16 v[156:159], v[220:223], v[72:75], v[156:159]\n"
    "s_waitcnt lgkmcnt(6)\n"
    "v_mfma_f32_16x16x32_bf16 v[144:147], v[224:227], v[84:87], v[144:147]\n"
    "s_mov_b32 s64, 114688\n"
    "ds_read_b64_tr_b16 v[240:241], v6 offset:12288\n"
    "v_mfma_f32_16x16x32_bf16 v[148:151], v[224:227], v[88:91], v[148:151]\n"
    "ds_read_b64_tr_b16 v[242:243], v6 offset:14336\n"
    "v_mfma_f32_16x16x32_bf16 v[152:155], v[224:227], v[92:95], v[152:155]\n"
    "s_waitcnt lgkmcnt(6)\n"
    "v_mfma_f32_16x16x32_bf16 v[164:167], v[228:231], v[92:95], v[164:167]\n"
    "ds_read_b64_tr_b16 v[244:245], v7 offset:12288\n"
    "v_mfma_f32_16x16x32_bf16 v[160:163], v[228:231], v[88:91], v[160:163]\n"
    "ds_read_b64_tr_b16 v[246:247], v7 offset:14336\n"
    "v_mfma_f32_16x16x32_bf16 v[156:159], v[228:231], v[84:87], v[156:159]\n"
    "s_waitcnt lgkmcnt(6)\n"
    "buffer_store_dwordx4 v[128:131], v11, s[96:99], s64 offen sc1\n"
    "v_mfma_f32_16x16x32_bf16 v[144:147], v[232:235], v[96:99], v[144:147]\n"
    "v_mfma_f32_16x16x32_bf16 v[148:151], v[232:235], v[100:103], v[148:151]\n"
    "v_mfma_f32_16x16x32_bf16 v[152:155], v[232:235], v[104:107], v[152:155]\n"
    "s_waitcnt lgkmcnt(4)\n"
    "v_mfma_f32_16x16x32_bf16 v[164:167], v[236:239], v[104:107], v[164:167]\n"
    "v_mfma_f32_16x16x32_bf16 v[160:163], v[236:239], v[100:103], v[160:163]\n"
    "v_mfma_f32_16x16x32_bf16 v[156:159], v[236:239], v[96:99], v[156:159]\n"
    "s_waitcnt lgkmcnt(2)\n"
    "v_mfma_f32_16x16x32_bf16 v[144:147], v[240:243], v[108:111], v[144:147]\n"
    "v_mfma_f32_16x16x32_bf16 v[148:151], v[240:243], v[112:115], v[148:151]\n"
    "v_mfma_f32_16x16x32_bf16 v[152:155], v[240:243], v[116:119], v[152:155]\n"
    "s_waitcnt lgkmcnt(0)\n"
    "v_mfma_f32_16x16x32_bf16 v[164:167], v[244:247], v[116:119], v[164:167]\n"
    "v_mfma_f32_16x16x32_bf16 v[160:163], v[244:247], v[112:115], v[160:163]\n"
    "s_nop 2\n"
    "v_mul_f32 v144, v12, v144\n"
    "v_mul_f32 v145, v12, v145\n"
    "v_mfma_f32_16x16x32_bf16 v[156:159], v[244:247], v[108:111], v[156:159]\n"
    "v_mul_f32 v146, v12, v146\n"
    "v_mul_f32 v147, v12, v147\n"
    "v_cvt_pk_bf16_f32 v144, v144, v145\n"
    "v_cvt_pk_bf16_f32 v145, v146, v147\n"
    "s_nop 3\n"
    "v_mul_f32 v156, v12, v156\n"
    "v_mul_f32 v157, v12, v157\n"
    "v_mul_f32 v158, v12, v158\n"
    "v_mul_f32 v159, v12, v159\n"
    "v_cvt_pk_bf16_f32 v146, v156, v157\n"
    "v_cvt_pk_bf16_f32 v147, v158, v159\n"
    "s_mov_b32 s65, 111616\n"
    "buffer_store_dwordx4 v[144:147], v11, s[96:99], s65 offen sc1\n"
    "v_mul_f32 v148, v13, v148\n"
    "v_mul_f32 v149, v13, v149\n"
    "v_mul_f32 v150, v13, v150\n"
    "v_mul_f32 v151, v13, v151\n"
    "v_cvt_pk_bf16_f32 v148, v148, v149\n"
    "v_cvt_pk_bf16_f32 v149, v150, v151\n"
    "v_mul_f32 v160, v13, v160\n"
    "v_mul_f32 v161, v13, v161\n"
    "v_mul_f32 v162, v13, v162\n"
    "v_mul_f32 v163, v13, v163\n"
    "v_cvt_pk_bf16_f32 v150, v160, v161\n"
    "v_cvt_pk_bf16_f32 v151, v162, v163\n"
    "s_mov_b32 s64, 113664\n"
    "buffer_store_dwordx4 v[148:151], v11, s[96:99], s64 offen sc1\n"
    "v_mul_f32 v152, v14, v152\n"
    "v_mul_f32 v153, v14, v153\n"
    "v_mul_f32 v154, v14, v154\n"
    "v_mul_f32 v155, v14, v155\n"
    "v_cvt_pk_bf16_f32 v152, v152, v153\n"
    "v_cvt_pk_bf16_f32 v153, v154, v155\n"
    "v_mul_f32 v164, v14, v164\n"
    "v_mul_f32 v165, v14, v165\n"
    "v_mul_f32 v166, v14, v166\n"
    "v_mul_f32 v167, v14, v167\n"
    "v_cvt_pk_bf16_f32 v154, v164, v165\n"
    "v_cvt_pk_bf16_f32 v155, v166, v167\n"
    "s_mov_b32 s65, 115712\n"
    "buffer_store_dwordx4 v[152:155], v11, s[96:99], s65 offen sc1\n"
    "v_add_u32 v254, 1, v254\n"
    "s_waitcnt vmcnt(7)\n"
    "buffer_store_dwordx2 v[254:255], v10, s[28:31], 0 offen sc1\n"
    "s_nop 1\n"
    "v_add_u32 v254, 1, v254\n"
    "s_waitcnt vmcnt(1)\n"
    "buffer_store_dwordx2 v[254:255], v10, s[28:31], 0 offen sc1\n"
    "s_nop 1\n"
    "s_branch END_%=\n"
    "TRAMP_P1_%=:\n"
    "s_cselect_b32 s64, 1, 0\n"
    "s_mov_b32 s62, 0\n"
    "s_branch RARE1_%=\n"
    "RET_P1_%=:\n"
    "s_cmp_lg_u32 s64, 0\n"
    "s_branch CONT_P1_%=\n"
    "TRAMP_L0_%=:\n"
    "s_cselect_b32 s64, 1, 0\n"
    "s_mov_b32 s62, 1\n"
    "s_branch RARE0_%=\n"
    "RET_L0_%=:\n"
    "s_cmp_lg_u32 s64, 0\n"
    "s_branch CONT_L0_%=\n"
    "TRAMP_L1_%=:\n"
    "s_cselect_b32 s64, 1, 0\n"
    "s_mov_b32 s62, 2\n"
    "s_branch RARE1_%=\n"
    "RET_L1_%=:\n"
    "s_cmp_lg_u32 s64, 0\n"
    "s_branch CONT_L1_%=\n"
    "TRAMP_P30_%=:\n"
    "s_cselect_b32 s64, 1, 0\n"
    "s_mov_b32 s62, 3\n"
    "s_branch RARE0_%=\n"
    "RET_P30_%=:\n"
    "s_cmp_lg_u32 s64, 0\n"
    "s_branch CONT_P30_%=\n"
    "TRAMP_P31_%=:\n"
    "s_cselect_b32 s64, 1, 0\n"
    "s_mov_b32 s62, 4\n"
    "s_branch RARE1_%=\n"
    "RET_P31_%=:\n"
    "s_cmp_lg_u32 s64, 0\n"
    "s_branch CONT_P31_%=\n"
    "RARE0_%=:\n"
    "s_nop 7\n"
    "s_nop 7\n"
    "v_max3_f32 v12, v120, v121, v122\n"
    "v_max3_f32 v12, v12, v123, v132\n"
    "v_max3_f32 v12, v12, v133, v134\n"
    "v_max_f32 v12, v12, v135\n"
    "v_max3_f32 v13, v124, v125, v126\n"
    "v_max3_f32 v13, v13, v127, v136\n"
    "v_max3_f32 v13, v13, v137, v138\n"
    "v_max_f32 v13, v13, v139\n"
    "v_max3_f32 v14, v128, v129, v130\n"
    "v_max3_f32 v14, v14, v131, v140\n"
    "v_max3_f32 v14, v14, v141, v142\n"
    "v_max_f32 v14, v14, v143\n"
    "v_mov_b32 v15, v12\n"
    "v_mov_b32 v16, v13\n"
    "v_mov_b32 v17, v14\n"
    "s_nop 1\n"
    "v_permlane32_swap_b32 v12, v15\n"
    "v_permlane32_swap_b32 v13, v16\n"
    "v_permlane32_swap_b32 v14, v17\n"
    "s_nop 1\n"
    "v_max_f32 v12, v12, v15\n"
    "v_max_f32 v13, v13, v16\n"
    "v_max_f32 v14, v14, v17\n"
    "v_mov_b32 v15, v12\n"
    "v_mov_b32 v16, v13\n"
    "v_mov_b32 v17, v14\n"
    "s_nop 1\n"
    "v_permlane16_swap_b32 v12, v15\n"
    "v_permlane16_swap_b32 v13, v16\n"
    "v_permlane16_swap_b32 v14, v17\n"
    "s_nop 1\n"
    "v_max_f32 v12, v12, v15\n"
    "v_max_f32 v13, v13, v16\n"
    "v_max_f32 v14, v14, v17\n"
    "v_max_f32 v12, 0, v12\n"
    "v_max_f32 v13, 0, v13\n"
    "v_max_f32 v14, 0, v14\n"
    "v_sub_f32 v204, v204, v12\n"
    "v_sub_f32 v205, v205, v12\n"
    "v_sub_f32 v206, v206, v12\n"
    "v_sub_f32 v207, v207, v12\n"
    "v_sub_f32 v120, v120, v12\n"
    "v_sub_f32 v121, v121, v12\n"
    "v_sub_f32 v122, v122, v12\n"
    "v_sub_f32 v123, v123, v12\n"
    "v_sub_f32 v132, v132, v12\n"
    "v_sub_f32 v133, v133, v12\n"
    "v_sub_f32 v134, v134, v12\n"
    "v_sub_f32 v135, v135, v12\n"
    "v_sub_f32 v208, v208, v13\n"
    "v_sub_f32 v209, v209, v13\n"
    "v_sub_f32 v210, v210, v13\n"
    "v_sub_f32 v211, v211, v13\n"
    "v_sub_f32 v124, v124, v13\n"
    "v_sub_f32 v125, v125, v13\n"
    "v_sub_f32 v126, v126, v13\n"
    "v_sub_f32 v127, v127, v13\n"
    "v_sub_f32 v136, v136, v13\n"
    "v_sub_f32 v137, v137, v13\n"
    "v_sub_f32 v138, v138, v13\n"
    "v_sub_f32 v139, v139, v13\n"
    "v_sub_f32 v212, v212, v14\n"
    "v_sub_f32 v213, v213, v14\n"
    "v_sub_f32 v214, v214, v14\n"
    "v_sub_f32 v215, v215, v14\n"
    "v_sub_f32 v128, v128, v14\n"
    "v_sub_f32 v129, v129, v14\n"
    "v_sub_f32 v130, v130, v14\n"
    "v_sub_f32 v131, v131, v14\n"
    "v_sub_f32 v140, v140, v14\n"
    "v_sub_f32 v141, v141, v14\n"
    "v_sub_f32 v142, v142, v14\n"
    "v_sub_f32 v143, v143, v14\n"
    "v_exp_f32 v12, -v12\n"
    "v_exp_f32 v13, -v13\n"
    "v_exp_f32 v14, -v14\n"
    "s_nop 0\n"
    "v_mul_f32 v9, v12, v9\n"
    "v_accvgpr_read_b32 v15, a0\n"
    "v_accvgpr_read_b32 v16, a1\n"
    "v_accvgpr_read_b32 v17, a2\n"
    "v_accvgpr_read_b32 v18, a3\n"
    "v_mul_f32 v15, v12, v15\n"
    "v_mul_f32 v16, v12, v16\n"
    "v_mul_f32 v17, v12, v17\n"
    "v_mul_f32 v18, v12, v18\n"
    "v_accvgpr_write_b32 a0, v15\n"
    "v_accvgpr_write_b32 a1, v16\n"
    "v_accvgpr_write_b32 a2, v17\n"
    "v_accvgpr_write_b32 a3, v18\n"
    "v_accvgpr_read_b32 v15, a12\n"
    "v_accvgpr_read_b32 v16, a13\n"
    "v_accvgpr_read_b32 v17, a14\n"
    "v_accvgpr_read_b32 v18, a15\n"
    "v_mul_f32 v15, v12, v15\n"
    "v_mul_f32 v16, v12, v16\n"
    "v_mul_f32 v17, v12, v17\n"
    "v_mul_f32 v18, v12, v18\n"
    "v_accvgpr_write_b32 a12, v15\n"
    "v_accvgpr_write_b32 a13, v16\n"
    "v_accvgpr_write_b32 a14, v17\n"
    "v_accvgpr_write_b32 a15, v18\n"
    "v_accvgpr_read_b32 v15, a24\n"
    "v_accvgpr_read_b32 v16, a25\n"
    "v_accvgpr_read_b32 v17, a26\n"
    "v_accvgpr_read_b32 v18, a27\n"
    "v_mul_f32 v15, v12, v15\n"
    "v_mul_f32 v16, v12, v16\n"
    "v_mul_f32 v17, v12, v17\n"
    "v_mul_f32 v18, v12, v18\n"
    "v_accvgpr_write_b32 a24, v15\n"
    "v_accvgpr_write_b32 a25, v16\n"
    "v_accvgpr_write_b32 a26, v17\n"
    "v_accvgpr_write_b32 a27, v18\n"
    "v_accvgpr_read_b32 v15, a36\n"
    "v_accvgpr_read_b32 v16, a37\n"
    "v_accvgpr_read_b32 v17, a38\n"
    "v_accvgpr_read_b32 v18, a39\n"
    "v_mul_f32 v15, v12, v15\n"
    "v_mul_f32 v16, v12, v16\n"
    "v_mul_f32 v17, v12, v17\n"
    "v_mul_f32 v18, v12, v18\n"
    "v_accvgpr_write_b32 a36, v15\n"
    "v_accvgpr_write_b32 a37, v16\n"
    "v_accvgpr_write_b32 a38, v17\n"
    "v_accvgpr_write_b32 a39, v18\n"
    "v_accvgpr_read_b32 v15, a48\n"
    "v_accvgpr_read_b32 v16, a49\n"
    "v_accvgpr_read_b32 v17, a50\n"
    "v_accvgpr_read_b32 v18, a51\n"
    "v_mul_f32 v15, v12, v15\n"
    "v_mul_f32 v16, v12, v16\n"
    "v_mul_f32 v17, v12, v17\n"
    "v_mul_f32 v18, v12, v18\n"
    "v_accvgpr_write_b32 a48, v15\n"
    "v_accvgpr_write_b32 a49, v16\n"
    "v_accvgpr_write_b32 a50, v17\n"
    "v_accvgpr_write_b32 a51, v18\n"
    "v_accvgpr_read_b32 v15, a60\n"
    "v_accvgpr_read_b32 v16, a61\n"
    "v_accvgpr_read_b32 v17, a62\n"
    "v_accvgpr_read_b32 v18, a63\n"
    "v_mul_f32 v15, v12, v15\n"
    "v_mul_f32 v16, v12, v16\n"
    "v_mul_f32 v17, v12, v17\n"
    "v_mul_f32 v18, v12, v18\n"
    "v_accvgpr_write_b32 a60, v15\n"
    "v_accvgpr_write_b32 a61, v16\n"
    "v_accvgpr_write_b32 a62, v17\n"
    "v_accvgpr_write_b32 a63, v18\n"
    "v_accvgpr_read_b32 v15, a72\n"
    "v_accvgpr_read_b32 v16, a73\n"
    "v_accvgpr_read_b32 v17, a74\n"
    "v_accvgpr_read_b32 v18, a75\n"
    "v_mul_f32 v15, v12, v15\n"
    "v_mul_f32 v16, v12, v16\n"
    "v_mul_f32 v17, v12, v17\n"
    "v_mul_f32 v18, v12, v18\n"
    "v_accvgpr_write_b32 a72, v15\n"
    "v_accvgpr_write_b32 a73, v16\n"
    "v_accvgpr_write_b32 a74, v17\n"
    "v_accvgpr_write_b32 a75, v18\n"
    "v_accvgpr_read_b32 v15, a84\n"
    "v_accvgpr_read_b32 v16, a85\n"
    "v_accvgpr_read_b32 v17, a86\n"
    "v_accvgpr_read_b32 v18, a87\n"
    "v_mul_f32 v15, v12, v15\n"
    "v_mul_f32 v16, v12, v16\n"
    "v_mul_f32 v17, v12, v17\n"
    "v_mul_f32 v18, v12, v18\n"
    "v_accvgpr_write_b32 a84, v15\n"
    "v_accvgpr_write_b32 a85, v16\n"
    "v_accvgpr_write_b32 a86, v17\n"
    "v_accvgpr_write_b32 a87, v18\n"
    "v_accvgpr_read_b32 v15, a96\n"
    "v_accvgpr_read_b32 v16, a97\n"
    "v_accvgpr_read_b32 v17, a98\n"
    "v_accvgpr_read_b32 v18, a99\n"
    "v_mul_f32 v15, v12, v15\n"
    "v_mul_f32 v16, v12, v16\n"
    "v_mul_f32 v17, v12, v17\n"
    "v_mul_f32 v18, v12, v18\n"
    "v_accvgpr_write_b32 a96, v15\n"
    "v_accvgpr_write_b32 a97, v16\n"
    "v_accvgpr_write_b32 a98, v17\n"
    "v_accvgpr_write_b32 a99, v18\n"
    "v_accvgpr_read_b32 v15, a108\n"
    "v_accvgpr_read_b32 v16, a109\n"
    "v_accvgpr_read_b32 v17, a110\n"
    "v_accvgpr_read_b32 v18, a111\n"
    "v_mul_f32 v15, v12, v15\n"
    "v_mul_f32 v16, v12, v16\n"
    "v_mul_f32 v17, v12, v17\n"
    "v_mul_f32 v18, v12, v18\n"
    "v_accvgpr_write_b32 a108, v15\n"
    "v_accvgpr_write_b32 a109, v16\n"
    "v_accvgpr_write_b32 a110, v17\n"
    "v_accvgpr_write_b32 a111, v18\n"
    "v_accvgpr_read_b32 v15, a120\n"
    "v_accvgpr_read_b32 v16, a121\n"
    "v_accvgpr_read_b32 v17, a122\n"
    "v_accvgpr_read_b32 v18, a123\n"
    "v_mul_f32 v15, v12, v15\n"
    "v_mul_f32 v16, v12, v16\n"
    "v_mul_f32 v17, v12, v17\n"
    "v_mul_f32 v18, v12, v18\n"
    "v_accvgpr_write_b32 a120, v15\n"
    "v_accvgpr_write_b32 a121, v16\n"
    "v_accvgpr_write_b32 a122, v17\n"
    "v_accvgpr_write_b32 a123, v18\n"
    "v_accvgpr_read_b32 v15, a132\n"
    "v_accvgpr_read_b32 v16, a133\n"
    "v_accvgpr_read_b32 v17, a134\n"
    "v_accvgpr_read_b32 v18, a135\n"
    "v_mul_f32 v15, v12, v15\n"
    "v_mul_f32 v16, v12, v16\n"
    "v_mul_f32 v17, v12, v17\n"
    "v_mul_f32 v18, v12, v18\n"
    "v_accvgpr_write_b32 a132, v15\n"
    "v_accvgpr_write_b32 a133, v16\n"
    "v_accvgpr_write_b32 a134, v17\n"
    "v_accvgpr_write_b32 a135, v18\n"
    "v_accvgpr_read_b32 v15, a144\n"
    "v_accvgpr_read_b32 v16, a145\n"
    "v_accvgpr_read_b32 v17, a146\n"
    "v_accvgpr_read_b32 v18, a147\n"
    "v_mul_f32 v15, v12, v15\n"
    "v_mul_f32 v16, v12, v16\n"
    "v_mul_f32 v17, v12, v17\n"
    "v_mul_f32 v18, v12, v18\n"
    "v_accvgpr_write_b32 a144, v15\n"
    "v_accvgpr_write_b32 a145, v16\n"
    "v_accvgpr_write_b32 a146, v17\n"
    "v_accvgpr_write_b32 a147, v18\n"
    "v_accvgpr_read_b32 v15, a156\n"
    "v_accvgpr_read_b32 v16, a157\n"
    "v_accvgpr_read_b32 v17, a158\n"
    "v_accvgpr_read_b32 v18, a159\n"
    "v_mul_f32 v15, v12, v15\n"
    "v_mul_f32 v16, v12, v16\n"
    "v_mul_f32 v17, v12, v17\n"
    "v_mul_f32 v18, v12, v18\n"
    "v_accvgpr_write_b32 a156, v15\n"
    "v_accvgpr_write_b32 a157, v16\n"
    "v_accvgpr_write_b32 a158, v17\n"
    "v_accvgpr_write_b32 a159, v18\n"
    "v_accvgpr_read_b32 v15, a168\n"
    "v_accvgpr_read_b32 v16, a169\n"
    "v_accvgpr_read_b32 v17, a170\n"
    "v_accvgpr_read_b32 v18, a171\n"
    "v_mul_f32 v15, v12, v15\n"
    "v_mul_f32 v16, v12, v16\n"
    "v_mul_f32 v17, v12, v17\n"
    "v_mul_f32 v18, v12, v18\n"
    "v_accvgpr_write_b32 a168, v15\n"
    "v_accvgpr_write_b32 a169, v16\n"
    "v_accvgpr_write_b32 a170, v17\n"
    "v_accvgpr_write_b32 a171, v18\n"
    "v_accvgpr_read_b32 v15, a180\n"
    "v_accvgpr_read_b32 v16, a181\n"
    "v_accvgpr_read_b32 v17, a182\n"
    "v_accvgpr_read_b32 v18, a183\n"
    "v_mul_f32 v15, v12, v15\n"
    "v_mul_f32 v16, v12, v16\n"
    "v_mul_f32 v17, v12, v17\n"
    "v_mul_f32 v18, v12, v18\n"
    "v_accvgpr_write_b32 a180, v15\n"
    "v_accvgpr_write_b32 a181, v16\n"
    "v_accvgpr_write_b32 a182, v17\n"
    "v_accvgpr_write_b32 a183, v18\n"
    "v_mul_f32 v252, v13, v252\n"
    "v_accvgpr_read_b32 v15, a4\n"
    "v_accvgpr_read_b32 v16, a5\n"
    "v_accvgpr_read_b32 v17, a6\n"
    "v_accvgpr_read_b32 v18, a7\n"
    "v_mul_f32 v15, v13, v15\n"
    "v_mul_f32 v16, v13, v16\n"
    "v_mul_f32 v17, v13, v17\n"
    "v_mul_f32 v18, v13, v18\n"
    "v_accvgpr_write_b32 a4, v15\n"
    "v_accvgpr_write_b32 a5, v16\n"
    "v_accvgpr_write_b32 a6, v17\n"
    "v_accvgpr_write_b32 a7, v18\n"
    "v_accvgpr_read_b32 v15, a16\n"
    "v_accvgpr_read_b32 v16, a17\n"
    "v_accvgpr_read_b32 v17, a18\n"
    "v_accvgpr_read_b32 v18, a19\n"
    "v_mul_f32 v15, v13, v15\n"
    "v_mul_f32 v16, v13, v16\n"
    "v_mul_f32 v17, v13, v17\n"
    "v_mul_f32 v18, v13, v18\n"
    "v_accvgpr_write_b32 a16, v15\n"
    "v_accvgpr_write_b32 a17, v16\n"
    "v_accvgpr_write_b32 a18, v17\n"
    "v_accvgpr_write_b32 a19, v18\n"
    "v_accvgpr_read_b32 v15, a28\n"
    "v_accvgpr_read_b32 v16, a29\n"
    "v_accvgpr_read_b32 v17, a30\n"
    "v_accvgpr_read_b32 v18, a31\n"
    "v_mul_f32 v15, v13, v15\n"
    "v_mul_f32 v16, v13, v16\n"
    "v_mul_f32 v17, v13, v17\n"
    "v_mul_f32 v18, v13, v18\n"
    "v_accvgpr_write_b32 a28, v15\n"
    "v_accvgpr_write_b32 a29, v16\n"
    "v_accvgpr_write_b32 a30, v17\n"
    "v_accvgpr_write_b32 a31, v18\n"
    "v_accvgpr_read_b32 v15, a40\n"
    "v_accvgpr_read_b32 v16, a41\n"
    "v_accvgpr_read_b32 v17, a42\n"
    "v_accvgpr_read_b32 v18, a43\n"
    "v_mul_f32 v15, v13, v15\n"
    "v_mul_f32 v16, v13, v16\n"
    "v_mul_f32 v17, v13, v17\n"
    "v_mul_f32 v18, v13, v18\n"
    "v_accvgpr_write_b32 a40, v15\n"
    "v_accvgpr_write_b32 a41, v16\n"
    "v_accvgpr_write_b32 a42, v17\n"
    "v_accvgpr_write_b32 a43, v18\n"
    "v_accvgpr_read_b32 v15, a52\n"
    "v_accvgpr_read_b32 v16, a53\n"
    "v_accvgpr_read_b32 v17, a54\n"
    "v_accvgpr_read_b32 v18, a55\n"
    "v_mul_f32 v15, v13, v15\n"
    "v_mul_f32 v16, v13, v16\n"
    "v_mul_f32 v17, v13, v17\n"
    "v_mul_f32 v18, v13, v18\n"
    "v_accvgpr_write_b32 a52, v15\n"
    "v_accvgpr_write_b32 a53, v16\n"
    "v_accvgpr_write_b32 a54, v17\n"
    "v_accvgpr_write_b32 a55, v18\n"
    "v_accvgpr_read_b32 v15, a64\n"
    "v_accvgpr_read_b32 v16, a65\n"
    "v_accvgpr_read_b32 v17, a66\n"
    "v_accvgpr_read_b32 v18, a67\n"
    "v_mul_f32 v15, v13, v15\n"
    "v_mul_f32 v16, v13, v16\n"
    "v_mul_f32 v17, v13, v17\n"
    "v_mul_f32 v18, v13, v18\n"
    "v_accvgpr_write_b32 a64, v15\n"
    "v_accvgpr_write_b32 a65, v16\n"
    "v_accvgpr_write_b32 a66, v17\n"
    "v_accvgpr_write_b32 a67, v18\n"
    "v_accvgpr_read_b32 v15, a76\n"
    "v_accvgpr_read_b32 v16, a77\n"
    "v_accvgpr_read_b32 v17, a78\n"
    "v_accvgpr_read_b32 v18, a79\n"
    "v_mul_f32 v15, v13, v15\n"
    "v_mul_f32 v16, v13, v16\n"
    "v_mul_f32 v17, v13, v17\n"
    "v_mul_f32 v18, v13, v18\n"
    "v_accvgpr_write_b32 a76, v15\n"
    "v_accvgpr_write_b32 a77, v16\n"
    "v_accvgpr_write_b32 a78, v17\n"
    "v_accvgpr_write_b32 a79, v18\n"
    "v_accvgpr_read_b32 v15, a88\n"
    "v_accvgpr_read_b32 v16, a89\n"
    "v_accvgpr_read_b32 v17, a90\n"
    "v_accvgpr_read_b32 v18, a91\n"
    "v_mul_f32 v15, v13, v15\n"
    "v_mul_f32 v16, v13, v16\n"
    "v_mul_f32 v17, v13, v17\n"
    "v_mul_f32 v18, v13, v18\n"
    "v_accvgpr_write_b32 a88, v15\n"
    "v_accvgpr_write_b32 a89, v16\n"
    "v_accvgpr_write_b32 a90, v17\n"
    "v_accvgpr_write_b32 a91, v18\n"
    "v_accvgpr_read_b32 v15, a100\n"
    "v_accvgpr_read_b32 v16, a101\n"
    "v_accvgpr_read_b32 v17, a102\n"
    "v_accvgpr_read_b32 v18, a103\n"
    "v_mul_f32 v15, v13, v15\n"
    "v_mul_f32 v16, v13, v16\n"
    "v_mul_f32 v17, v13, v17\n"
    "v_mul_f32 v18, v13, v18\n"
    "v_accvgpr_write_b32 a100, v15\n"
    "v_accvgpr_write_b32 a101, v16\n"
    "v_accvgpr_write_b32 a102, v17\n"
    "v_accvgpr_write_b32 a103, v18\n"
    "v_accvgpr_read_b32 v15, a112\n"
    "v_accvgpr_read_b32 v16, a113\n"
    "v_accvgpr_read_b32 v17, a114\n"
    "v_accvgpr_read_b32 v18, a115\n"
    "v_mul_f32 v15, v13, v15\n"
    "v_mul_f32 v16, v13, v16\n"
    "v_mul_f32 v17, v13, v17\n"
    "v_mul_f32 v18, v13, v18\n"
    "v_accvgpr_write_b32 a112, v15\n"
    "v_accvgpr_write_b32 a113, v16\n"
    "v_accvgpr_write_b32 a114, v17\n"
    "v_accvgpr_write_b32 a115, v18\n"
    "v_accvgpr_read_b32 v15, a124\n"
    "v_accvgpr_read_b32 v16, a125\n"
    "v_accvgpr_read_b32 v17, a126\n"
    "v_accvgpr_read_b32 v18, a127\n"
    "v_mul_f32 v15, v13, v15\n"
    "v_mul_f32 v16, v13, v16\n"
    "v_mul_f32 v17, v13, v17\n"
    "v_mul_f32 v18, v13, v18\n"
    "v_accvgpr_write_b32 a124, v15\n"
    "v_accvgpr_write_b32 a125, v16\n"
    "v_accvgpr_write_b32 a126, v17\n"
    "v_accvgpr_write_b32 a127, v18\n"
    "v_accvgpr_read_b32 v15, a136\n"
    "v_accvgpr_read_b32 v16, a137\n"
    "v_accvgpr_read_b32 v17, a138\n"
    "v_accvgpr_read_b32 v18, a139\n"
    "v_mul_f32 v15, v13, v15\n"
    "v_mul_f32 v16, v13, v16\n"
    "v_mul_f32 v17, v13, v17\n"
    "v_mul_f32 v18, v13, v18\n"
    "v_accvgpr_write_b32 a136, v15\n"
    "v_accvgpr_write_b32 a137, v16\n"
    "v_accvgpr_write_b32 a138, v17\n"
    "v_accvgpr_write_b32 a139, v18\n"
    "v_accvgpr_read_b32 v15, a148\n"
    "v_accvgpr_read_b32 v16, a149\n"
    "v_accvgpr_read_b32 v17, a150\n"
    "v_accvgpr_read_b32 v18, a151\n"
    "v_mul_f32 v15, v13, v15\n"
    "v_mul_f32 v16, v13, v16\n"
    "v_mul_f32 v17, v13, v17\n"
    "v_mul_f32 v18, v13, v18\n"
    "v_accvgpr_write_b32 a148, v15\n"
    "v_accvgpr_write_b32 a149, v16\n"
    "v_accvgpr_write_b32 a150, v17\n"
    "v_accvgpr_write_b32 a151, v18\n"
    "v_accvgpr_read_b32 v15, a160\n"
    "v_accvgpr_read_b32 v16, a161\n"
    "v_accvgpr_read_b32 v17, a162\n"
    "v_accvgpr_read_b32 v18, a163\n"
    "v_mul_f32 v15, v13, v15\n"
    "v_mul_f32 v16, v13, v16\n"
    "v_mul_f32 v17, v13, v17\n"
    "v_mul_f32 v18, v13, v18\n"
    "v_accvgpr_write_b32 a160, v15\n"
    "v_accvgpr_write_b32 a161, v16\n"
    "v_accvgpr_write_b32 a162, v17\n"
    "v_accvgpr_write_b32 a163, v18\n"
    "v_accvgpr_read_b32 v15, a172\n"
    "v_accvgpr_read_b32 v16, a173\n"
    "v_accvgpr_read_b32 v17, a174\n"
    "v_accvgpr_read_b32 v18, a175\n"
    "v_mul_f32 v15, v13, v15\n"
    "v_mul_f32 v16, v13, v16\n"
    "v_mul_f32 v17, v13, v17\n"
    "v_mul_f32 v18, v13, v18\n"
    "v_accvgpr_write_b32 a172, v15\n"
    "v_accvgpr_write_b32 a173, v16\n"
    "v_accvgpr_write_b32 a174, v17\n"
    "v_accvgpr_write_b32 a175, v18\n"
    "v_accvgpr_read_b32 v15, a184\n"
    "v_accvgpr_read_b32 v16, a185\n"
    "v_accvgpr_read_b32 v17, a186\n"
    "v_accvgpr_read_b32 v18, a187\n"
    "v_mul_f32 v15, v13, v15\n"
    "v_mul_f32 v16, v13, v16\n"
    "v_mul_f32 v17, v13, v17\n"
    "v_mul_f32 v18, v13, v18\n"
    "v_accvgpr_write_b32 a184, v15\n"
    "v_accvgpr_write_b32 a185, v16\n"
    "v_accvgpr_write_b32 a186, v17\n"
    "v_accvgpr_write_b32 a187, v18\n"
    "v_mul_f32 v253, v14, v253\n"
    "v_accvgpr_read_b32 v15, a8\n"
    "v_accvgpr_read_b32 v16, a9\n"
    "v_accvgpr_read_b32 v17, a10\n"
    "v_accvgpr_read_b32 v18, a11\n"
    "v_mul_f32 v15, v14, v15\n"
    "v_mul_f32 v16, v14, v16\n"
    "v_mul_f32 v17, v14, v17\n"
    "v_mul_f32 v18, v14, v18\n"
    "v_accvgpr_write_b32 a8, v15\n"
    "v_accvgpr_write_b32 a9, v16\n"
    "v_accvgpr_write_b32 a10, v17\n"
    "v_accvgpr_write_b32 a11, v18\n"
    "v_accvgpr_read_b32 v15, a20\n"
    "v_accvgpr_read_b32 v16, a21\n"
    "v_accvgpr_read_b32 v17, a22\n"
    "v_accvgpr_read_b32 v18, a23\n"
    "v_mul_f32 v15, v14, v15\n"
    "v_mul_f32 v16, v14, v16\n"
    "v_mul_f32 v17, v14, v17\n"
    "v_mul_f32 v18, v14, v18\n"
    "v_accvgpr_write_b32 a20, v15\n"
    "v_accvgpr_write_b32 a21, v16\n"
    "v_accvgpr_write_b32 a22, v17\n"
    "v_accvgpr_write_b32 a23, v18\n"
    "v_accvgpr_read_b32 v15, a32\n"
    "v_accvgpr_read_b32 v16, a33\n"
    "v_accvgpr_read_b32 v17, a34\n"
    "v_accvgpr_read_b32 v18, a35\n"
    "v_mul_f32 v15, v14, v15\n"
    "v_mul_f32 v16, v14, v16\n"
    "v_mul_f32 v17, v14, v17\n"
    "v_mul_f32 v18, v14, v18\n"
    "v_accvgpr_write_b32 a32, v15\n"
    "v_accvgpr_write_b32 a33, v16\n"
    "v_accvgpr_write_b32 a34, v17\n"
    "v_accvgpr_write_b32 a35, v18\n"
    "v_accvgpr_read_b32 v15, a44\n"
    "v_accvgpr_read_b32 v16, a45\n"
    "v_accvgpr_read_b32 v17, a46\n"
    "v_accvgpr_read_b32 v18, a47\n"
    "v_mul_f32 v15, v14, v15\n"
    "v_mul_f32 v16, v14, v16\n"
    "v_mul_f32 v17, v14, v17\n"
    "v_mul_f32 v18, v14, v18\n"
    "v_accvgpr_write_b32 a44, v15\n"
    "v_accvgpr_write_b32 a45, v16\n"
    "v_accvgpr_write_b32 a46, v17\n"
    "v_accvgpr_write_b32 a47, v18\n"
    "v_accvgpr_read_b32 v15, a56\n"
    "v_accvgpr_read_b32 v16, a57\n"
    "v_accvgpr_read_b32 v17, a58\n"
    "v_accvgpr_read_b32 v18, a59\n"
    "v_mul_f32 v15, v14, v15\n"
    "v_mul_f32 v16, v14, v16\n"
    "v_mul_f32 v17, v14, v17\n"
    "v_mul_f32 v18, v14, v18\n"
    "v_accvgpr_write_b32 a56, v15\n"
    "v_accvgpr_write_b32 a57, v16\n"
    "v_accvgpr_write_b32 a58, v17\n"
    "v_accvgpr_write_b32 a59, v18\n"
    "v_accvgpr_read_b32 v15, a68\n"
    "v_accvgpr_read_b32 v16, a69\n"
    "v_accvgpr_read_b32 v17, a70\n"
    "v_accvgpr_read_b32 v18, a71\n"
    "v_mul_f32 v15, v14, v15\n"
    "v_mul_f32 v16, v14, v16\n"
    "v_mul_f32 v17, v14, v17\n"
    "v_mul_f32 v18, v14, v18\n"
    "v_accvgpr_write_b32 a68, v15\n"
    "v_accvgpr_write_b32 a69, v16\n"
    "v_accvgpr_write_b32 a70, v17\n"
    "v_accvgpr_write_b32 a71, v18\n"
    "v_accvgpr_read_b32 v15, a80\n"
    "v_accvgpr_read_b32 v16, a81\n"
    "v_accvgpr_read_b32 v17, a82\n"
    "v_accvgpr_read_b32 v18, a83\n"
    "v_mul_f32 v15, v14, v15\n"
    "v_mul_f32 v16, v14, v16\n"
    "v_mul_f32 v17, v14, v17\n"
    "v_mul_f32 v18, v14, v18\n"
    "v_accvgpr_write_b32 a80, v15\n"
    "v_accvgpr_write_b32 a81, v16\n"
    "v_accvgpr_write_b32 a82, v17\n"
    "v_accvgpr_write_b32 a83, v18\n"
    "v_accvgpr_read_b32 v15, a92\n"
    "v_accvgpr_read_b32 v16, a93\n"
    "v_accvgpr_read_b32 v17, a94\n"
    "v_accvgpr_read_b32 v18, a95\n"
    "v_mul_f32 v15, v14, v15\n"
    "v_mul_f32 v16, v14, v16\n"
    "v_mul_f32 v17, v14, v17\n"
    "v_mul_f32 v18, v14, v18\n"
    "v_accvgpr_write_b32 a92, v15\n"
    "v_accvgpr_write_b32 a93, v16\n"
    "v_accvgpr_write_b32 a94, v17\n"
    "v_accvgpr_write_b32 a95, v18\n"
    "v_accvgpr_read_b32 v15, a104\n"
    "v_accvgpr_read_b32 v16, a105\n"
    "v_accvgpr_read_b32 v17, a106\n"
    "v_accvgpr_read_b32 v18, a107\n"
    "v_mul_f32 v15, v14, v15\n"
    "v_mul_f32 v16, v14, v16\n"
    "v_mul_f32 v17, v14, v17\n"
    "v_mul_f32 v18, v14, v18\n"
    "v_accvgpr_write_b32 a104, v15\n"
    "v_accvgpr_write_b32 a105, v16\n"
    "v_accvgpr_write_b32 a106, v17\n"
    "v_accvgpr_write_b32 a107, v18\n"
    "v_accvgpr_read_b32 v15, a116\n"
    "v_accvgpr_read_b32 v16, a117\n"
    "v_accvgpr_read_b32 v17, a118\n"
    "v_accvgpr_read_b32 v18, a119\n"
    "v_mul_f32 v15, v14, v15\n"
    "v_mul_f32 v16, v14, v16\n"
    "v_mul_f32 v17, v14, v17\n"
    "v_mul_f32 v18, v14, v18\n"
    "v_accvgpr_write_b32 a116, v15\n"
    "v_accvgpr_write_b32 a117, v16\n"
    "v_accvgpr_write_b32 a118, v17\n"
    "v_accvgpr_write_b32 a119, v18\n"
    "v_accvgpr_read_b32 v15, a128\n"
    "v_accvgpr_read_b32 v16, a129\n"
    "v_accvgpr_read_b32 v17, a130\n"
    "v_accvgpr_read_b32 v18, a131\n"
    "v_mul_f32 v15, v14, v15\n"
    "v_mul_f32 v16, v14, v16\n"
    "v_mul_f32 v17, v14, v17\n"
    "v_mul_f32 v18, v14, v18\n"
    "v_accvgpr_write_b32 a128, v15\n"
    "v_accvgpr_write_b32 a129, v16\n"
    "v_accvgpr_write_b32 a130, v17\n"
    "v_accvgpr_write_b32 a131, v18\n"
    "v_accvgpr_read_b32 v15, a140\n"
    "v_accvgpr_read_b32 v16, a141\n"
    "v_accvgpr_read_b32 v17, a142\n"
    "v_accvgpr_read_b32 v18, a143\n"
    "v_mul_f32 v15, v14, v15\n"
    "v_mul_f32 v16, v14, v16\n"
    "v_mul_f32 v17, v14, v17\n"
    "v_mul_f32 v18, v14, v18\n"
    "v_accvgpr_write_b32 a140, v15\n"
    "v_accvgpr_write_b32 a141, v16\n"
    "v_accvgpr_write_b32 a142, v17\n"
    "v_accvgpr_write_b32 a143, v18\n"
    "v_accvgpr_read_b32 v15, a152\n"
    "v_accvgpr_read_b32 v16, a153\n"
    "v_accvgpr_read_b32 v17, a154\n"
    "v_accvgpr_read_b32 v18, a155\n"
    "v_mul_f32 v15, v14, v15\n"
    "v_mul_f32 v16, v14, v16\n"
    "v_mul_f32 v17, v14, v17\n"
    "v_mul_f32 v18, v14, v18\n"
    "v_accvgpr_write_b32 a152, v15\n"
    "v_accvgpr_write_b32 a153, v16\n"
    "v_accvgpr_write_b32 a154, v17\n"
    "v_accvgpr_write_b32 a155, v18\n"
    "v_accvgpr_read_b32 v15, a164\n"
    "v_accvgpr_read_b32 v16, a165\n"
    "v_accvgpr_read_b32 v17, a166\n"
    "v_accvgpr_read_b32 v18, a167\n"
    "v_mul_f32 v15, v14, v15\n"
    "v_mul_f32 v16, v14, v16\n"
    "v_mul_f32 v17, v14, v17\n"
    "v_mul_f32 v18, v14, v18\n"
    "v_accvgpr_write_b32 a164, v15\n"
    "v_accvgpr_write_b32 a165, v16\n"
    "v_accvgpr_write_b32 a166, v17\n"
    "v_accvgpr_write_b32 a167, v18\n"
    "v_accvgpr_read_b32 v15, a176\n"
    "v_accvgpr_read_b32 v16, a177\n"
    "v_accvgpr_read_b32 v17, a178\n"
    "v_accvgpr_read_b32 v18, a179\n"
    "v_mul_f32 v15, v14, v15\n"
    "v_mul_f32 v16, v14, v16\n"
    "v_mul_f32 v17, v14, v17\n"
    "v_mul_f32 v18, v14, v18\n"
    "v_accvgpr_write_b32 a176, v15\n"
    "v_accvgpr_write_b32 a177, v16\n"
    "v_accvgpr_write_b32 a178, v17\n"
    "v_accvgpr_write_b32 a179, v18\n"
    "v_accvgpr_read_b32 v15, a188\n"
    "v_accvgpr_read_b32 v16, a189\n"
    "v_accvgpr_read_b32 v17, a190\n"
    "v_accvgpr_read_b32 v18, a191\n"
    "v_mul_f32 v15, v14, v15\n"
    "v_mul_f32 v16, v14, v16\n"
    "v_mul_f32 v17, v14, v17\n"
    "v_mul_f32 v18, v14, v18\n"
    "v_accvgpr_write_b32 a188, v15\n"
    "v_accvgpr_write_b32 a189, v16\n"
    "v_accvgpr_write_b32 a190, v17\n"
    "v_accvgpr_write_b32 a191, v18\n"
    "s_nop 1\n"
    "s_cmp_eq_u32 s62, 1\n"
    "s_cbranch_scc1 RET_L0_%=\n"
    "s_branch RET_P30_%=\n"
    "RARE1_%=:\n"
    "s_nop 7\n"
    "s_nop 7\n"
    "v_max3_f32 v12, v168, v169, v170\n"
    "v_max3_f32 v12, v12, v171, v180\n"
    "v_max3_f32 v12, v12, v181, v182\n"
    "v_max_f32 v12, v12, v183\n"
    "v_max3_f32 v13, v172, v173, v174\n"
    "v_max3_f32 v13, v13, v175, v184\n"
    "v_max3_f32 v13, v13, v185, v186\n"
    "v_max_f32 v13, v13, v187\n"
    "v_max3_f32 v14, v176, v177, v178\n"
    "v_max3_f32 v14, v14, v179, v188\n"
    "v_max3_f32 v14, v14, v189, v190\n"
    "v_max_f32 v14, v14, v191\n"
    "v_mov_b32 v15, v12\n"
    "v_mov_b32 v16, v13\n"
    "v_mov_b32 v17, v14\n"
    "s_nop 1\n"
    "v_permlane32_swap_b32 v12, v15\n"
    "v_permlane32_swap_b32 v13, v16\n"
    "v_permlane32_swap_b32 v14, v17\n"
    "s_nop 1\n"
    "v_max_f32 v12, v12, v15\n"
    "v_max_f32 v13, v13, v16\n"
    "v_max_f32 v14, v14, v17\n"
    "v_mov_b32 v15, v12\n"
    "v_mov_b32 v16, v13\n"
    "v_mov_b32 v17, v14\n"
    "s_nop 1\n"
    "v_permlane16_swap_b32 v12, v15\n"
    "v_permlane16_swap_b32 v13, v16\n"
    "v_permlane16_swap_b32 v14, v17\n"
    "s_nop 1\n"
    "v_max_f32 v12, v12, v15\n"
    "v_max_f32 v13, v13, v16\n"
    "v_max_f32 v14, v14, v17\n"
    "v_max_f32 v12, 0, v12\n"
    "v_max_f32 v13, 0, v13\n"
    "v_max_f32 v14, 0, v14\n"
    "v_sub_f32 v204, v204, v12\n"
    "v_sub_f32 v205, v205, v12\n"
    "v_sub_f32 v206, v206, v12\n"
    "v_sub_f32 v207, v207, v12\n"
    "v_sub_f32 v168, v168, v12\n"
    "v_sub_f32 v169, v169, v12\n"
    "v_sub_f32 v170, v170, v12\n"
    "v_sub_f32 v171, v171, v12\n"
    "v_sub_f32 v180, v180, v12\n"
    "v_sub_f32 v181, v181, v12\n"
    "v_sub_f32 v182, v182, v12\n"
    "v_sub_f32 v183, v183, v12\n"
    "v_sub_f32 v208, v208, v13\n"
    "v_sub_f32 v209, v209, v13\n"
    "v_sub_f32 v210, v210, v13\n"
    "v_sub_f32 v211, v211, v13\n"
    "v_sub_f32 v172, v172, v13\n"
    "v_sub_f32 v173, v173, v13\n"
    "v_sub_f32 v174, v174, v13\n"
    "v_sub_f32 v175, v175, v13\n"
    "v_sub_f32 v184, v184, v13\n"
    "v_sub_f32 v185, v185, v13\n"
    "v_sub_f32 v186, v186, v13\n"
    "v_sub_f32 v187, v187, v13\n"
    "v_sub_f32 v212, v212, v14\n"
    "v_sub_f32 v213, v213, v14\n"
    "v_sub_f32 v214, v214, v14\n"
    "v_sub_f32 v215, v215, v14\n"
    "v_sub_f32 v176, v176, v14\n"
    "v_sub_f32 v177, v177, v14\n"
    "v_sub_f32 v178, v178, v14\n"
    "v_sub_f32 v179, v179, v14\n"
    "v_sub_f32 v188, v188, v14\n"
    "v_sub_f32 v189, v189, v14\n"
    "v_sub_f32 v190, v190, v14\n"
    "v_sub_f32 v191, v191, v14\n"
    "v_exp_f32 v12, -v12\n"
    "v_exp_f32 v13, -v13\n"
    "v_exp_f32 v14, -v14\n"
    "s_nop 0\n"
    "v_mul_f32 v9, v12, v9\n"
    "v_accvgpr_read_b32 v15, a0\n"
    "v_accvgpr_read_b32 v16, a1\n"
    "v_accvgpr_read_b32 v17, a2\n"
    "v_accvgpr_read_b32 v18, a3\n"
    "v_mul_f32 v15, v12, v15\n"
    "v_mul_f32 v16, v12, v16\n"
    "v_mul_f32 v17, v12, v17\n"
    "v_mul_f32 v18, v12, v18\n"
    "v_accvgpr_write_b32 a0, v15\n"
    "v_accvgpr_write_b32 a1, v16\n"
    "v_accvgpr_write_b32 a2, v17\n"
    "v_accvgpr_write_b32 a3, v18\n"
    "v_accvgpr_read_b32 v15, a12\n"
    "v_accvgpr_read_b32 v16, a13\n"
    "v_accvgpr_read_b32 v17, a14\n"
    "v_accvgpr_read_b32 v18, a15\n"
    "v_mul_f32 v15, v12, v15\n"
    "v_mul_f32 v16, v12, v16\n"
    "v_mul_f32 v17, v12, v17\n"
    "v_mul_f32 v18, v12, v18\n"
    "v_accvgpr_write_b32 a12, v15\n"
    "v_accvgpr_write_b32 a13, v16\n"
    "v_accvgpr_write_b32 a14, v17\n"
    "v_accvgpr_write_b32 a15, v18\n"
    "v_accvgpr_read_b32 v15, a24\n"
    "v_accvgpr_read_b32 v16, a25\n"
    "v_accvgpr_read_b32 v17, a26\n"
    "v_accvgpr_read_b32 v18, a27\n"
    "v_mul_f32 v15, v12, v15\n"
    "v_mul_f32 v16, v12, v16\n"
    "v_mul_f32 v17, v12, v17\n"
    "v_mul_f32 v18, v12, v18\n"
    "v_accvgpr_write_b32 a24, v15\n"
    "v_accvgpr_write_b32 a25, v16\n"
    "v_accvgpr_write_b32 a26, v17\n"
    "v_accvgpr_write_b32 a27, v18\n"
    "v_accvgpr_read_b32 v15, a36\n"
    "v_accvgpr_read_b32 v16, a37\n"
    "v_accvgpr_read_b32 v17, a38\n"
    "v_accvgpr_read_b32 v18, a39\n"
    "v_mul_f32 v15, v12, v15\n"
    "v_mul_f32 v16, v12, v16\n"
    "v_mul_f32 v17, v12, v17\n"
    "v_mul_f32 v18, v12, v18\n"
    "v_accvgpr_write_b32 a36, v15\n"
    "v_accvgpr_write_b32 a37, v16\n"
    "v_accvgpr_write_b32 a38, v17\n"
    "v_accvgpr_write_b32 a39, v18\n"
    "v_accvgpr_read_b32 v15, a48\n"
    "v_accvgpr_read_b32 v16, a49\n"
    "v_accvgpr_read_b32 v17, a50\n"
    "v_accvgpr_read_b32 v18, a51\n"
    "v_mul_f32 v15, v12, v15\n"
    "v_mul_f32 v16, v12, v16\n"
    "v_mul_f32 v17, v12, v17\n"
    "v_mul_f32 v18, v12, v18\n"
    "v_accvgpr_write_b32 a48, v15\n"
    "v_accvgpr_write_b32 a49, v16\n"
    "v_accvgpr_write_b32 a50, v17\n"
    "v_accvgpr_write_b32 a51, v18\n"
    "v_accvgpr_read_b32 v15, a60\n"
    "v_accvgpr_read_b32 v16, a61\n"
    "v_accvgpr_read_b32 v17, a62\n"
    "v_accvgpr_read_b32 v18, a63\n"
    "v_mul_f32 v15, v12, v15\n"
    "v_mul_f32 v16, v12, v16\n"
    "v_mul_f32 v17, v12, v17\n"
    "v_mul_f32 v18, v12, v18\n"
    "v_accvgpr_write_b32 a60, v15\n"
    "v_accvgpr_write_b32 a61, v16\n"
    "v_accvgpr_write_b32 a62, v17\n"
    "v_accvgpr_write_b32 a63, v18\n"
    "v_accvgpr_read_b32 v15, a72\n"
    "v_accvgpr_read_b32 v16, a73\n"
    "v_accvgpr_read_b32 v17, a74\n"
    "v_accvgpr_read_b32 v18, a75\n"
    "v_mul_f32 v15, v12, v15\n"
    "v_mul_f32 v16, v12, v16\n"
    "v_mul_f32 v17, v12, v17\n"
    "v_mul_f32 v18, v12, v18\n"
    "v_accvgpr_write_b32 a72, v15\n"
    "v_accvgpr_write_b32 a73, v16\n"
    "v_accvgpr_write_b32 a74, v17\n"
    "v_accvgpr_write_b32 a75, v18\n"
    "v_accvgpr_read_b32 v15, a84\n"
    "v_accvgpr_read_b32 v16, a85\n"
    "v_accvgpr_read_b32 v17, a86\n"
    "v_accvgpr_read_b32 v18, a87\n"
    "v_mul_f32 v15, v12, v15\n"
    "v_mul_f32 v16, v12, v16\n"
    "v_mul_f32 v17, v12, v17\n"
    "v_mul_f32 v18, v12, v18\n"
    "v_accvgpr_write_b32 a84, v15\n"
    "v_accvgpr_write_b32 a85, v16\n"
    "v_accvgpr_write_b32 a86, v17\n"
    "v_accvgpr_write_b32 a87, v18\n"
    "v_accvgpr_read_b32 v15, a96\n"
    "v_accvgpr_read_b32 v16, a97\n"
    "v_accvgpr_read_b32 v17, a98\n"
    "v_accvgpr_read_b32 v18, a99\n"
    "v_mul_f32 v15, v12, v15\n"
    "v_mul_f32 v16, v12, v16\n"
    "v_mul_f32 v17, v12, v17\n"
    "v_mul_f32 v18, v12, v18\n"
    "v_accvgpr_write_b32 a96, v15\n"
    "v_accvgpr_write_b32 a97, v16\n"
    "v_accvgpr_write_b32 a98, v17\n"
    "v_accvgpr_write_b32 a99, v18\n"
    "v_accvgpr_read_b32 v15, a108\n"
    "v_accvgpr_read_b32 v16, a109\n"
    "v_accvgpr_read_b32 v17, a110\n"
    "v_accvgpr_read_b32 v18, a111\n"
    "v_mul_f32 v15, v12, v15\n"
    "v_mul_f32 v16, v12, v16\n"
    "v_mul_f32 v17, v12, v17\n"
    "v_mul_f32 v18, v12, v18\n"
    "v_accvgpr_write_b32 a108, v15\n"
    "v_accvgpr_write_b32 a109, v16\n"
    "v_accvgpr_write_b32 a110, v17\n"
    "v_accvgpr_write_b32 a111, v18\n"
    "v_accvgpr_read_b32 v15, a120\n"
    "v_accvgpr_read_b32 v16, a121\n"
    "v_accvgpr_read_b32 v17, a122\n"
    "v_accvgpr_read_b32 v18, a123\n"
    "v_mul_f32 v15, v12, v15\n"
    "v_mul_f32 v16, v12, v16\n"
    "v_mul_f32 v17, v12, v17\n"
    "v_mul_f32 v18, v12, v18\n"
    "v_accvgpr_write_b32 a120, v15\n"
    "v_accvgpr_write_b32 a121, v16\n"
    "v_accvgpr_write_b32 a122, v17\n"
    "v_accvgpr_write_b32 a123, v18\n"
    "v_accvgpr_read_b32 v15, a132\n"
    "v_accvgpr_read_b32 v16, a133\n"
    "v_accvgpr_read_b32 v17, a134\n"
    "v_accvgpr_read_b32 v18, a135\n"
    "v_mul_f32 v15, v12, v15\n"
    "v_mul_f32 v16, v12, v16\n"
    "v_mul_f32 v17, v12, v17\n"
    "v_mul_f32 v18, v12, v18\n"
    "v_accvgpr_write_b32 a132, v15\n"
    "v_accvgpr_write_b32 a133, v16\n"
    "v_accvgpr_write_b32 a134, v17\n"
    "v_accvgpr_write_b32 a135, v18\n"
    "v_accvgpr_read_b32 v15, a144\n"
    "v_accvgpr_read_b32 v16, a145\n"
    "v_accvgpr_read_b32 v17, a146\n"
    "v_accvgpr_read_b32 v18, a147\n"
    "v_mul_f32 v15, v12, v15\n"
    "v_mul_f32 v16, v12, v16\n"
    "v_mul_f32 v17, v12, v17\n"
    "v_mul_f32 v18, v12, v18\n"
    "v_accvgpr_write_b32 a144, v15\n"
    "v_accvgpr_write_b32 a145, v16\n"
    "v_accvgpr_write_b32 a146, v17\n"
    "v_accvgpr_write_b32 a147, v18\n"
    "v_accvgpr_read_b32 v15, a156\n"
    "v_accvgpr_read_b32 v16, a157\n"
    "v_accvgpr_read_b32 v17, a158\n"
    "v_accvgpr_read_b32 v18, a159\n"
    "v_mul_f32 v15, v12, v15\n"
    "v_mul_f32 v16, v12, v16\n"
    "v_mul_f32 v17, v12, v17\n"
    "v_mul_f32 v18, v12, v18\n"
    "v_accvgpr_write_b32 a156, v15\n"
    "v_accvgpr_write_b32 a157, v16\n"
    "v_accvgpr_write_b32 a158, v17\n"
    "v_accvgpr_write_b32 a159, v18\n"
    "v_accvgpr_read_b32 v15, a168\n"
    "v_accvgpr_read_b32 v16, a169\n"
    "v_accvgpr_read_b32 v17, a170\n"
    "v_accvgpr_read_b32 v18, a171\n"
    "v_mul_f32 v15, v12, v15\n"
    "v_mul_f32 v16, v12, v16\n"
    "v_mul_f32 v17, v12, v17\n"
    "v_mul_f32 v18, v12, v18\n"
    "v_accvgpr_write_b32 a168, v15\n"
    "v_accvgpr_write_b32 a169, v16\n"
    "v_accvgpr_write_b32 a170, v17\n"
    "v_accvgpr_write_b32 a171, v18\n"
    "v_accvgpr_read_b32 v15, a180\n"
    "v_accvgpr_read_b32 v16, a181\n"
    "v_accvgpr_read_b32 v17, a182\n"
    "v_accvgpr_read_b32 v18, a183\n"
    "v_mul_f32 v15, v12, v15\n"
    "v_mul_f32 v16, v12, v16\n"
    "v_mul_f32 v17, v12, v17\n"
    "v_mul_f32 v18, v12, v18\n"
    "v_accvgpr_write_b32 a180, v15\n"
    "v_accvgpr_write_b32 a181, v16\n"
    "v_accvgpr_write_b32 a182, v17\n"
    "v_accvgpr_write_b32 a183, v18\n"
    "v_mul_f32 v252, v13, v252\n"
    "v_accvgpr_read_b32 v15, a4\n"
    "v_accvgpr_read_b32 v16, a5\n"
    "v_accvgpr_read_b32 v17, a6\n"
    "v_accvgpr_read_b32 v18, a7\n"
    "v_mul_f32 v15, v13, v15\n"
    "v_mul_f32 v16, v13, v16\n"
    "v_mul_f32 v17, v13, v17\n"
    "v_mul_f32 v18, v13, v18\n"
    "v_accvgpr_write_b32 a4, v15\n"
    "v_accvgpr_write_b32 a5, v16\n"
    "v_accvgpr_write_b32 a6, v17\n"
    "v_accvgpr_write_b32 a7, v18\n"
    "v_accvgpr_read_b32 v15, a16\n"
    "v_accvgpr_read_b32 v16, a17\n"
    "v_accvgpr_read_b32 v17, a18\n"
    "v_accvgpr_read_b32 v18, a19\n"
    "v_mul_f32 v15, v13, v15\n"
    "v_mul_f32 v16, v13, v16\n"
    "v_mul_f32 v17, v13, v17\n"
    "v_mul_f32 v18, v13, v18\n"
    "v_accvgpr_write_b32 a16, v15\n"
    "v_accvgpr_write_b32 a17, v16\n"
    "v_accvgpr_write_b32 a18, v17\n"
    "v_accvgpr_write_b32 a19, v18\n"
    "v_accvgpr_read_b32 v15, a28\n"
    "v_accvgpr_read_b32 v16, a29\n"
    "v_accvgpr_read_b32 v17, a30\n"
    "v_accvgpr_read_b32 v18, a31\n"
    "v_mul_f32 v15, v13, v15\n"
    "v_mul_f32 v16, v13, v16\n"
    "v_mul_f32 v17, v13, v17\n"
    "v_mul_f32 v18, v13, v18\n"
    "v_accvgpr_write_b32 a28, v15\n"
    "v_accvgpr_write_b32 a29, v16\n"
    "v_accvgpr_write_b32 a30, v17\n"
    "v_accvgpr_write_b32 a31, v18\n"
    "v_accvgpr_read_b32 v15, a40\n"
    "v_accvgpr_read_b32 v16, a41\n"
    "v_accvgpr_read_b32 v17, a42\n"
    "v_accvgpr_read_b32 v18, a43\n"
    "v_mul_f32 v15, v13, v15\n"
    "v_mul_f32 v16, v13, v16\n"
    "v_mul_f32 v17, v13, v17\n"
    "v_mul_f32 v18, v13, v18\n"
    "v_accvgpr_write_b32 a40, v15\n"
    "v_accvgpr_write_b32 a41, v16\n"
    "v_accvgpr_write_b32 a42, v17\n"
    "v_accvgpr_write_b32 a43, v18\n"
    "v_accvgpr_read_b32 v15, a52\n"
    "v_accvgpr_read_b32 v16, a53\n"
    "v_accvgpr_read_b32 v17, a54\n"
    "v_accvgpr_read_b32 v18, a55\n"
    "v_mul_f32 v15, v13, v15\n"
    "v_mul_f32 v16, v13, v16\n"
    "v_mul_f32 v17, v13, v17\n"
    "v_mul_f32 v18, v13, v18\n"
    "v_accvgpr_write_b32 a52, v15\n"
    "v_accvgpr_write_b32 a53, v16\n"
    "v_accvgpr_write_b32 a54, v17\n"
    "v_accvgpr_write_b32 a55, v18\n"
    "v_accvgpr_read_b32 v15, a64\n"
    "v_accvgpr_read_b32 v16, a65\n"
    "v_accvgpr_read_b32 v17, a66\n"
    "v_accvgpr_read_b32 v18, a67\n"
    "v_mul_f32 v15, v13, v15\n"
    "v_mul_f32 v16, v13, v16\n"
    "v_mul_f32 v17, v13, v17\n"
    "v_mul_f32 v18, v13, v18\n"
    "v_accvgpr_write_b32 a64, v15\n"
    "v_accvgpr_write_b32 a65, v16\n"
    "v_accvgpr_write_b32 a66, v17\n"
    "v_accvgpr_write_b32 a67, v18\n"
    "v_accvgpr_read_b32 v15, a76\n"
    "v_accvgpr_read_b32 v16, a77\n"
    "v_accvgpr_read_b32 v17, a78\n"
    "v_accvgpr_read_b32 v18, a79\n"
    "v_mul_f32 v15, v13, v15\n"
    "v_mul_f32 v16, v13, v16\n"
    "v_mul_f32 v17, v13, v17\n"
    "v_mul_f32 v18, v13, v18\n"
    "v_accvgpr_write_b32 a76, v15\n"
    "v_accvgpr_write_b32 a77, v16\n"
    "v_accvgpr_write_b32 a78, v17\n"
    "v_accvgpr_write_b32 a79, v18\n"
    "v_accvgpr_read_b32 v15, a88\n"
    "v_accvgpr_read_b32 v16, a89\n"
    "v_accvgpr_read_b32 v17, a90\n"
    "v_accvgpr_read_b32 v18, a91\n"
    "v_mul_f32 v15, v13, v15\n"
    "v_mul_f32 v16, v13, v16\n"
    "v_mul_f32 v17, v13, v17\n"
    "v_mul_f32 v18, v13, v18\n"
    "v_accvgpr_write_b32 a88, v15\n"
    "v_accvgpr_write_b32 a89, v16\n"
    "v_accvgpr_write_b32 a90, v17\n"
    "v_accvgpr_write_b32 a91, v18\n"
    "v_accvgpr_read_b32 v15, a100\n"
    "v_accvgpr_read_b32 v16, a101\n"
    "v_accvgpr_read_b32 v17, a102\n"
    "v_accvgpr_read_b32 v18, a103\n"
    "v_mul_f32 v15, v13, v15\n"
    "v_mul_f32 v16, v13, v16\n"
    "v_mul_f32 v17, v13, v17\n"
    "v_mul_f32 v18, v13, v18\n"
    "v_accvgpr_write_b32 a100, v15\n"
    "v_accvgpr_write_b32 a101, v16\n"
    "v_accvgpr_write_b32 a102, v17\n"
    "v_accvgpr_write_b32 a103, v18\n"
    "v_accvgpr_read_b32 v15, a112\n"
    "v_accvgpr_read_b32 v16, a113\n"
    "v_accvgpr_read_b32 v17, a114\n"
    "v_accvgpr_read_b32 v18, a115\n"
    "v_mul_f32 v15, v13, v15\n"
    "v_mul_f32 v16, v13, v16\n"
    "v_mul_f32 v17, v13, v17\n"
    "v_mul_f32 v18, v13, v18\n"
    "v_accvgpr_write_b32 a112, v15\n"
    "v_accvgpr_write_b32 a113, v16\n"
    "v_accvgpr_write_b32 a114, v17\n"
    "v_accvgpr_write_b32 a115, v18\n"
    "v_accvgpr_read_b32 v15, a124\n"
    "v_accvgpr_read_b32 v16, a125\n"
    "v_accvgpr_read_b32 v17, a126\n"
    "v_accvgpr_read_b32 v18, a127\n"
    "v_mul_f32 v15, v13, v15\n"
    "v_mul_f32 v16, v13, v16\n"
    "v_mul_f32 v17, v13, v17\n"
    "v_mul_f32 v18, v13, v18\n"
    "v_accvgpr_write_b32 a124, v15\n"
    "v_accvgpr_write_b32 a125, v16\n"
    "v_accvgpr_write_b32 a126, v17\n"
    "v_accvgpr_write_b32 a127, v18\n"
    "v_accvgpr_read_b32 v15, a136\n"
    "v_accvgpr_read_b32 v16, a137\n"
    "v_accvgpr_read_b32 v17, a138\n"
    "v_accvgpr_read_b32 v18, a139\n"
    "v_mul_f32 v15, v13, v15\n"
    "v_mul_f32 v16, v13, v16\n"
    "v_mul_f32 v17, v13, v17\n"
    "v_mul_f32 v18, v13, v18\n"
    "v_accvgpr_write_b32 a136, v15\n"
    "v_accvgpr_write_b32 a137, v16\n"
    "v_accvgpr_write_b32 a138, v17\n"
    "v_accvgpr_write_b32 a139, v18\n"
    "v_accvgpr_read_b32 v15, a148\n"
    "v_accvgpr_read_b32 v16, a149\n"
    "v_accvgpr_read_b32 v17, a150\n"
    "v_accvgpr_read_b32 v18, a151\n"
    "v_mul_f32 v15, v13, v15\n"
    "v_mul_f32 v16, v13, v16\n"
    "v_mul_f32 v17, v13, v17\n"
    "v_mul_f32 v18, v13, v18\n"
    "v_accvgpr_write_b32 a148, v15\n"
    "v_accvgpr_write_b32 a149, v16\n"
    "v_accvgpr_write_b32 a150, v17\n"
    "v_accvgpr_write_b32 a151, v18\n"
    "v_accvgpr_read_b32 v15, a160\n"
    "v_accvgpr_read_b32 v16, a161\n"
    "v_accvgpr_read_b32 v17, a162\n"
    "v_accvgpr_read_b32 v18, a163\n"
    "v_mul_f32 v15, v13, v15\n"
    "v_mul_f32 v16, v13, v16\n"
    "v_mul_f32 v17, v13, v17\n"
    "v_mul_f32 v18, v13, v18\n"
    "v_accvgpr_write_b32 a160, v15\n"
    "v_accvgpr_write_b32 a161, v16\n"
    "v_accvgpr_write_b32 a162, v17\n"
    "v_accvgpr_write_b32 a163, v18\n"
    "v_accvgpr_read_b32 v15, a172\n"
    "v_accvgpr_read_b32 v16, a173\n"
    "v_accvgpr_read_b32 v17, a174\n"
    "v_accvgpr_read_b32 v18, a175\n"
    "v_mul_f32 v15, v13, v15\n"
    "v_mul_f32 v16, v13, v16\n"
    "v_mul_f32 v17, v13, v17\n"
    "v_mul_f32 v18, v13, v18\n"
    "v_accvgpr_write_b32 a172, v15\n"
    "v_accvgpr_write_b32 a173, v16\n"
    "v_accvgpr_write_b32 a174, v17\n"
    "v_accvgpr_write_b32 a175, v18\n"
    "v_accvgpr_read_b32 v15, a184\n"
    "v_accvgpr_read_b32 v16, a185\n"
    "v_accvgpr_read_b32 v17, a186\n"
    "v_accvgpr_read_b32 v18, a187\n"
    "v_mul_f32 v15, v13, v15\n"
    "v_mul_f32 v16, v13, v16\n"
    "v_mul_f32 v17, v13, v17\n"
    "v_mul_f32 v18, v13, v18\n"
    "v_accvgpr_write_b32 a184, v15\n"
    "v_accvgpr_write_b32 a185, v16\n"
    "v_accvgpr_write_b32 a186, v17\n"
    "v_accvgpr_write_b32 a187, v18\n"
    "v_mul_f32 v253, v14, v253\n"
    "v_accvgpr_read_b32 v15, a8\n"
    "v_accvgpr_read_b32 v16, a9\n"
    "v_accvgpr_read_b32 v17, a10\n"
    "v_accvgpr_read_b32 v18, a11\n"
    "v_mul_f32 v15, v14, v15\n"
    "v_mul_f32 v16, v14, v16\n"
    "v_mul_f32 v17, v14, v17\n"
    "v_mul_f32 v18, v14, v18\n"
    "v_accvgpr_write_b32 a8, v15\n"
    "v_accvgpr_write_b32 a9, v16\n"
    "v_accvgpr_write_b32 a10, v17\n"
    "v_accvgpr_write_b32 a11, v18\n"
    "v_accvgpr_read_b32 v15, a20\n"
    "v_accvgpr_read_b32 v16, a21\n"
    "v_accvgpr_read_b32 v17, a22\n"
    "v_accvgpr_read_b32 v18, a23\n"
    "v_mul_f32 v15, v14, v15\n"
    "v_mul_f32 v16, v14, v16\n"
    "v_mul_f32 v17, v14, v17\n"
    "v_mul_f32 v18, v14, v18\n"
    "v_accvgpr_write_b32 a20, v15\n"
    "v_accvgpr_write_b32 a21, v16\n"
    "v_accvgpr_write_b32 a22, v17\n"
    "v_accvgpr_write_b32 a23, v18\n"
    "v_accvgpr_read_b32 v15, a32\n"
    "v_accvgpr_read_b32 v16, a33\n"
    "v_accvgpr_read_b32 v17, a34\n"
    "v_accvgpr_read_b32 v18, a35\n"
    "v_mul_f32 v15, v14, v15\n"
    "v_mul_f32 v16, v14, v16\n"
    "v_mul_f32 v17, v14, v17\n"
    "v_mul_f32 v18, v14, v18\n"
    "v_accvgpr_write_b32 a32, v15\n"
    "v_accvgpr_write_b32 a33, v16\n"
    "v_accvgpr_write_b32 a34, v17\n"
    "v_accvgpr_write_b32 a35, v18\n"
    "v_accvgpr_read_b32 v15, a44\n"
    "v_accvgpr_read_b32 v16, a45\n"
    "v_accvgpr_read_b32 v17, a46\n"
    "v_accvgpr_read_b32 v18, a47\n"
    "v_mul_f32 v15, v14, v15\n"
    "v_mul_f32 v16, v14, v16\n"
    "v_mul_f32 v17, v14, v17\n"
    "v_mul_f32 v18, v14, v18\n"
    "v_accvgpr_write_b32 a44, v15\n"
    "v_accvgpr_write_b32 a45, v16\n"
    "v_accvgpr_write_b32 a46, v17\n"
    "v_accvgpr_write_b32 a47, v18\n"
    "v_accvgpr_read_b32 v15, a56\n"
    "v_accvgpr_read_b32 v16, a57\n"
    "v_accvgpr_read_b32 v17, a58\n"
    "v_accvgpr_read_b32 v18, a59\n"
    "v_mul_f32 v15, v14, v15\n"
    "v_mul_f32 v16, v14, v16\n"
    "v_mul_f32 v17, v14, v17\n"
    "v_mul_f32 v18, v14, v18\n"
    "v_accvgpr_write_b32 a56, v15\n"
    "v_accvgpr_write_b32 a57, v16\n"
    "v_accvgpr_write_b32 a58, v17\n"
    "v_accvgpr_write_b32 a59, v18\n"
    "v_accvgpr_read_b32 v15, a68\n"
    "v_accvgpr_read_b32 v16, a69\n"
    "v_accvgpr_read_b32 v17, a70\n"
    "v_accvgpr_read_b32 v18, a71\n"
    "v_mul_f32 v15, v14, v15\n"
    "v_mul_f32 v16, v14, v16\n"
    "v_mul_f32 v17, v14, v17\n"
    "v_mul_f32 v18, v14, v18\n"
    "v_accvgpr_write_b32 a68, v15\n"
    "v_accvgpr_write_b32 a69, v16\n"
    "v_accvgpr_write_b32 a70, v17\n"
    "v_accvgpr_write_b32 a71, v18\n"
    "v_accvgpr_read_b32 v15, a80\n"
    "v_accvgpr_read_b32 v16, a81\n"
    "v_accvgpr_read_b32 v17, a82\n"
    "v_accvgpr_read_b32 v18, a83\n"
    "v_mul_f32 v15, v14, v15\n"
    "v_mul_f32 v16, v14, v16\n"
    "v_mul_f32 v17, v14, v17\n"
    "v_mul_f32 v18, v14, v18\n"
    "v_accvgpr_write_b32 a80, v15\n"
    "v_accvgpr_write_b32 a81, v16\n"
    "v_accvgpr_write_b32 a82, v17\n"
    "v_accvgpr_write_b32 a83, v18\n"
    "v_accvgpr_read_b32 v15, a92\n"
    "v_accvgpr_read_b32 v16, a93\n"
    "v_accvgpr_read_b32 v17, a94\n"
    "v_accvgpr_read_b32 v18, a95\n"
    "v_mul_f32 v15, v14, v15\n"
    "v_mul_f32 v16, v14, v16\n"
    "v_mul_f32 v17, v14, v17\n"
    "v_mul_f32 v18, v14, v18\n"
    "v_accvgpr_write_b32 a92, v15\n"
    "v_accvgpr_write_b32 a93, v16\n"
    "v_accvgpr_write_b32 a94, v17\n"
    "v_accvgpr_write_b32 a95, v18\n"
    "v_accvgpr_read_b32 v15, a104\n"
    "v_accvgpr_read_b32 v16, a105\n"
    "v_accvgpr_read_b32 v17, a106\n"
    "v_accvgpr_read_b32 v18, a107\n"
    "v_mul_f32 v15, v14, v15\n"
    "v_mul_f32 v16, v14, v16\n"
    "v_mul_f32 v17, v14, v17\n"
    "v_mul_f32 v18, v14, v18\n"
    "v_accvgpr_write_b32 a104, v15\n"
    "v_accvgpr_write_b32 a105, v16\n"
    "v_accvgpr_write_b32 a106, v17\n"
    "v_accvgpr_write_b32 a107, v18\n"
    "v_accvgpr_read_b32 v15, a116\n"
    "v_accvgpr_read_b32 v16, a117\n"
    "v_accvgpr_read_b32 v17, a118\n"
    "v_accvgpr_read_b32 v18, a119\n"
    "v_mul_f32 v15, v14, v15\n"
    "v_mul_f32 v16, v14, v16\n"
    "v_mul_f32 v17, v14, v17\n"
    "v_mul_f32 v18, v14, v18\n"
    "v_accvgpr_write_b32 a116, v15\n"
    "v_accvgpr_write_b32 a117, v16\n"
    "v_accvgpr_write_b32 a118, v17\n"
    "v_accvgpr_write_b32 a119, v18\n"
    "v_accvgpr_read_b32 v15, a128\n"
    "v_accvgpr_read_b32 v16, a129\n"
    "v_accvgpr_read_b32 v17, a130\n"
    "v_accvgpr_read_b32 v18, a131\n"
    "v_mul_f32 v15, v14, v15\n"
    "v_mul_f32 v16, v14, v16\n"
    "v_mul_f32 v17, v14, v17\n"
    "v_mul_f32 v18, v14, v18\n"
    "v_accvgpr_write_b32 a128, v15\n"
    "v_accvgpr_write_b32 a129, v16\n"
    "v_accvgpr_write_b32 a130, v17\n"
    "v_accvgpr_write_b32 a131, v18\n"
    "v_accvgpr_read_b32 v15, a140\n"
    "v_accvgpr_read_b32 v16, a141\n"
    "v_accvgpr_read_b32 v17, a142\n"
    "v_accvgpr_read_b32 v18, a143\n"
    "v_mul_f32 v15, v14, v15\n"
    "v_mul_f32 v16, v14, v16\n"
    "v_mul_f32 v17, v14, v17\n"
    "v_mul_f32 v18, v14, v18\n"
    "v_accvgpr_write_b32 a140, v15\n"
    "v_accvgpr_write_b32 a141, v16\n"
    "v_accvgpr_write_b32 a142, v17\n"
    "v_accvgpr_write_b32 a143, v18\n"
    "v_accvgpr_read_b32 v15, a152\n"
    "v_accvgpr_read_b32 v16, a153\n"
    "v_accvgpr_read_b32 v17, a154\n"
    "v_accvgpr_read_b32 v18, a155\n"
    "v_mul_f32 v15, v14, v15\n"
    "v_mul_f32 v16, v14, v16\n"
    "v_mul_f32 v17, v14, v17\n"
    "v_mul_f32 v18, v14, v18\n"
    "v_accvgpr_write_b32 a152, v15\n"
    "v_accvgpr_write_b32 a153, v16\n"
    "v_accvgpr_write_b32 a154, v17\n"
    "v_accvgpr_write_b32 a155, v18\n"
    "v_accvgpr_read_b32 v15, a164\n"
    "v_accvgpr_read_b32 v16, a165\n"
    "v_accvgpr_read_b32 v17, a166\n"
    "v_accvgpr_read_b32 v18, a167\n"
    "v_mul_f32 v15, v14, v15\n"
    "v_mul_f32 v16, v14, v16\n"
    "v_mul_f32 v17, v14, v17\n"
    "v_mul_f32 v18, v14, v18\n"
    "v_accvgpr_write_b32 a164, v15\n"
    "v_accvgpr_write_b32 a165, v16\n"
    "v_accvgpr_write_b32 a166, v17\n"
    "v_accvgpr_write_b32 a167, v18\n"
    "v_accvgpr_read_b32 v15, a176\n"
    "v_accvgpr_read_b32 v16, a177\n"
    "v_accvgpr_read_b32 v17, a178\n"
    "v_accvgpr_read_b32 v18, a179\n"
    "v_mul_f32 v15, v14, v15\n"
    "v_mul_f32 v16, v14, v16\n"
    "v_mul_f32 v17, v14, v17\n"
    "v_mul_f32 v18, v14, v18\n"
    "v_accvgpr_write_b32 a176, v15\n"
    "v_accvgpr_write_b32 a177, v16\n"
    "v_accvgpr_write_b32 a178, v17\n"
    "v_accvgpr_write_b32 a179, v18\n"
    "v_accvgpr_read_b32 v15, a188\n"
    "v_accvgpr_read_b32 v16, a189\n"
    "v_accvgpr_read_b32 v17, a190\n"
    "v_accvgpr_read_b32 v18, a191\n"
    "v_mul_f32 v15, v14, v15\n"
    "v_mul_f32 v16, v14, v16\n"
    "v_mul_f32 v17, v14, v17\n"
    "v_mul_f32 v18, v14, v18\n"
    "v_accvgpr_write_b32 a188, v15\n"
    "v_accvgpr_write_b32 a189, v16\n"
    "v_accvgpr_write_b32 a190, v17\n"
    "v_accvgpr_write_b32 a191, v18\n"
    "s_nop 1\n"
    "s_cmp_eq_u32 s62, 0\n"
    "s_cbranch_scc1 RET_P1_%=\n"
    "s_cmp_eq_u32 s62, 2\n"
    "s_cbranch_scc1 RET_L1_%=\n"
    "s_branch RET_P31_%=\n"
    "LDM_%=:\n"
    "s_add_u32 s65, s58, 128\n"
    "s_mov_b32 m0, 32768\n"
    "s_nop 0\n"
    "buffer_load_dwordx4 v48, s[44:47], s65 offen offset:0 nt lds\n"
    "buffer_load_dwordx4 v49, s[44:47], s65 offen offset:1024 nt lds\n"
    "buffer_load_dwordx4 v50, s[44:47], s65 offen offset:2048 nt lds\n"
    "buffer_load_dwordx4 v51, s[44:47], s65 offen offset:3072 nt lds\n"
    "s_mov_b32 m0, 36864\n"
    "s_nop 0\n"
    "buffer_load_dwordx4 v52, s[44:47], s65 offen offset:0 nt lds\n"
    "buffer_load_dwordx4 v53, s[44:47], s65 offen offset:1024 nt lds\n"
    "buffer_load_dwordx4 v54, s[44:47], s65 offen offset:2048 nt lds\n"
    "buffer_load_dwordx4 v55, s[44:47], s65 offen offset:3072 nt lds\n"
    "s_add_u32 s65, s58, 256\n"
    "s_mov_b32 m0, 40960\n"
    "s_nop 0\n"
    "buffer_load_dwordx4 v48, s[44:47], s65 offen offset:0 nt lds\n"
    "buffer_load_dwordx4 v49, s[44:47], s65 offen offset:1024 nt lds\n"
    "buffer_load_dwordx4 v50, s[44:47], s65 offen offset:2048 nt lds\n"
    "buffer_load_dwordx4 v51, s[44:47], s65 offen offset:3072 nt lds\n"
    "s_mov_b32 m0, 45056\n"
    "s_nop 0\n"
    "buffer_load_dwordx4 v52, s[44:47], s65 offen offset:0 nt lds\n"
    "buffer_load_dwordx4 v53, s[44:47], s65 offen offset:1024 nt lds\n"
    "buffer_load_dwordx4 v54, s[44:47], s65 offen offset:2048 nt lds\n"
    "buffer_load_dwordx4 v55, s[44:47], s65 offen offset:3072 nt lds\n"
    "s_mov_b64 s[36:37], %[kp]\n"
    "s_and_b32 s37, s37, 0xffff\n"
    "s_mov_b32 s38, 524288\n"
    "s_mov_b32 s39, 0x20000\n"
    "s_mov_b64 s[40:41], %[vp]\n"
    "s_and_b32 s41, s41, 0xffff\n"
    "s_mov_b32 s42, 524288\n"
    "s_mov_b32 s43, 0x20000\n"
    "s_mov_b64 s[48:49], %[wo]\n"
    "s_and_b32 s49, s49, 0xffff\n"
    "s_mov_b32 s50, 229376\n"
    "s_mov_b32 s51, 0x20000\n"
    "s_load_dword s95, %[kp], 0x0\n"
    "s_load_dword s95, %[vp], 0x0\n"
    "s_load_dword s95, %[kp], 0x7ff00\n"
    "s_load_dword s95, %[vp], 0x7ff00\n"
    "s_load_dword s95, %[wo], 0x0\n"
    "s_load_dword s95, %[wo], 0x37f00\n"
    "s_load_dword s95, %[fp], 0x3000\n"
    "v_and_b32 v2, 7, v1\n"
    "v_bfe_u32 v3, v1, 4, 2\n"
    "v_lshlrev_b32 v3, 1, v3\n"
    "v_xor_b32 v5, v2, v3\n"
    "v_lshlrev_b32 v9, 4, v1\n"
    "v_lshlrev_b32 v2, 9, v4\n"
    "v_lshl_add_u32 v2, v5, 4, v2\n"
    "v_add_u32 v16, 0, v2\n"
    "v_add_u32 v17, 3072, v2\n"
    "v_add_u32 v18, 6144, v2\n"
    "v_add_u32 v19, 9216, v2\n"
    "v_add_u32 v20, 128, v2\n"
    "v_add_u32 v21, 3200, v2\n"
    "v_add_u32 v22, 6272, v2\n"
    "v_add_u32 v23, 9344, v2\n"
    "v_add_u32 v24, 256, v2\n"
    "v_add_u32 v25, 3328, v2\n"
    "v_add_u32 v26, 6400, v2\n"
    "v_add_u32 v27, 9472, v2\n"
    "v_add_u32 v28, 384, v2\n"
    "v_add_u32 v29, 3456, v2\n"
    "v_add_u32 v30, 6528, v2\n"
    "v_add_u32 v31, 9600, v2\n"
    "v_mul_u32_u24 v2, 896, v4\n"
    "v_lshl_add_u32 v2, v5, 4, v2\n"
    "v_add_u32 v32, 0, v2\n"
    "v_add_u32 v33, 6144, v2\n"
    "v_add_u32 v34, 12288, v2\n"
    "v_add_u32 v35, 18432, v2\n"
    "v_add_u32 v36, 28672, v2\n"
    "v_add_u32 v37, 34816, v2\n"
    "v_add_u32 v38, 40960, v2\n"
    "v_add_u32 v39, 47104, v2\n"
    "v_add_u32 v40, 57344, v2\n"
    "v_add_u32 v41, 63488, v2\n"
    "v_add_u32 v42, 69632, v2\n"
    "v_add_u32 v43, 75776, v2\n"
    "v_add_u32 v44, 86016, v2\n"
    "v_add_u32 v45, 92160, v2\n"
    "v_add_u32 v46, 98304, v2\n"
    "v_add_u32 v47, 104448, v2\n"
    "s_cmp_lt_u32 %[slot], 16\n"
    "s_cselect_b32 s69, 32, 32\n"
    "s_add_u32 s69, s69, 57\n"
    "s_waitcnt vmcnt(33)\n"
    "s_barrier\n"
    "s_add_u32 s65, s59, 0\n"
    "s_mov_b32 m0, 49152\n"
    "s_nop 0\n"
    "buffer_load_dwordx4 v48, s[44:47], s65 offen offset:0 nt lds\n"
    "buffer_load_dwordx4 v49, s[44:47], s65 offen offset:1024 nt lds\n"
    "buffer_load_dwordx4 v50, s[44:47], s65 offen offset:2048 nt lds\n"
    "buffer_load_dwordx4 v51, s[44:47], s65 offen offset:3072 nt lds\n"
    "s_mov_b32 m0, 53248\n"
    "s_nop 0\n"
    "buffer_load_dwordx4 v52, s[44:47], s65 offen offset:0 nt lds\n"
    "buffer_load_dwordx4 v53, s[44:47], s65 offen offset:1024 nt lds\n"
    "buffer_load_dwordx4 v54, s[44:47], s65 offen offset:2048 nt lds\n"
    "buffer_load_dwordx4 v55, s[44:47], s65 offen offset:3072 nt lds\n"
    "s_add_u32 s65, s59, 128\n"
    "s_mov_b32 m0, 57344\n"
    "s_nop 0\n"
    "buffer_load_dwordx4 v48, s[44:47], s65 offen offset:0 nt lds\n"
    "buffer_load_dwordx4 v49, s[44:47], s65 offen offset:1024 nt lds\n"
    "buffer_load_dwordx4 v50, s[44:47], s65 offen offset:2048 nt lds\n"
    "buffer_load_dwordx4 v51, s[44:47], s65 offen offset:3072 nt lds\n"
    "s_mov_b32 m0, 61440\n"
    "s_nop 0\n"
    "buffer_load_dwordx4 v52, s[44:47], s65 offen offset:0 nt lds\n"
    "buffer_load_dwordx4 v53, s[44:47], s65 offen offset:1024 nt lds\n"
    "buffer_load_dwordx4 v54, s[44:47], s65 offen offset:2048 nt lds\n"
    "buffer_load_dwordx4 v55, s[44:47], s65 offen offset:3072 nt lds\n"
    "s_waitcnt vmcnt(33)\n"
    "s_barrier\n"
    "s_add_u32 s65, s59, 256\n"
    "s_mov_b32 m0, 65536\n"
    "s_nop 0\n"
    "buffer_load_dwordx4 v48, s[44:47], s65 offen offset:0 nt lds\n"
    "buffer_load_dwordx4 v49, s[44:47], s65 offen offset:1024 nt lds\n"
    "buffer_load_dwordx4 v50, s[44:47], s65 offen offset:2048 nt lds\n"
    "buffer_load_dwordx4 v51, s[44:47], s65 offen offset:3072 nt lds\n"
    "s_mov_b32 m0, 69632\n"
    "s_nop 0\n"
    "buffer_load_dwordx4 v52, s[44:47], s65 offen offset:0 nt lds\n"
    "buffer_load_dwordx4 v53, s[44:47], s65 offen offset:1024 nt lds\n"
    "buffer_load_dwordx4 v54, s[44:47], s65 offen offset:2048 nt lds\n"
    "buffer_load_dwordx4 v55, s[44:47], s65 offen offset:3072 nt lds\n"
    "s_add_u32 s65, s60, 0\n"
    "s_mov_b32 m0, 73728\n"
    "s_nop 0\n"
    "buffer_load_dwordx4 v48, s[44:47], s65 offen offset:0 nt lds\n"
    "buffer_load_dwordx4 v49, s[44:47], s65 offen offset:1024 nt lds\n"
    "buffer_load_dwordx4 v50, s[44:47], s65 offen offset:2048 nt lds\n"
    "buffer_load_dwordx4 v51, s[44:47], s65 offen offset:3072 nt lds\n"
    "s_mov_b32 m0, 77824\n"
    "s_nop 0\n"
    "buffer_load_dwordx4 v52, s[44:47], s65 offen offset:0 nt lds\n"
    "buffer_load_dwordx4 v53, s[44:47], s65 offen offset:1024 nt lds\n"
    "buffer_load_dwordx4 v54, s[44:47], s65 offen offset:2048 nt lds\n"
    "buffer_load_dwordx4 v55, s[44:47], s65 offen offset:3072 nt lds\n"
    "s_add_u32 s65, s60, 128\n"
    "s_mov_b32 m0, 81920\n"
    "s_nop 0\n"
    "buffer_load_dwordx4 v48, s[44:47], s65 offen offset:0 nt lds\n"
    "buffer_load_dwordx4 v49, s[44:47], s65 offen offset:1024 nt lds\n"
    "buffer_load_dwordx4 v50, s[44:47], s65 offen offset:2048 nt lds\n"
    "buffer_load_dwordx4 v51, s[44:47], s65 offen offset:3072 nt lds\n"
    "s_mov_b32 m0, 86016\n"
    "s_nop 0\n"
    "buffer_load_dwordx4 v52, s[44:47], s65 offen offset:0 nt lds\n"
    "buffer_load_dwordx4 v53, s[44:47], s65 offen offset:1024 nt lds\n"
    "buffer_load_dwordx4 v54, s[44:47], s65 offen offset:2048 nt lds\n"
    "buffer_load_dwordx4 v55, s[44:47], s65 offen offset:3072 nt lds\n"
    "s_add_u32 s65, s60, 256\n"
    "s_mov_b32 m0, 90112\n"
    "s_nop 0\n"
    "buffer_load_dwordx4 v48, s[44:47], s65 offen offset:0 nt lds\n"
    "buffer_load_dwordx4 v49, s[44:47], s65 offen offset:1024 nt lds\n"
    "buffer_load_dwordx4 v50, s[44:47], s65 offen offset:2048 nt lds\n"
    "buffer_load_dwordx4 v51, s[44:47], s65 offen offset:3072 nt lds\n"
    "s_mov_b32 m0, 94208\n"
    "s_waitcnt vmcnt(59)\n"
    "buffer_load_dwordx4 v52, s[44:47], s65 offen offset:0 nt lds\n"
    "buffer_load_dwordx4 v53, s[44:47], s65 offen offset:1024 nt lds\n"
    "buffer_load_dwordx4 v54, s[44:47], s65 offen offset:2048 nt lds\n"
    "buffer_load_dwordx4 v55, s[44:47], s65 offen offset:3072 nt lds\n"
    "s_waitcnt vmcnt(48)\n"
    "s_barrier\n"
    "s_add_u32 s65, s61, 0\n"
    "s_mov_b32 m0, 98304\n"
    "s_nop 0\n"
    "buffer_load_dwordx4 v48, s[44:47], s65 offen offset:0 nt lds\n"
    "buffer_load_dwordx4 v49, s[44:47], s65 offen offset:1024 nt lds\n"
    "buffer_load_dwordx4 v50, s[44:47], s65 offen offset:2048 nt lds\n"
    "buffer_load_dwordx4 v51, s[44:47], s65 offen offset:3072 nt lds\n"
    "s_mov_b32 m0, 102400\n"
    "s_nop 0\n"
    "buffer_load_dwordx4 v52, s[44:47], s65 offen offset:0 nt lds\n"
    "buffer_load_dwordx4 v53, s[44:47], s65 offen offset:1024 nt lds\n"
    "buffer_load_dwordx4 v54, s[44:47], s65 offen offset:2048 nt lds\n"
    "buffer_load_dwordx4 v55, s[44:47], s65 offen offset:3072 nt lds\n"
    "s_add_u32 s65, s61, 128\n"
    "s_mov_b32 m0, 106496\n"
    "s_nop 0\n"
    "buffer_load_dwordx4 v48, s[44:47], s65 offen offset:0 nt lds\n"
    "buffer_load_dwordx4 v49, s[44:47], s65 offen offset:1024 nt lds\n"
    "buffer_load_dwordx4 v50, s[44:47], s65 offen offset:2048 nt lds\n"
    "buffer_load_dwordx4 v51, s[44:47], s65 offen offset:3072 nt lds\n"
    "s_mov_b32 m0, 110592\n"
    "s_waitcnt vmcnt(59)\n"
    "buffer_load_dwordx4 v52, s[44:47], s65 offen offset:0 nt lds\n"
    "buffer_load_dwordx4 v53, s[44:47], s65 offen offset:1024 nt lds\n"
    "buffer_load_dwordx4 v54, s[44:47], s65 offen offset:2048 nt lds\n"
    "buffer_load_dwordx4 v55, s[44:47], s65 offen offset:3072 nt lds\n"
    "s_waitcnt vmcnt(48)\n"
    "s_barrier\n"
    "s_add_u32 s65, s61, 256\n"
    "s_mov_b32 m0, 0\n"
    "s_nop 0\n"
    "buffer_load_dwordx4 v48, s[44:47], s65 offen offset:0 nt lds\n"
    "buffer_load_dwordx4 v49, s[44:47], s65 offen offset:1024 nt lds\n"
    "buffer_load_dwordx4 v50, s[44:47], s65 offen offset:2048 nt lds\n"
    "buffer_load_dwordx4 v51, s[44:47], s65 offen offset:3072 nt lds\n"
    "s_mov_b32 m0, 4096\n"
    "s_nop 0\n"
    "buffer_load_dwordx4 v52, s[44:47], s65 offen offset:0 nt lds\n"
    "buffer_load_dwordx4 v53, s[44:47], s65 offen offset:1024 nt lds\n"
    "buffer_load_dwordx4 v54, s[44:47], s65 offen offset:2048 nt lds\n"
    "buffer_load_dwordx4 v55, s[44:47], s65 offen offset:3072 nt lds\n"
    "s_add_u32 s65, s62, 0\n"
    "s_mov_b32 m0, 8192\n"
    "s_nop 0\n"
    "buffer_load_dwordx4 v48, s[44:47], s65 offen offset:0 nt lds\n"
    "buffer_load_dwordx4 v49, s[44:47], s65 offen offset:1024 nt lds\n"
    "buffer_load_dwordx4 v50, s[44:47], s65 offen offset:2048 nt lds\n"
    "buffer_load_dwordx4 v51, s[44:47], s65 offen offset:3072 nt lds\n"
    "s_mov_b32 m0, 12288\n"
    "s_waitcnt vmcnt(59)\n"
    "buffer_load_dwordx4 v52, s[44:47], s65 offen offset:0 nt lds\n"
    "buffer_load_dwordx4 v53, s[44:47], s65 offen offset:1024 nt lds\n"
    "buffer_load_dwordx4 v54, s[44:47], s65 offen offset:2048 nt lds\n"
    "buffer_load_dwordx4 v55, s[44:47], s65 offen offset:3072 nt lds\n"
    "s_waitcnt vmcnt(48)\n"
    "s_barrier\n"
    "s_add_u32 s65, s62, 128\n"
    "s_mov_b32 m0, 16384\n"
    "s_nop 0\n"
    "buffer_load_dwordx4 v48, s[44:47], s65 offen offset:0 nt lds\n"
    "buffer_load_dwordx4 v49, s[44:47], s65 offen offset:1024 nt lds\n"
    "buffer_load_dwordx4 v50, s[44:47], s65 offen offset:2048 nt lds\n"
    "buffer_load_dwordx4 v51, s[44:47], s65 offen offset:3072 nt lds\n"
    "s_mov_b32 m0, 20480\n"
    "s_nop 0\n"
    "buffer_load_dwordx4 v52, s[44:47], s65 offen offset:0 nt lds\n"
    "buffer_load_dwordx4 v53, s[44:47], s65 offen offset:1024 nt lds\n"
    "buffer_load_dwordx4 v54, s[44:47], s65 offen offset:2048 nt lds\n"
    "buffer_load_dwordx4 v55, s[44:47], s65 offen offset:3072 nt lds\n"
    "s_add_u32 s65, s62, 256\n"
    "s_mov_b32 m0, 24576\n"
    "s_nop 0\n"
    "buffer_load_dwordx4 v48, s[44:47], s65 offen offset:0 nt lds\n"
    "buffer_load_dwordx4 v49, s[44:47], s65 offen offset:1024 nt lds\n"
    "buffer_load_dwordx4 v50, s[44:47], s65 offen offset:2048 nt lds\n"
    "buffer_load_dwordx4 v51, s[44:47], s65 offen offset:3072 nt lds\n"
    "s_mov_b32 m0, 28672\n"
    "s_waitcnt vmcnt(59)\n"
    "buffer_load_dwordx4 v52, s[44:47], s65 offen offset:0 nt lds\n"
    "buffer_load_dwordx4 v53, s[44:47], s65 offen offset:1024 nt lds\n"
    "buffer_load_dwordx4 v54, s[44:47], s65 offen offset:2048 nt lds\n"
    "buffer_load_dwordx4 v55, s[44:47], s65 offen offset:3072 nt lds\n"
    "s_waitcnt vmcnt(48)\n"
    "s_barrier\n"
    "s_add_u32 s65, s63, 0\n"
    "s_mov_b32 m0, 32768\n"
    "s_nop 0\n"
    "buffer_load_dwordx4 v48, s[44:47], s65 offen offset:0 nt lds\n"
    "buffer_load_dwordx4 v49, s[44:47], s65 offen offset:1024 nt lds\n"
    "buffer_load_dwordx4 v50, s[44:47], s65 offen offset:2048 nt lds\n"
    "buffer_load_dwordx4 v51, s[44:47], s65 offen offset:3072 nt lds\n"
    "s_mov_b32 m0, 36864\n"
    "s_nop 0\n"
    "buffer_load_dwordx4 v52, s[44:47], s65 offen offset:0 nt lds\n"
    "buffer_load_dwordx4 v53, s[44:47], s65 offen offset:1024 nt lds\n"
    "buffer_load_dwordx4 v54, s[44:47], s65 offen offset:2048 nt lds\n"
    "buffer_load_dwordx4 v55, s[44:47], s65 offen offset:3072 nt lds\n"
    "s_add_u32 s65, s63, 128\n"
    "s_mov_b32 m0, 40960\n"
    "s_nop 0\n"
    "buffer_load_dwordx4 v48, s[44:47], s65 offen offset:0 nt lds\n"
    "buffer_load_dwordx4 v49, s[44:47], s65 offen offset:1024 nt lds\n"
    "buffer_load_dwordx4 v50, s[44:47], s65 offen offset:2048 nt lds\n"
    "buffer_load_dwordx4 v51, s[44:47], s65 offen offset:3072 nt lds\n"
    "s_mov_b32 m0, 45056\n"
    "s_waitcnt vmcnt(59)\n"
    "buffer_load_dwordx4 v52, s[44:47], s65 offen offset:0 nt lds\n"
    "buffer_load_dwordx4 v53, s[44:47], s65 offen offset:1024 nt lds\n"
    "buffer_load_dwordx4 v54, s[44:47], s65 offen offset:2048 nt lds\n"
    "buffer_load_dwordx4 v55, s[44:47], s65 offen offset:3072 nt lds\n"
    "s_waitcnt vmcnt(48)\n"
    "s_barrier\n"
    "s_add_u32 s65, s63, 256\n"
    "s_mov_b32 m0, 49152\n"
    "s_nop 0\n"
    "buffer_load_dwordx4 v48, s[44:47], s65 offen offset:0 nt lds\n"
    "buffer_load_dwordx4 v49, s[44:47], s65 offen offset:1024 nt lds\n"
    "buffer_load_dwordx4 v50, s[44:47], s65 offen offset:2048 nt lds\n"
    "buffer_load_dwordx4 v51, s[44:47], s65 offen offset:3072 nt lds\n"
    "s_mov_b32 m0, 53248\n"
    "s_nop 0\n"
    "buffer_load_dwordx4 v52, s[44:47], s65 offen offset:0 nt lds\n"
    "buffer_load_dwordx4 v53, s[44:47], s65 offen offset:1024 nt lds\n"
    "buffer_load_dwordx4 v54, s[44:47], s65 offen offset:2048 nt lds\n"
    "buffer_load_dwordx4 v55, s[44:47], s65 offen offset:3072 nt lds\n"
    "s_add_u32 s65, s57, 384\n"
    "s_mov_b32 m0, 57344\n"
    "s_nop 0\n"
    "buffer_load_dwordx4 v48, s[44:47], s65 offen offset:0 nt lds\n"
    "buffer_load_dwordx4 v49, s[44:47], s65 offen offset:1024 nt lds\n"
    "buffer_load_dwordx4 v50, s[44:47], s65 offen offset:2048 nt lds\n"
    "buffer_load_dwordx4 v51, s[44:47], s65 offen offset:3072 nt lds\n"
    "s_mov_b32 m0, 61440\n"
    "s_waitcnt vmcnt(59)\n"
    "buffer_load_dwordx4 v52, s[44:47], s65 offen offset:0 nt lds\n"
    "buffer_load_dwordx4 v53, s[44:47], s65 offen offset:1024 nt lds\n"
    "buffer_load_dwordx4 v54, s[44:47], s65 offen offset:2048 nt lds\n"
    "buffer_load_dwordx4 v55, s[44:47], s65 offen offset:3072 nt lds\n"
    "s_waitcnt vmcnt(48)\n"
    "s_barrier\n"
    "s_add_u32 s65, s58, 384\n"
    "s_mov_b32 m0, 65536\n"
    "s_nop 0\n"
    "buffer_load_dwordx4 v48, s[44:47], s65 offen offset:0 nt lds\n"
    "buffer_load_dwordx4 v49, s[44:47], s65 offen offset:1024 nt lds\n"
    "buffer_load_dwordx4 v50, s[44:47], s65 offen offset:2048 nt lds\n"
    "buffer_load_dwordx4 v51, s[44:47], s65 offen offset:3072 nt lds\n"
    "s_mov_b32 m0, 69632\n"
    "s_nop 0\n"
    "buffer_load_dwordx4 v52, s[44:47], s65 offen offset:0 nt lds\n"
    "buffer_load_dwordx4 v53, s[44:47], s65 offen offset:1024 nt lds\n"
    "buffer_load_dwordx4 v54, s[44:47], s65 offen offset:2048 nt lds\n"
    "buffer_load_dwordx4 v55, s[44:47], s65 offen offset:3072 nt lds\n"
    "s_add_u32 s65, s59, 384\n"
    "s_mov_b32 m0, 73728\n"
    "s_nop 0\n"
    "buffer_load_dwordx4 v48, s[44:47], s65 offen offset:0 nt lds\n"
    "buffer_load_dwordx4 v49, s[44:47], s65 offen offset:1024 nt lds\n"
    "buffer_load_dwordx4 v50, s[44:47], s65 offen offset:2048 nt lds\n"
    "buffer_load_dwordx4 v51, s[44:47], s65 offen offset:3072 nt lds\n"
    "s_mov_b32 m0, 77824\n"
    "s_waitcnt vmcnt(59)\n"
    "buffer_load_dwordx4 v52, s[44:47], s65 offen offset:0 nt lds\n"
    "buffer_load_dwordx4 v53, s[44:47], s65 offen offset:1024 nt lds\n"
    "buffer_load_dwordx4 v54, s[44:47], s65 offen offset:2048 nt lds\n"
    "buffer_load_dwordx4 v55, s[44:47], s65 offen offset:3072 nt lds\n"
    "s_waitcnt vmcnt(48)\n"
    "s_barrier\n"
    "s_add_u32 s65, s60, 384\n"
    "s_mov_b32 m0, 81920\n"
    "s_nop 0\n"
    "buffer_load_dwordx4 v48, s[44:47], s65 offen offset:0 nt lds\n"
    "buffer_load_dwordx4 v49, s[44:47], s65 offen offset:1024 nt lds\n"
    "buffer_load_dwordx4 v50, s[44:47], s65 offen offset:2048 nt lds\n"
    "buffer_load_dwordx4 v51, s[44:47], s65 offen offset:3072 nt lds\n"
    "s_mov_b32 m0, 86016\n"
    "s_nop 0\n"
    "buffer_load_dwordx4 v52, s[44:47], s65 offen offset:0 nt lds\n"
    "buffer_load_dwordx4 v53, s[44:47], s65 offen offset:1024 nt lds\n"
    "buffer_load_dwordx4 v54, s[44:47], s65 offen offset:2048 nt lds\n"
    "buffer_load_dwordx4 v55, s[44:47], s65 offen offset:3072 nt lds\n"
    "s_add_u32 s65, s61, 384\n"
    "s_mov_b32 m0, 90112\n"
    "s_nop 0\n"
    "buffer_load_dwordx4 v48, s[44:47], s65 offen offset:0 nt lds\n"
    "buffer_load_dwordx4 v49, s[44:47], s65 offen offset:1024 nt lds\n"
    "buffer_load_dwordx4 v50, s[44:47], s65 offen offset:2048 nt lds\n"
    "buffer_load_dwordx4 v51, s[44:47], s65 offen offset:3072 nt lds\n"
    "s_mov_b32 m0, 94208\n"
    "s_waitcnt vmcnt(59)\n"
    "buffer_load_dwordx4 v52, s[44:47], s65 offen offset:0 nt lds\n"
    "buffer_load_dwordx4 v53, s[44:47], s65 offen offset:1024 nt lds\n"
    "buffer_load_dwordx4 v54, s[44:47], s65 offen offset:2048 nt lds\n"
    "buffer_load_dwordx4 v55, s[44:47], s65 offen offset:3072 nt lds\n"
    "s_waitcnt vmcnt(48)\n"
    "s_barrier\n"
    "s_add_u32 s65, s62, 384\n"
    "s_mov_b32 m0, 98304\n"
    "s_nop 0\n"
    "buffer_load_dwordx4 v48, s[44:47], s65 offen offset:0 nt lds\n"
    "buffer_load_dwordx4 v49, s[44:47], s65 offen offset:1024 nt lds\n"
    "buffer_load_dwordx4 v50, s[44:47], s65 offen offset:2048 nt lds\n"
    "buffer_load_dwordx4 v51, s[44:47], s65 offen offset:3072 nt lds\n"
    "s_mov_b32 m0, 102400\n"
    "s_nop 0\n"
    "buffer_load_dwordx4 v52, s[44:47], s65 offen offset:0 nt lds\n"
    "buffer_load_dwordx4 v53, s[44:47], s65 offen offset:1024 nt lds\n"
    "buffer_load_dwordx4 v54, s[44:47], s65 offen offset:2048 nt lds\n"
    "buffer_load_dwordx4 v55, s[44:47], s65 offen offset:3072 nt lds\n"
    "s_add_u32 s65, s63, 384\n"
    "s_mov_b32 m0, 106496\n"
    "s_nop 0\n"
    "buffer_load_dwordx4 v48, s[44:47], s65 offen offset:0 nt lds\n"
    "buffer_load_dwordx4 v49, s[44:47], s65 offen offset:1024 nt lds\n"
    "buffer_load_dwordx4 v50, s[44:47], s65 offen offset:2048 nt lds\n"
    "buffer_load_dwordx4 v51, s[44:47], s65 offen offset:3072 nt lds\n"
    "s_mov_b32 m0, 110592\n"
    "s_waitcnt vmcnt(59)\n"
    "buffer_load_dwordx4 v52, s[44:47], s65 offen offset:0 nt lds\n"
    "buffer_load_dwordx4 v53, s[44:47], s65 offen offset:1024 nt lds\n"
    "buffer_load_dwordx4 v54, s[44:47], s65 offen offset:2048 nt lds\n"
    "buffer_load_dwordx4 v55, s[44:47], s65 offen offset:3072 nt lds\n"
    "s_waitcnt vmcnt(48)\n"
    "s_barrier\n"
    "s_mov_b32 s65, 0\n"
    "s_mov_b32 m0, 114688\n"
    "s_nop 0\n"
    "buffer_load_dwordx4 v16, s[36:39], s65 offen offset:0 nt lds\n"
    "buffer_load_dwordx4 v17, s[36:39], s65 offen offset:1024 nt lds\n"
    "buffer_load_dwordx4 v18, s[36:39], s65 offen offset:2048 nt lds\n"
    "buffer_load_dwordx4 v19, s[36:39], s65 offen offset:3072 nt lds\n"
    "s_mov_b32 m0, 118784\n"
    "s_nop 0\n"
    "buffer_load_dwordx4 v20, s[36:39], s65 offen offset:0 nt lds\n"
    "buffer_load_dwordx4 v21, s[36:39], s65 offen offset:1024 nt lds\n"
    "buffer_load_dwordx4 v22, s[36:39], s65 offen offset:2048 nt lds\n"
    "buffer_load_dwordx4 v23, s[36:39], s65 offen offset:3072 nt lds\n"
    "s_mov_b32 m0, 122880\n"
    "s_nop 0\n"
    "buffer_load_dwordx4 v24, s[36:39], s65 offen offset:0 nt lds\n"
    "buffer_load_dwordx4 v25, s[36:39], s65 offen offset:1024 nt lds\n"
    "buffer_load_dwordx4 v26, s[36:39], s65 offen offset:2048 nt lds\n"
    "buffer_load_dwordx4 v27, s[36:39], s65 offen offset:3072 nt lds\n"
    "s_mov_b32 m0, 126976\n"
    "s_waitcnt vmcnt(59)\n"
    "buffer_load_dwordx4 v28, s[36:39], s65 offen offset:0 nt lds\n"
    "buffer_load_dwordx4 v29, s[36:39], s65 offen offset:1024 nt lds\n"
    "buffer_load_dwordx4 v30, s[36:39], s65 offen offset:2048 nt lds\n"
    "buffer_load_dwordx4 v31, s[36:39], s65 offen offset:3072 nt lds\n"
    "s_waitcnt vmcnt(48)\n"
    "s_barrier\n"
    "s_mov_b32 s65, 16384\n"
    "s_mov_b32 m0, 131072\n"
    "s_nop 0\n"
    "buffer_load_dwordx4 v16, s[36:39], s65 offen offset:0 nt lds\n"
    "buffer_load_dwordx4 v17, s[36:39], s65 offen offset:1024 nt lds\n"
    "buffer_load_dwordx4 v18, s[36:39], s65 offen offset:2048 nt lds\n"
    "buffer_load_dwordx4 v19, s[36:39], s65 offen offset:3072 nt lds\n"
    "s_mov_b32 m0, 135168\n"
    "s_nop 0\n"
    "buffer_load_dwordx4 v20, s[36:39], s65 offen offset:0 nt lds\n"
    "buffer_load_dwordx4 v21, s[36:39], s65 offen offset:1024 nt lds\n"
    "buffer_load_dwordx4 v22, s[36:39], s65 offen offset:2048 nt lds\n"
    "buffer_load_dwordx4 v23, s[36:39], s65 offen offset:3072 nt lds\n"
    "s_mov_b32 m0, 139264\n"
    "s_nop 0\n"
    "buffer_load_dwordx4 v24, s[36:39], s65 offen offset:0 nt lds\n"
    "buffer_load_dwordx4 v25, s[36:39], s65 offen offset:1024 nt lds\n"
    "buffer_load_dwordx4 v26, s[36:39], s65 offen offset:2048 nt lds\n"
    "buffer_load_dwordx4 v27, s[36:39], s65 offen offset:3072 nt lds\n"
    "s_mov_b32 m0, 143360\n"
    "s_waitcnt vmcnt(59)\n"
    "buffer_load_dwordx4 v28, s[36:39], s65 offen offset:0 nt lds\n"
    "buffer_load_dwordx4 v29, s[36:39], s65 offen offset:1024 nt lds\n"
    "buffer_load_dwordx4 v30, s[36:39], s65 offen offset:2048 nt lds\n"
    "buffer_load_dwordx4 v31, s[36:39], s65 offen offset:3072 nt lds\n"
    "s_waitcnt vmcnt(48)\n"
    "s_barrier\n"
    "s_mov_b32 s65, 0\n"
    "s_mov_b32 m0, 147456\n"
    "s_nop 0\n"
    "buffer_load_dwordx4 v16, s[40:43], s65 offen offset:0 nt lds\n"
    "buffer_load_dwordx4 v17, s[40:43], s65 offen offset:1024 nt lds\n"
    "buffer_load_dwordx4 v18, s[40:43], s65 offen offset:2048 nt lds\n"
    "buffer_load_dwordx4 v19, s[40:43], s65 offen offset:3072 nt lds\n"
    "s_mov_b32 m0, 151552\n"
    "s_nop 0\n"
    "buffer_load_dwordx4 v20, s[40:43], s65 offen offset:0 nt lds\n"
    "buffer_load_dwordx4 v21, s[40:43], s65 offen offset:1024 nt lds\n"
    "buffer_load_dwordx4 v22, s[40:43], s65 offen offset:2048 nt lds\n"
    "buffer_load_dwordx4 v23, s[40:43], s65 offen offset:3072 nt lds\n"
    "s_mov_b32 m0, 155648\n"
    "s_nop 0\n"
    "buffer_load_dwordx4 v24, s[40:43], s65 offen offset:0 nt lds\n"
    "buffer_load_dwordx4 v25, s[40:43], s65 offen offset:1024 nt lds\n"
    "buffer_load_dwordx4 v26, s[40:43], s65 offen offset:2048 nt lds\n"
    "buffer_load_dwordx4 v27, s[40:43], s65 offen offset:3072 nt lds\n"
    "s_mov_b32 m0, 159744\n"
    "s_waitcnt vmcnt(59)\n"
    "buffer_load_dwordx4 v28, s[40:43], s65 offen offset:0 nt lds\n"
    "buffer_load_dwordx4 v29, s[40:43], s65 offen offset:1024 nt lds\n"
    "buffer_load_dwordx4 v30, s[40:43], s65 offen offset:2048 nt lds\n"
    "buffer_load_dwordx4 v31, s[40:43], s65 offen offset:3072 nt lds\n"
    "s_waitcnt vmcnt(48)\n"
    "s_barrier\n"
    "s_mov_b32 s65, 32768\n"
    "s_mov_b32 m0, 0\n"
    "s_nop 0\n"
    "buffer_load_dwordx4 v16, s[36:39], s65 offen offset:0 nt lds\n"
    "buffer_load_dwordx4 v17, s[36:39], s65 offen offset:1024 nt lds\n"
    "buffer_load_dwordx4 v18, s[36:39], s65 offen offset:2048 nt lds\n"
    "buffer_load_dwordx4 v19, s[36:39], s65 offen offset:3072 nt lds\n"
    "s_mov_b32 m0, 4096\n"
    "s_nop 0\n"
    "buffer_load_dwordx4 v20, s[36:39], s65 offen offset:0 nt lds\n"
    "buffer_load_dwordx4 v21, s[36:39], s65 offen offset:1024 nt lds\n"
    "buffer_load_dwordx4 v22, s[36:39], s65 offen offset:2048 nt lds\n"
    "buffer_load_dwordx4 v23, s[36:39], s65 offen offset:3072 nt lds\n"
    "s_mov_b32 m0, 8192\n"
    "s_nop 0\n"
    "buffer_load_dwordx4 v24, s[36:39], s65 offen offset:0 nt lds\n"
    "buffer_load_dwordx4 v25, s[36:39], s65 offen offset:1024 nt lds\n"
    "buffer_load_dwordx4 v26, s[36:39], s65 offen offset:2048 nt lds\n"
    "buffer_load_dwordx4 v27, s[36:39], s65 offen offset:3072 nt lds\n"
    "s_mov_b32 m0, 12288\n"
    "s_waitcnt vmcnt(59)\n"
    "buffer_load_dwordx4 v28, s[36:39], s65 offen offset:0 nt lds\n"
    "buffer_load_dwordx4 v29, s[36:39], s65 offen offset:1024 nt lds\n"
    "buffer_load_dwordx4 v30, s[36:39], s65 offen offset:2048 nt lds\n"
    "buffer_load_dwordx4 v31, s[36:39], s65 offen offset:3072 nt lds\n"
    "s_waitcnt vmcnt(48)\n"
    "s_barrier\n"
    "s_mov_b32 s65, 16384\n"
    "s_mov_b32 m0, 16384\n"
    "s_nop 0\n"
    "buffer_load_dwordx4 v16, s[40:43], s65 offen offset:0 nt lds\n"
    "buffer_load_dwordx4 v17, s[40:43], s65 offen offset:1024 nt lds\n"
    "buffer_load_dwordx4 v18, s[40:43], s65 offen offset:2048 nt lds\n"
    "buffer_load_dwordx4 v19, s[40:43], s65 offen offset:3072 nt lds\n"
    "s_mov_b32 m0, 20480\n"
    "s_nop 0\n"
    "buffer_load_dwordx4 v20, s[40:43], s65 offen offset:0 nt lds\n"
    "buffer_load_dwordx4 v21, s[40:43], s65 offen offset:1024 nt lds\n"
    "buffer_load_dwordx4 v22, s[40:43], s65 offen offset:2048 nt lds\n"
    "buffer_load_dwordx4 v23, s[40:43], s65 offen offset:3072 nt lds\n"
    "s_mov_b32 m0, 24576\n"
    "s_nop 0\n"
    "buffer_load_dwordx4 v24, s[40:43], s65 offen offset:0 nt lds\n"
    "buffer_load_dwordx4 v25, s[40:43], s65 offen offset:1024 nt lds\n"
    "buffer_load_dwordx4 v26, s[40:43], s65 offen offset:2048 nt lds\n"
    "buffer_load_dwordx4 v27, s[40:43], s65 offen offset:3072 nt lds\n"
    "s_mov_b32 m0, 28672\n"
    "s_waitcnt vmcnt(59)\n"
    "buffer_load_dwordx4 v28, s[40:43], s65 offen offset:0 nt lds\n"
    "buffer_load_dwordx4 v29, s[40:43], s65 offen offset:1024 nt lds\n"
    "buffer_load_dwordx4 v30, s[40:43], s65 offen offset:2048 nt lds\n"
    "buffer_load_dwordx4 v31, s[40:43], s65 offen offset:3072 nt lds\n"
    "s_waitcnt vmcnt(48)\n"
    "PC15_%=:\n"
    "s_memrealtime s[66:67]\n"
    "s_waitcnt lgkmcnt(0)\n"
    "s_add_u32 s68, s66, s69\n"
    "s_barrier\n"
    "s_mov_b32 s65, 49152\n"
    "s_mov_b32 m0, 32768\n"
    "s_nop 0\n"
    "buffer_load_dwordx4 v16, s[36:39], s65 offen offset:0 nt lds\n"
    "buffer_load_dwordx4 v17, s[36:39], s65 offen offset:1024 nt lds\n"
    "buffer_load_dwordx4 v18, s[36:39], s65 offen offset:2048 nt lds\n"
    "buffer_load_dwordx4 v19, s[36:39], s65 offen offset:3072 nt lds\n"
    "s_mov_b32 m0, 36864\n"
    "s_nop 0\n"
    "buffer_load_dwordx4 v20, s[36:39], s65 offen offset:0 nt lds\n"
    "buffer_load_dwordx4 v21, s[36:39], s65 offen offset:1024 nt lds\n"
    "buffer_load_dwordx4 v22, s[36:39], s65 offen offset:2048 nt lds\n"
    "buffer_load_dwordx4 v23, s[36:39], s65 offen offset:3072 nt lds\n"
    "s_mov_b32 m0, 40960\n"
    "s_nop 0\n"
    "buffer_load_dwordx4 v24, s[36:39], s65 offen offset:0 nt lds\n"
    "buffer_load_dwordx4 v25, s[36:39], s65 offen offset:1024 nt lds\n"
    "buffer_load_dwordx4 v26, s[36:39], s65 offen offset:2048 nt lds\n"
    "buffer_load_dwordx4 v27, s[36:39], s65 offen offset:3072 nt lds\n"
    "s_mov_b32 m0, 45056\n"
    "s_waitcnt vmcnt(59)\n"
    "buffer_load_dwordx4 v28, s[36:39], s65 offen offset:0 nt lds\n"
    "buffer_load_dwordx4 v29, s[36:39], s65 offen offset:1024 nt lds\n"
    "buffer_load_dwordx4 v30, s[36:39], s65 offen offset:2048 nt lds\n"
    "buffer_load_dwordx4 v31, s[36:39], s65 offen offset:3072 nt lds\n"
    "s_waitcnt vmcnt(48)\n"
    "s_barrier\n"
    "s_mov_b32 s65, 32768\n"
    "s_mov_b32 m0, 49152\n"
    "s_nop 0\n"
    "buffer_load_dwordx4 v16, s[40:43], s65 offen offset:0 nt lds\n"
    "buffer_load_dwordx4 v17, s[40:43], s65 offen offset:1024 nt lds\n"
    "buffer_load_dwordx4 v18, s[40:43], s65 offen offset:2048 nt lds\n"
    "buffer_load_dwordx4 v19, s[40:43], s65 offen offset:3072 nt lds\n"
    "s_mov_b32 m0, 53248\n"
    "s_nop 0\n"
    "buffer_load_dwordx4 v20, s[40:43], s65 offen offset:0 nt lds\n"
    "buffer_load_dwordx4 v21, s[40:43], s65 offen offset:1024 nt lds\n"
    "buffer_load_dwordx4 v22, s[40:43], s65 offen offset:2048 nt lds\n"
    "buffer_load_dwordx4 v23, s[40:43], s65 offen offset:3072 nt lds\n"
    "s_mov_b32 m0, 57344\n"
    "s_nop 0\n"
    "buffer_load_dwordx4 v24, s[40:43], s65 offen offset:0 nt lds\n"
    "buffer_load_dwordx4 v25, s[40:43], s65 offen offset:1024 nt lds\n"
    "buffer_load_dwordx4 v26, s[40:43], s65 offen offset:2048 nt lds\n"
    "buffer_load_dwordx4 v27, s[40:43], s65 offen offset:3072 nt lds\n"
    "s_mov_b32 m0, 61440\n"
    "s_waitcnt vmcnt(59)\n"
    "buffer_load_dwordx4 v28, s[40:43], s65 offen offset:0 nt lds\n"
    "buffer_load_dwordx4 v29, s[40:43], s65 offen offset:1024 nt lds\n"
    "buffer_load_dwordx4 v30, s[40:43], s65 offen offset:2048 nt lds\n"
    "buffer_load_dwordx4 v31, s[40:43], s65 offen offset:3072 nt lds\n"
    "s_waitcnt vmcnt(48)\n"
    "PC17_%=:\n"
    "s_memrealtime s[66:67]\n"
    "s_waitcnt lgkmcnt(0)\n"
    "s_sub_u32 s64, s66, s68\n"
    "s_cmp_lt_i32 s64, 0\n"
    "s_cbranch_scc1 PC17_%=\n"
    "s_min_u32 s64, s64, 8\n"
    "s_sub_u32 s66, s66, s64\n"
    "s_add_u32 s68, s66, s69\n"
    "s_barrier\n"
    "s_mov_b32 s65, 65536\n"
    "s_mov_b32 m0, 65536\n"
    "s_nop 0\n"
    "buffer_load_dwordx4 v16, s[36:39], s65 offen offset:0 nt lds\n"
    "buffer_load_dwordx4 v17, s[36:39], s65 offen offset:1024 nt lds\n"
    "buffer_load_dwordx4 v18, s[36:39], s65 offen offset:2048 nt lds\n"
    "buffer_load_dwordx4 v19, s[36:39], s65 offen offset:3072 nt lds\n"
    "s_mov_b32 m0, 69632\n"
    "s_nop 0\n"
    "buffer_load_dwordx4 v20, s[36:39], s65 offen offset:0 nt lds\n"
    "buffer_load_dwordx4 v21, s[36:39], s65 offen offset:1024 nt lds\n"
    "buffer_load_dwordx4 v22, s[36:39], s65 offen offset:2048 nt lds\n"
    "buffer_load_dwordx4 v23, s[36:39], s65 offen offset:3072 nt lds\n"
    "s_mov_b32 m0, 73728\n"
    "s_nop 0\n"
    "buffer_load_dwordx4 v24, s[36:39], s65 offen offset:0 nt lds\n"
    "buffer_load_dwordx4 v25, s[36:39], s65 offen offset:1024 nt lds\n"
    "buffer_load_dwordx4 v26, s[36:39], s65 offen offset:2048 nt lds\n"
    "buffer_load_dwordx4 v27, s[36:39], s65 offen offset:3072 nt lds\n"
    "s_mov_b32 m0, 77824\n"
    "s_waitcnt vmcnt(59)\n"
    "buffer_load_dwordx4 v28, s[36:39], s65 offen offset:0 nt lds\n"
    "buffer_load_dwordx4 v29, s[36:39], s65 offen offset:1024 nt lds\n"
    "buffer_load_dwordx4 v30, s[36:39], s65 offen offset:2048 nt lds\n"
    "buffer_load_dwordx4 v31, s[36:39], s65 offen offset:3072 nt lds\n"
    "s_waitcnt vmcnt(48)\n"
    "s_barrier\n"
    "s_mov_b32 s65, 49152\n"
    "s_mov_b32 m0, 81920\n"
    "s_nop 0\n"
    "buffer_load_dwordx4 v16, s[40:43], s65 offen offset:0 nt lds\n"
    "buffer_load_dwordx4 v17, s[40:43], s65 offen offset:1024 nt lds\n"
    "buffer_load_dwordx4 v18, s[40:43], s65 offen offset:2048 nt lds\n"
    "buffer_load_dwordx4 v19, s[40:43], s65 offen offset:3072 nt lds\n"
    "s_mov_b32 m0, 86016\n"
    "s_nop 0\n"
    "buffer_load_dwordx4 v20, s[40:43], s65 offen offset:0 nt lds\n"
    "buffer_load_dwordx4 v21, s[40:43], s65 offen offset:1024 nt lds\n"
    "buffer_load_dwordx4 v22, s[40:43], s65 offen offset:2048 nt lds\n"
    "buffer_load_dwordx4 v23, s[40:43], s65 offen offset:3072 nt lds\n"
    "s_mov_b32 m0, 90112\n"
    "s_nop 0\n"
    "buffer_load_dwordx4 v24, s[40:43], s65 offen offset:0 nt lds\n"
    "buffer_load_dwordx4 v25, s[40:43], s65 offen offset:1024 nt lds\n"
    "buffer_load_dwordx4 v26, s[40:43], s65 offen offset:2048 nt lds\n"
    "buffer_load_dwordx4 v27, s[40:43], s65 offen offset:3072 nt lds\n"
    "s_mov_b32 m0, 94208\n"
    "s_waitcnt vmcnt(59)\n"
    "buffer_load_dwordx4 v28, s[40:43], s65 offen offset:0 nt lds\n"
    "buffer_load_dwordx4 v29, s[40:43], s65 offen offset:1024 nt lds\n"
    "buffer_load_dwordx4 v30, s[40:43], s65 offen offset:2048 nt lds\n"
    "buffer_load_dwordx4 v31, s[40:43], s65 offen offset:3072 nt lds\n"
    "s_waitcnt vmcnt(48)\n"
    "PC19_%=:\n"
    "s_memrealtime s[66:67]\n"
    "s_waitcnt lgkmcnt(0)\n"
    "s_sub_u32 s64, s66, s68\n"
    "s_cmp_lt_i32 s64, 0\n"
    "s_cbranch_scc1 PC19_%=\n"
    "s_min_u32 s64, s64, 8\n"
    "s_sub_u32 s66, s66, s64\n"
    "s_add_u32 s68, s66, s69\n"
    "s_barrier\n"
    "s_mov_b32 s65, 81920\n"
    "s_mov_b32 m0, 98304\n"
    "s_nop 0\n"
    "buffer_load_dwordx4 v16, s[36:39], s65 offen offset:0 nt lds\n"
    "buffer_load_dwordx4 v17, s[36:39], s65 offen offset:1024 nt lds\n"
    "buffer_load_dwordx4 v18, s[36:39], s65 offen offset:2048 nt lds\n"
    "buffer_load_dwordx4 v19, s[36:39], s65 offen offset:3072 nt lds\n"
    "s_mov_b32 m0, 102400\n"
    "s_nop 0\n"
    "buffer_load_dwordx4 v20, s[36:39], s65 offen offset:0 nt lds\n"
    "buffer_load_dwordx4 v21, s[36:39], s65 offen offset:1024 nt lds\n"
    "buffer_load_dwordx4 v22, s[36:39], s65 offen offset:2048 nt lds\n"
    "buffer_load_dwordx4 v23, s[36:39], s65 offen offset:3072 nt lds\n"
    "s_mov_b32 m0, 106496\n"
    "s_nop 0\n"
    "buffer_load_dwordx4 v24, s[36:39], s65 offen offset:0 nt lds\n"
    "buffer_load_dwordx4 v25, s[36:39], s65 offen offset:1024 nt lds\n"
    "buffer_load_dwordx4 v26, s[36:39], s65 offen offset:2048 nt lds\n"
    "buffer_load_dwordx4 v27, s[36:39], s65 offen offset:3072 nt lds\n"
    "s_mov_b32 m0, 110592\n"
    "s_waitcnt vmcnt(59)\n"
    "buffer_load_dwordx4 v28, s[36:39], s65 offen offset:0 nt lds\n"
    "buffer_load_dwordx4 v29, s[36:39], s65 offen offset:1024 nt lds\n"
    "buffer_load_dwordx4 v30, s[36:39], s65 offen offset:2048 nt lds\n"
    "buffer_load_dwordx4 v31, s[36:39], s65 offen offset:3072 nt lds\n"
    "s_waitcnt vmcnt(48)\n"
    "s_barrier\n"
    "s_mov_b32 s65, 65536\n"
    "s_mov_b32 m0, 114688\n"
    "s_nop 0\n"
    "buffer_load_dwordx4 v16, s[40:43], s65 offen offset:0 nt lds\n"
    "buffer_load_dwordx4 v17, s[40:43], s65 offen offset:1024 nt lds\n"
    "buffer_load_dwordx4 v18, s[40:43], s65 offen offset:2048 nt lds\n"
    "buffer_load_dwordx4 v19, s[40:43], s65 offen offset:3072 nt lds\n"
    "s_mov_b32 m0, 118784\n"
    "s_nop 0\n"
    "buffer_load_dwordx4 v20, s[40:43], s65 offen offset:0 nt lds\n"
    "buffer_load_dwordx4 v21, s[40:43], s65 offen offset:1024 nt lds\n"
    "buffer_load_dwordx4 v22, s[40:43], s65 offen offset:2048 nt lds\n"
    "buffer_load_dwordx4 v23, s[40:43], s65 offen offset:3072 nt lds\n"
    "s_mov_b32 m0, 122880\n"
    "s_nop 0\n"
    "buffer_load_dwordx4 v24, s[40:43], s65 offen offset:0 nt lds\n"
    "buffer_load_dwordx4 v25, s[40:43], s65 offen offset:1024 nt lds\n"
    "buffer_load_dwordx4 v26, s[40:43], s65 offen offset:2048 nt lds\n"
    "buffer_load_dwordx4 v27, s[40:43], s65 offen offset:3072 nt lds\n"
    "s_mov_b32 m0, 126976\n"
    "s_waitcnt vmcnt(59)\n"
    "buffer_load_dwordx4 v28, s[40:43], s65 offen offset:0 nt lds\n"
    "buffer_load_dwordx4 v29, s[40:43], s65 offen offset:1024 nt lds\n"
    "buffer_load_dwordx4 v30, s[40:43], s65 offen offset:2048 nt lds\n"
    "buffer_load_dwordx4 v31, s[40:43], s65 offen offset:3072 nt lds\n"
    "s_waitcnt vmcnt(48)\n"
    "PC21_%=:\n"
    "s_memrealtime s[66:67]\n"
    "s_waitcnt lgkmcnt(0)\n"
    "s_sub_u32 s64, s66, s68\n"
    "s_cmp_lt_i32 s64, 0\n"
    "s_cbranch_scc1 PC21_%=\n"
    "s_min_u32 s64, s64, 8\n"
    "s_sub_u32 s66, s66, s64\n"
    "s_add_u32 s68, s66, s69\n"
    "s_barrier\n"
    "s_mov_b32 s65, 98304\n"
    "s_mov_b32 m0, 131072\n"
    "s_nop 0\n"
    "buffer_load_dwordx4 v16, s[36:39], s65 offen offset:0 nt lds\n"
    "buffer_load_dwordx4 v17, s[36:39], s65 offen offset:1024 nt lds\n"
    "buffer_load_dwordx4 v18, s[36:39], s65 offen offset:2048 nt lds\n"
    "buffer_load_dwordx4 v19, s[36:39], s65 offen offset:3072 nt lds\n"
    "s_mov_b32 m0, 135168\n"
    "s_nop 0\n"
    "buffer_load_dwordx4 v20, s[36:39], s65 offen offset:0 nt lds\n"
    "buffer_load_dwordx4 v21, s[36:39], s65 offen offset:1024 nt lds\n"
    "buffer_load_dwordx4 v22, s[36:39], s65 offen offset:2048 nt lds\n"
    "buffer_load_dwordx4 v23, s[36:39], s65 offen offset:3072 nt lds\n"
    "s_mov_b32 m0, 139264\n"
    "s_nop 0\n"
    "buffer_load_dwordx4 v24, s[36:39], s65 offen offset:0 nt lds\n"
    "buffer_load_dwordx4 v25, s[36:39], s65 offen offset:1024 nt lds\n"
    "buffer_load_dwordx4 v26, s[36:39], s65 offen offset:2048 nt lds\n"
    "buffer_load_dwordx4 v27, s[36:39], s65 offen offset:3072 nt lds\n"
    "s_mov_b32 m0, 143360\n"
    "s_waitcnt vmcnt(59)\n"
    "buffer_load_dwordx4 v28, s[36:39], s65 offen offset:0 nt lds\n"
    "buffer_load_dwordx4 v29, s[36:39], s65 offen offset:1024 nt lds\n"
    "buffer_load_dwordx4 v30, s[36:39], s65 offen offset:2048 nt lds\n"
    "buffer_load_dwordx4 v31, s[36:39], s65 offen offset:3072 nt lds\n"
    "s_waitcnt vmcnt(48)\n"
    "s_barrier\n"
    "s_mov_b32 s65, 81920\n"
    "s_mov_b32 m0, 147456\n"
    "s_nop 0\n"
    "buffer_load_dwordx4 v16, s[40:43], s65 offen offset:0 nt lds\n"
    "buffer_load_dwordx4 v17, s[40:43], s65 offen offset:1024 nt lds\n"
    "buffer_load_dwordx4 v18, s[40:43], s65 offen offset:2048 nt lds\n"
    "buffer_load_dwordx4 v19, s[40:43], s65 offen offset:3072 nt lds\n"
    "s_mov_b32 m0, 151552\n"
    "s_nop 0\n"
    "buffer_load_dwordx4 v20, s[40:43], s65 offen offset:0 nt lds\n"
    "buffer_load_dwordx4 v21, s[40:43], s65 offen offset:1024 nt lds\n"
    "buffer_load_dwordx4 v22, s[40:43], s65 offen offset:2048 nt lds\n"
    "buffer_load_dwordx4 v23, s[40:43], s65 offen offset:3072 nt lds\n"
    "s_mov_b32 m0, 155648\n"
    "s_nop 0\n"
    "buffer_load_dwordx4 v24, s[40:43], s65 offen offset:0 nt lds\n"
    "buffer_load_dwordx4 v25, s[40:43], s65 offen offset:1024 nt lds\n"
    "buffer_load_dwordx4 v26, s[40:43], s65 offen offset:2048 nt lds\n"
    "buffer_load_dwordx4 v27, s[40:43], s65 offen offset:3072 nt lds\n"
    "s_mov_b32 m0, 159744\n"
    "s_waitcnt vmcnt(59)\n"
    "buffer_load_dwordx4 v28, s[40:43], s65 offen offset:0 nt lds\n"
    "buffer_load_dwordx4 v29, s[40:43], s65 offen offset:1024 nt lds\n"
    "buffer_load_dwordx4 v30, s[40:43], s65 offen offset:2048 nt lds\n"
    "buffer_load_dwordx4 v31, s[40:43], s65 offen offset:3072 nt lds\n"
    "s_waitcnt vmcnt(48)\n"
    "PC23_%=:\n"
    "s_memrealtime s[66:67]\n"
    "s_waitcnt lgkmcnt(0)\n"
    "s_sub_u32 s64, s66, s68\n"
    "s_cmp_lt_i32 s64, 0\n"
    "s_cbranch_scc1 PC23_%=\n"
    "s_min_u32 s64, s64, 8\n"
    "s_sub_u32 s66, s66, s64\n"
    "s_add_u32 s68, s66, s69\n"
    "s_barrier\n"
    "s_mov_b32 s65, 114688\n"
    "s_mov_b32 m0, 0\n"
    "s_nop 0\n"
    "buffer_load_dwordx4 v16, s[36:39], s65 offen offset:0 nt lds\n"
    "buffer_load_dwordx4 v17, s[36:39], s65 offen offset:1024 nt lds\n"
    "buffer_load_dwordx4 v18, s[36:39], s65 offen offset:2048 nt lds\n"
    "buffer_load_dwordx4 v19, s[36:39], s65 offen offset:3072 nt lds\n"
    "s_mov_b32 m0, 4096\n"
    "s_nop 0\n"
    "buffer_load_dwordx4 v20, s[36:39], s65 offen offset:0 nt lds\n"
    "buffer_load_dwordx4 v21, s[36:39], s65 offen offset:1024 nt lds\n"
    "buffer_load_dwordx4 v22, s[36:39], s65 offen offset:2048 nt lds\n"
    "buffer_load_dwordx4 v23, s[36:39], s65 offen offset:3072 nt lds\n"
    "s_mov_b32 m0, 8192\n"
    "s_nop 0\n"
    "buffer_load_dwordx4 v24, s[36:39], s65 offen offset:0 nt lds\n"
    "buffer_load_dwordx4 v25, s[36:39], s65 offen offset:1024 nt lds\n"
    "buffer_load_dwordx4 v26, s[36:39], s65 offen offset:2048 nt lds\n"
    "buffer_load_dwordx4 v27, s[36:39], s65 offen offset:3072 nt lds\n"
    "s_mov_b32 m0, 12288\n"
    "s_waitcnt vmcnt(59)\n"
    "buffer_load_dwordx4 v28, s[36:39], s65 offen offset:0 nt lds\n"
    "buffer_load_dwordx4 v29, s[36:39], s65 offen offset:1024 nt lds\n"
    "buffer_load_dwordx4 v30, s[36:39], s65 offen offset:2048 nt lds\n"
    "buffer_load_dwordx4 v31, s[36:39], s65 offen offset:3072 nt lds\n"
    "s_waitcnt vmcnt(48)\n"
    "s_barrier\n"
    "s_mov_b32 s65, 98304\n"
    "s_mov_b32 m0, 16384\n"
    "s_nop 0\n"
    "buffer_load_dwordx4 v16, s[40:43], s65 offen offset:0 nt lds\n"
    "buffer_load_dwordx4 v17, s[40:43], s65 offen offset:1024 nt lds\n"
    "buffer_load_dwordx4 v18, s[40:43], s65 offen offset:2048 nt lds\n"
    "buffer_load_dwordx4 v19, s[40:43], s65 offen offset:3072 nt lds\n"
    "s_mov_b32 m0, 20480\n"
    "s_nop 0\n"
    "buffer_load_dwordx4 v20, s[40:43], s65 offen offset:0 nt lds\n"
    "buffer_load_dwordx4 v21, s[40:43], s65 offen offset:1024 nt lds\n"
    "buffer_load_dwordx4 v22, s[40:43], s65 offen offset:2048 nt lds\n"
    "buffer_load_dwordx4 v23, s[40:43], s65 offen offset:3072 nt lds\n"
    "s_mov_b32 m0, 24576\n"
    "s_nop 0\n"
    "buffer_load_dwordx4 v24, s[40:43], s65 offen offset:0 nt lds\n"
    "buffer_load_dwordx4 v25, s[40:43], s65 offen offset:1024 nt lds\n"
    "buffer_load_dwordx4 v26, s[40:43], s65 offen offset:2048 nt lds\n"
    "buffer_load_dwordx4 v27, s[40:43], s65 offen offset:3072 nt lds\n"
    "s_mov_b32 m0, 28672\n"
    "s_waitcnt vmcnt(59)\n"
    "buffer_load_dwordx4 v28, s[40:43], s65 offen offset:0 nt lds\n"
    "buffer_load_dwordx4 v29, s[40:43], s65 offen offset:1024 nt lds\n"
    "buffer_load_dwordx4 v30, s[40:43], s65 offen offset:2048 nt lds\n"
    "buffer_load_dwordx4 v31, s[40:43], s65 offen offset:3072 nt lds\n"
    "s_waitcnt vmcnt(48)\n"
    "PC25_%=:\n"
    "s_memrealtime s[66:67]\n"
    "s_waitcnt lgkmcnt(0)\n"
    "s_sub_u32 s64, s66, s68\n"
    "s_cmp_lt_i32 s64, 0\n"
    "s_cbranch_scc1 PC25_%=\n"
    "s_min_u32 s64, s64, 8\n"
    "s_sub_u32 s66, s66, s64\n"
    "s_add_u32 s68, s66, s69\n"
    "s_barrier\n"
    "s_mov_b32 s65, 131072\n"
    "s_mov_b32 m0, 32768\n"
    "s_nop 0\n"
    "buffer_load_dwordx4 v16, s[36:39], s65 offen offset:0 nt lds\n"
    "buffer_load_dwordx4 v17, s[36:39], s65 offen offset:1024 nt lds\n"
    "buffer_load_dwordx4 v18, s[36:39], s65 offen offset:2048 nt lds\n"
    "buffer_load_dwordx4 v19, s[36:39], s65 offen offset:3072 nt lds\n"
    "s_mov_b32 m0, 36864\n"
    "s_nop 0\n"
    "buffer_load_dwordx4 v20, s[36:39], s65 offen offset:0 nt lds\n"
    "buffer_load_dwordx4 v21, s[36:39], s65 offen offset:1024 nt lds\n"
    "buffer_load_dwordx4 v22, s[36:39], s65 offen offset:2048 nt lds\n"
    "buffer_load_dwordx4 v23, s[36:39], s65 offen offset:3072 nt lds\n"
    "s_mov_b32 m0, 40960\n"
    "s_nop 0\n"
    "buffer_load_dwordx4 v24, s[36:39], s65 offen offset:0 nt lds\n"
    "buffer_load_dwordx4 v25, s[36:39], s65 offen offset:1024 nt lds\n"
    "buffer_load_dwordx4 v26, s[36:39], s65 offen offset:2048 nt lds\n"
    "buffer_load_dwordx4 v27, s[36:39], s65 offen offset:3072 nt lds\n"
    "s_mov_b32 m0, 45056\n"
    "s_waitcnt vmcnt(59)\n"
    "buffer_load_dwordx4 v28, s[36:39], s65 offen offset:0 nt lds\n"
    "buffer_load_dwordx4 v29, s[36:39], s65 offen offset:1024 nt lds\n"
    "buffer_load_dwordx4 v30, s[36:39], s65 offen offset:2048 nt lds\n"
    "buffer_load_dwordx4 v31, s[36:39], s65 offen offset:3072 nt lds\n"
    "s_waitcnt vmcnt(48)\n"
    "s_barrier\n"
    "s_mov_b32 s65, 114688\n"
    "s_mov_b32 m0, 49152\n"
    "s_nop 0\n"
    "buffer_load_dwordx4 v16, s[40:43], s65 offen offset:0 nt lds\n"
    "buffer_load_dwordx4 v17, s[40:43], s65 offen offset:1024 nt lds\n"
    "buffer_load_dwordx4 v18, s[40:43], s65 offen offset:2048 nt lds\n"
    "buffer_load_dwordx4 v19, s[40:43], s65 offen offset:3072 nt lds\n"
    "s_mov_b32 m0, 53248\n"
    "s_nop 0\n"
    "buffer_load_dwordx4 v20, s[40:43], s65 offen offset:0 nt lds\n"
    "buffer_load_dwordx4 v21, s[40:43], s65 offen offset:1024 nt lds\n"
    "buffer_load_dwordx4 v22, s[40:43], s65 offen offset:2048 nt lds\n"
    "buffer_load_dwordx4 v23, s[40:43], s65 offen offset:3072 nt lds\n"
    "s_mov_b32 m0, 57344\n"
    "s_nop 0\n"
    "buffer_load_dwordx4 v24, s[40:43], s65 offen offset:0 nt lds\n"
    "buffer_load_dwordx4 v25, s[40:43], s65 offen offset:1024 nt lds\n"
    "buffer_load_dwordx4 v26, s[40:43], s65 offen offset:2048 nt lds\n"
    "buffer_load_dwordx4 v27, s[40:43], s65 offen offset:3072 nt lds\n"
    "s_mov_b32 m0, 61440\n"
    "s_waitcnt vmcnt(59)\n"
    "buffer_load_dwordx4 v28, s[40:43], s65 offen offset:0 nt lds\n"
    "buffer_load_dwordx4 v29, s[40:43], s65 offen offset:1024 nt lds\n"
    "buffer_load_dwordx4 v30, s[40:43], s65 offen offset:2048 nt lds\n"
    "buffer_load_dwordx4 v31, s[40:43], s65 offen offset:3072 nt lds\n"
    "s_waitcnt vmcnt(48)\n"
    "PC27_%=:\n"
    "s_memrealtime s[66:67]\n"
    "s_waitcnt lgkmcnt(0)\n"
    "s_sub_u32 s64, s66, s68\n"
    "s_cmp_lt_i32 s64, 0\n"
    "s_cbranch_scc1 PC27_%=\n"
    "s_min_u32 s64, s64, 8\n"
    "s_sub_u32 s66, s66, s64\n"
    "s_add_u32 s68, s66, s69\n"
    "s_barrier\n"
    "s_mov_b32 s65, 147456\n"
    "s_mov_b32 m0, 65536\n"
    "s_nop 0\n"
    "buffer_load_dwordx4 v16, s[36:39], s65 offen offset:0 nt lds\n"
    "buffer_load_dwordx4 v17, s[36:39], s65 offen offset:1024 nt lds\n"
    "buffer_load_dwordx4 v18, s[36:39], s65 offen offset:2048 nt lds\n"
    "buffer_load_dwordx4 v19, s[36:39], s65 offen offset:3072 nt lds\n"
    "s_mov_b32 m0, 69632\n"
    "s_nop 0\n"
    "buffer_load_dwordx4 v20, s[36:39], s65 offen offset:0 nt lds\n"
    "buffer_load_dwordx4 v21, s[36:39], s65 offen offset:1024 nt lds\n"
    "buffer_load_dwordx4 v22, s[36:39], s65 offen offset:2048 nt lds\n"
    "buffer_load_dwordx4 v23, s[36:39], s65 offen offset:3072 nt lds\n"
    "s_mov_b32 m0, 73728\n"
    "s_nop 0\n"
    "buffer_load_dwordx4 v24, s[36:39], s65 offen offset:0 nt lds\n"
    "buffer_load_dwordx4 v25, s[36:39], s65 offen offset:1024 nt lds\n"
    "buffer_load_dwordx4 v26, s[36:39], s65 offen offset:2048 nt lds\n"
    "buffer_load_dwordx4 v27, s[36:39], s65 offen offset:3072 nt lds\n"
    "s_mov_b32 m0, 77824\n"
    "s_waitcnt vmcnt(59)\n"
    "buffer_load_dwordx4 v28, s[36:39], s65 offen offset:0 nt lds\n"
    "buffer_load_dwordx4 v29, s[36:39], s65 offen offset:1024 nt lds\n"
    "buffer_load_dwordx4 v30, s[36:39], s65 offen offset:2048 nt lds\n"
    "buffer_load_dwordx4 v31, s[36:39], s65 offen offset:3072 nt lds\n"
    "s_waitcnt vmcnt(48)\n"
    "s_barrier\n"
    "s_mov_b32 s65, 131072\n"
    "s_mov_b32 m0, 81920\n"
    "s_nop 0\n"
    "buffer_load_dwordx4 v16, s[40:43], s65 offen offset:0 nt lds\n"
    "buffer_load_dwordx4 v17, s[40:43], s65 offen offset:1024 nt lds\n"
    "buffer_load_dwordx4 v18, s[40:43], s65 offen offset:2048 nt lds\n"
    "buffer_load_dwordx4 v19, s[40:43], s65 offen offset:3072 nt lds\n"
    "s_mov_b32 m0, 86016\n"
    "s_nop 0\n"
    "buffer_load_dwordx4 v20, s[40:43], s65 offen offset:0 nt lds\n"
    "buffer_load_dwordx4 v21, s[40:43], s65 offen offset:1024 nt lds\n"
    "buffer_load_dwordx4 v22, s[40:43], s65 offen offset:2048 nt lds\n"
    "buffer_load_dwordx4 v23, s[40:43], s65 offen offset:3072 nt lds\n"
    "s_mov_b32 m0, 90112\n"
    "s_nop 0\n"
    "buffer_load_dwordx4 v24, s[40:43], s65 offen offset:0 nt lds\n"
    "buffer_load_dwordx4 v25, s[40:43], s65 offen offset:1024 nt lds\n"
    "buffer_load_dwordx4 v26, s[40:43], s65 offen offset:2048 nt lds\n"
    "buffer_load_dwordx4 v27, s[40:43], s65 offen offset:3072 nt lds\n"
    "s_mov_b32 m0, 94208\n"
    "s_waitcnt vmcnt(59)\n"
    "buffer_load_dwordx4 v28, s[40:43], s65 offen offset:0 nt lds\n"
    "buffer_load_dwordx4 v29, s[40:43], s65 offen offset:1024 nt lds\n"
    "buffer_load_dwordx4 v30, s[40:43], s65 offen offset:2048 nt lds\n"
    "buffer_load_dwordx4 v31, s[40:43], s65 offen offset:3072 nt lds\n"
    "s_waitcnt vmcnt(48)\n"
    "PC29_%=:\n"
    "s_memrealtime s[66:67]\n"
    "s_waitcnt lgkmcnt(0)\n"
    "s_sub_u32 s64, s66, s68\n"
    "s_cmp_lt_i32 s64, 0\n"
    "s_cbranch_scc1 PC29_%=\n"
    "s_min_u32 s64, s64, 8\n"
    "s_sub_u32 s66, s66, s64\n"
    "s_add_u32 s68, s66, s69\n"
    "s_barrier\n"
    "s_mov_b32 s65, 163840\n"
    "s_mov_b32 m0, 98304\n"
    "s_nop 0\n"
    "buffer_load_dwordx4 v16, s[36:39], s65 offen offset:0 nt lds\n"
    "buffer_load_dwordx4 v17, s[36:39], s65 offen offset:1024 nt lds\n"
    "buffer_load_dwordx4 v18, s[36:39], s65 offen offset:2048 nt lds\n"
    "buffer_load_dwordx4 v19, s[36:39], s65 offen offset:3072 nt lds\n"
    "s_mov_b32 m0, 102400\n"
    "s_nop 0\n"
    "buffer_load_dwordx4 v20, s[36:39], s65 offen offset:0 nt lds\n"
    "buffer_load_dwordx4 v21, s[36:39], s65 offen offset:1024 nt lds\n"
    "buffer_load_dwordx4 v22, s[36:39], s65 offen offset:2048 nt lds\n"
    "buffer_load_dwordx4 v23, s[36:39], s65 offen offset:3072 nt lds\n"
    "s_mov_b32 m0, 106496\n"
    "s_nop 0\n"
    "buffer_load_dwordx4 v24, s[36:39], s65 offen offset:0 nt lds\n"
    "buffer_load_dwordx4 v25, s[36:39], s65 offen offset:1024 nt lds\n"
    "buffer_load_dwordx4 v26, s[36:39], s65 offen offset:2048 nt lds\n"
    "buffer_load_dwordx4 v27, s[36:39], s65 offen offset:3072 nt lds\n"
    "s_mov_b32 m0, 110592\n"
    "s_waitcnt vmcnt(59)\n"
    "buffer_load_dwordx4 v28, s[36:39], s65 offen offset:0 nt lds\n"
    "buffer_load_dwordx4 v29, s[36:39], s65 offen offset:1024 nt lds\n"
    "buffer_load_dwordx4 v30, s[36:39], s65 offen offset:2048 nt lds\n"
    "buffer_load_dwordx4 v31, s[36:39], s65 offen offset:3072 nt lds\n"
    "s_waitcnt vmcnt(48)\n"
    "s_barrier\n"
    "s_mov_b32 s65, 147456\n"
    "s_mov_b32 m0, 114688\n"
    "s_nop 0\n"
    "buffer_load_dwordx4 v16, s[40:43], s65 offen offset:0 nt lds\n"
    "buffer_load_dwordx4 v17, s[40:43], s65 offen offset:1024 nt lds\n"
    "buffer_load_dwordx4 v18, s[40:43], s65 offen offset:2048 nt lds\n"
    "buffer_load_dwordx4 v19, s[40:43], s65 offen offset:3072 nt lds\n"
    "s_mov_b32 m0, 118784\n"
    "s_nop 0\n"
    "buffer_load_dwordx4 v20, s[40:43], s65 offen offset:0 nt lds\n"
    "buffer_load_dwordx4 v21, s[40:43], s65 offen offset:1024 nt lds\n"
    "buffer_load_dwordx4 v22, s[40:43], s65 offen offset:2048 nt lds\n"
    "buffer_load_dwordx4 v23, s[40:43], s65 offen offset:3072 nt lds\n"
    "s_mov_b32 m0, 122880\n"
    "s_nop 0\n"
    "buffer_load_dwordx4 v24, s[40:43], s65 offen offset:0 nt lds\n"
    "buffer_load_dwordx4 v25, s[40:43], s65 offen offset:1024 nt lds\n"
    "buffer_load_dwordx4 v26, s[40:43], s65 offen offset:2048 nt lds\n"
    "buffer_load_dwordx4 v27, s[40:43], s65 offen offset:3072 nt lds\n"
    "s_mov_b32 m0, 126976\n"
    "s_waitcnt vmcnt(59)\n"
    "buffer_load_dwordx4 v28, s[40:43], s65 offen offset:0 nt lds\n"
    "buffer_load_dwordx4 v29, s[40:43], s65 offen offset:1024 nt lds\n"
    "buffer_load_dwordx4 v30, s[40:43], s65 offen offset:2048 nt lds\n"
    "buffer_load_dwordx4 v31, s[40:43], s65 offen offset:3072 nt lds\n"
    "s_waitcnt vmcnt(48)\n"
    "PC31_%=:\n"
    "s_memrealtime s[66:67]\n"
    "s_waitcnt lgkmcnt(0)\n"
    "s_sub_u32 s64, s66, s68\n"
    "s_cmp_lt_i32 s64, 0\n"
    "s_cbranch_scc1 PC31_%=\n"
    "s_min_u32 s64, s64, 8\n"
    "s_sub_u32 s66, s66, s64\n"
    "s_add_u32 s68, s66, s69\n"
    "s_barrier\n"
    "s_mov_b32 s65, 180224\n"
    "s_mov_b32 m0, 131072\n"
    "s_nop 0\n"
    "buffer_load_dwordx4 v16, s[36:39], s65 offen offset:0 nt lds\n"
    "buffer_load_dwordx4 v17, s[36:39], s65 offen offset:1024 nt lds\n"
    "buffer_load_dwordx4 v18, s[36:39], s65 offen offset:2048 nt lds\n"
    "buffer_load_dwordx4 v19, s[36:39], s65 offen offset:3072 nt lds\n"
    "s_mov_b32 m0, 135168\n"
    "s_nop 0\n"
    "buffer_load_dwordx4 v20, s[36:39], s65 offen offset:0 nt lds\n"
    "buffer_load_dwordx4 v21, s[36:39], s65 offen offset:1024 nt lds\n"
    "buffer_load_dwordx4 v22, s[36:39], s65 offen offset:2048 nt lds\n"
    "buffer_load_dwordx4 v23, s[36:39], s65 offen offset:3072 nt lds\n"
    "s_mov_b32 m0, 139264\n"
    "s_nop 0\n"
    "buffer_load_dwordx4 v24, s[36:39], s65 offen offset:0 nt lds\n"
    "buffer_load_dwordx4 v25, s[36:39], s65 offen offset:1024 nt lds\n"
    "buffer_load_dwordx4 v26, s[36:39], s65 offen offset:2048 nt lds\n"
    "buffer_load_dwordx4 v27, s[36:39], s65 offen offset:3072 nt lds\n"
    "s_mov_b32 m0, 143360\n"
    "s_waitcnt vmcnt(59)\n"
    "buffer_load_dwordx4 v28, s[36:39], s65 offen offset:0 nt lds\n"
    "buffer_load_dwordx4 v29, s[36:39], s65 offen offset:1024 nt lds\n"
    "buffer_load_dwordx4 v30, s[36:39], s65 offen offset:2048 nt lds\n"
    "buffer_load_dwordx4 v31, s[36:39], s65 offen offset:3072 nt lds\n"
    "s_waitcnt vmcnt(48)\n"
    "s_barrier\n"
    "s_mov_b32 s65, 163840\n"
    "s_mov_b32 m0, 147456\n"
    "s_nop 0\n"
    "buffer_load_dwordx4 v16, s[40:43], s65 offen offset:0 nt lds\n"
    "buffer_load_dwordx4 v17, s[40:43], s65 offen offset:1024 nt lds\n"
    "buffer_load_dwordx4 v18, s[40:43], s65 offen offset:2048 nt lds\n"
    "buffer_load_dwordx4 v19, s[40:43], s65 offen offset:3072 nt lds\n"
    "s_mov_b32 m0, 151552\n"
    "s_nop 0\n"
    "buffer_load_dwordx4 v20, s[40:43], s65 offen offset:0 nt lds\n"
    "buffer_load_dwordx4 v21, s[40:43], s65 offen offset:1024 nt lds\n"
    "buffer_load_dwordx4 v22, s[40:43], s65 offen offset:2048 nt lds\n"
    "buffer_load_dwordx4 v23, s[40:43], s65 offen offset:3072 nt lds\n"
    "s_mov_b32 m0, 155648\n"
    "s_nop 0\n"
    "buffer_load_dwordx4 v24, s[40:43], s65 offen offset:0 nt lds\n"
    "buffer_load_dwordx4 v25, s[40:43], s65 offen offset:1024 nt lds\n"
    "buffer_load_dwordx4 v26, s[40:43], s65 offen offset:2048 nt lds\n"
    "buffer_load_dwordx4 v27, s[40:43], s65 offen offset:3072 nt lds\n"
    "s_mov_b32 m0, 159744\n"
    "s_waitcnt vmcnt(59)\n"
    "buffer_load_dwordx4 v28, s[40:43], s65 offen offset:0 nt lds\n"
    "buffer_load_dwordx4 v29, s[40:43], s65 offen offset:1024 nt lds\n"
    "buffer_load_dwordx4 v30, s[40:43], s65 offen offset:2048 nt lds\n"
    "buffer_load_dwordx4 v31, s[40:43], s65 offen offset:3072 nt lds\n"
    "s_waitcnt vmcnt(48)\n"
    "PC33_%=:\n"
    "s_memrealtime s[66:67]\n"
    "s_waitcnt lgkmcnt(0)\n"
    "s_sub_u32 s64, s66, s68\n"
    "s_cmp_lt_i32 s64, 0\n"
    "s_cbranch_scc1 PC33_%=\n"
    "s_min_u32 s64, s64, 8\n"
    "s_sub_u32 s66, s66, s64\n"
    "s_add_u32 s68, s66, s69\n"
    "s_barrier\n"
    "s_mov_b32 s65, 196608\n"
    "s_mov_b32 m0, 0\n"
    "s_nop 0\n"
    "buffer_load_dwordx4 v16, s[36:39], s65 offen offset:0 nt lds\n"
    "buffer_load_dwordx4 v17, s[36:39], s65 offen offset:1024 nt lds\n"
    "buffer_load_dwordx4 v18, s[36:39], s65 offen offset:2048 nt lds\n"
    "buffer_load_dwordx4 v19, s[36:39], s65 offen offset:3072 nt lds\n"
    "s_mov_b32 m0, 4096\n"
    "s_nop 0\n"
    "buffer_load_dwordx4 v20, s[36:39], s65 offen offset:0 nt lds\n"
    "buffer_load_dwordx4 v21, s[36:39], s65 offen offset:1024 nt lds\n"
    "buffer_load_dwordx4 v22, s[36:39], s65 offen offset:2048 nt lds\n"
    "buffer_load_dwordx4 v23, s[36:39], s65 offen offset:3072 nt lds\n"
    "s_mov_b32 m0, 8192\n"
    "s_nop 0\n"
    "buffer_load_dwordx4 v24, s[36:39], s65 offen offset:0 nt lds\n"
    "buffer_load_dwordx4 v25, s[36:39], s65 offen offset:1024 nt lds\n"
    "buffer_load_dwordx4 v26, s[36:39], s65 offen offset:2048 nt lds\n"
    "buffer_load_dwordx4 v27, s[36:39], s65 offen offset:3072 nt lds\n"
    "s_mov_b32 m0, 12288\n"
    "s_waitcnt vmcnt(59)\n"
    "buffer_load_dwordx4 v28, s[36:39], s65 offen offset:0 nt lds\n"
    "buffer_load_dwordx4 v29, s[36:39], s65 offen offset:1024 nt lds\n"
    "buffer_load_dwordx4 v30, s[36:39], s65 offen offset:2048 nt lds\n"
    "buffer_load_dwordx4 v31, s[36:39], s65 offen offset:3072 nt lds\n"
    "s_waitcnt vmcnt(48)\n"
    "s_barrier\n"
    "s_mov_b32 s65, 180224\n"
    "s_mov_b32 m0, 16384\n"
    "s_nop 0\n"
    "buffer_load_dwordx4 v16, s[40:43], s65 offen offset:0 nt lds\n"
    "buffer_load_dwordx4 v17, s[40:43], s65 offen offset:1024 nt lds\n"
    "buffer_load_dwordx4 v18, s[40:43], s65 offen offset:2048 nt lds\n"
    "buffer_load_dwordx4 v19, s[40:43], s65 offen offset:3072 nt lds\n"
    "s_mov_b32 m0, 20480\n"
    "s_nop 0\n"
    "buffer_load_dwordx4 v20, s[40:43], s65 offen offset:0 nt lds\n"
    "buffer_load_dwordx4 v21, s[40:43], s65 offen offset:1024 nt lds\n"
    "buffer_load_dwordx4 v22, s[40:43], s65 offen offset:2048 nt lds\n"
    "buffer_load_dwordx4 v23, s[40:43], s65 offen offset:3072 nt lds\n"
    "s_mov_b32 m0, 24576\n"
    "s_nop 0\n"
    "buffer_load_dwordx4 v24, s[40:43], s65 offen offset:0 nt lds\n"
    "buffer_load_dwordx4 v25, s[40:43], s65 offen offset:1024 nt lds\n"
    "buffer_load_dwordx4 v26, s[40:43], s65 offen offset:2048 nt lds\n"
    "buffer_load_dwordx4 v27, s[40:43], s65 offen offset:3072 nt lds\n"
    "s_mov_b32 m0, 28672\n"
    "s_waitcnt vmcnt(59)\n"
    "buffer_load_dwordx4 v28, s[40:43], s65 offen offset:0 nt lds\n"
    "buffer_load_dwordx4 v29, s[40:43], s65 offen offset:1024 nt lds\n"
    "buffer_load_dwordx4 v30, s[40:43], s65 offen offset:2048 nt lds\n"
    "buffer_load_dwordx4 v31, s[40:43], s65 offen offset:3072 nt lds\n"
    "s_waitcnt vmcnt(48)\n"
    "PC35_%=:\n"
    "s_memrealtime s[66:67]\n"
    "s_waitcnt lgkmcnt(0)\n"
    "s_sub_u32 s64, s66, s68\n"
    "s_cmp_lt_i32 s64, 0\n"
    "s_cbranch_scc1 PC35_%=\n"
    "s_min_u32 s64, s64, 8\n"
    "s_sub_u32 s66, s66, s64\n"
    "s_add_u32 s68, s66, s69\n"
    "s_barrier\n"
    "s_mov_b32 s65, 212992\n"
    "s_mov_b32 m0, 32768\n"
    "s_nop 0\n"
    "buffer_load_dwordx4 v16, s[36:39], s65 offen offset:0 nt lds\n"
    "buffer_load_dwordx4 v17, s[36:39], s65 offen offset:1024 nt lds\n"
    "buffer_load_dwordx4 v18, s[36:39], s65 offen offset:2048 nt lds\n"
    "buffer_load_dwordx4 v19, s[36:39], s65 offen offset:3072 nt lds\n"
    "s_mov_b32 m0, 36864\n"
    "s_nop 0\n"
    "buffer_load_dwordx4 v20, s[36:39], s65 offen offset:0 nt lds\n"
    "buffer_load_dwordx4 v21, s[36:39], s65 offen offset:1024 nt lds\n"
    "buffer_load_dwordx4 v22, s[36:39], s65 offen offset:2048 nt lds\n"
    "buffer_load_dwordx4 v23, s[36:39], s65 offen offset:3072 nt lds\n"
    "s_mov_b32 m0, 40960\n"
    "s_nop 0\n"
    "buffer_load_dwordx4 v24, s[36:39], s65 offen offset:0 nt lds\n"
    "buffer_load_dwordx4 v25, s[36:39], s65 offen offset:1024 nt lds\n"
    "buffer_load_dwordx4 v26, s[36:39], s65 offen offset:2048 nt lds\n"
    "buffer_load_dwordx4 v27, s[36:39], s65 offen offset:3072 nt lds\n"
    "s_mov_b32 m0, 45056\n"
    "s_waitcnt vmcnt(59)\n"
    "buffer_load_dwordx4 v28, s[36:39], s65 offen offset:0 nt lds\n"
    "buffer_load_dwordx4 v29, s[36:39], s65 offen offset:1024 nt lds\n"
    "buffer_load_dwordx4 v30, s[36:39], s65 offen offset:2048 nt lds\n"
    "buffer_load_dwordx4 v31, s[36:39], s65 offen offset:3072 nt lds\n"
    "s_waitcnt vmcnt(48)\n"
    "s_barrier\n"
    "s_mov_b32 s65, 196608\n"
    "s_mov_b32 m0, 49152\n"
    "s_nop 0\n"
    "buffer_load_dwordx4 v16, s[40:43], s65 offen offset:0 nt lds\n"
    "buffer_load_dwordx4 v17, s[40:43], s65 offen offset:1024 nt lds\n"
    "buffer_load_dwordx4 v18, s[40:43], s65 offen offset:2048 nt lds\n"
    "buffer_load_dwordx4 v19, s[40:43], s65 offen offset:3072 nt lds\n"
    "s_mov_b32 m0, 53248\n"
    "s_nop 0\n"
    "buffer_load_dwordx4 v20, s[40:43], s65 offen offset:0 nt lds\n"
    "buffer_load_dwordx4 v21, s[40:43], s65 offen offset:1024 nt lds\n"
    "buffer_load_dwordx4 v22, s[40:43], s65 offen offset:2048 nt lds\n"
    "buffer_load_dwordx4 v23, s[40:43], s65 offen offset:3072 nt lds\n"
    "s_mov_b32 m0, 57344\n"
    "s_nop 0\n"
    "buffer_load_dwordx4 v24, s[40:43], s65 offen offset:0 nt lds\n"
    "buffer_load_dwordx4 v25, s[40:43], s65 offen offset:1024 nt lds\n"
    "buffer_load_dwordx4 v26, s[40:43], s65 offen offset:2048 nt lds\n"
    "buffer_load_dwordx4 v27, s[40:43], s65 offen offset:3072 nt lds\n"
    "s_mov_b32 m0, 61440\n"
    "s_waitcnt vmcnt(59)\n"
    "buffer_load_dwordx4 v28, s[40:43], s65 offen offset:0 nt lds\n"
    "buffer_load_dwordx4 v29, s[40:43], s65 offen offset:1024 nt lds\n"
    "buffer_load_dwordx4 v30, s[40:43], s65 offen offset:2048 nt lds\n"
    "buffer_load_dwordx4 v31, s[40:43], s65 offen offset:3072 nt lds\n"
    "s_waitcnt vmcnt(48)\n"
    "PC37_%=:\n"
    "s_memrealtime s[66:67]\n"
    "s_waitcnt lgkmcnt(0)\n"
    "s_sub_u32 s64, s66, s68\n"
    "s_cmp_lt_i32 s64, 0\n"
    "s_cbranch_scc1 PC37_%=\n"
    "s_min_u32 s64, s64, 8\n"
    "s_sub_u32 s66, s66, s64\n"
    "s_add_u32 s68, s66, s69\n"
    "s_barrier\n"
    "s_mov_b32 s65, 229376\n"
    "s_mov_b32 m0, 65536\n"
    "s_nop 0\n"
    "buffer_load_dwordx4 v16, s[36:39], s65 offen offset:0 nt lds\n"
    "buffer_load_dwordx4 v17, s[36:39], s65 offen offset:1024 nt lds\n"
    "buffer_load_dwordx4 v18, s[36:39], s65 offen offset:2048 nt lds\n"
    "buffer_load_dwordx4 v19, s[36:39], s65 offen offset:3072 nt lds\n"
    "s_mov_b32 m0, 69632\n"
    "s_nop 0\n"
    "buffer_load_dwordx4 v20, s[36:39], s65 offen offset:0 nt lds\n"
    "buffer_load_dwordx4 v21, s[36:39], s65 offen offset:1024 nt lds\n"
    "buffer_load_dwordx4 v22, s[36:39], s65 offen offset:2048 nt lds\n"
    "buffer_load_dwordx4 v23, s[36:39], s65 offen offset:3072 nt lds\n"
    "s_mov_b32 m0, 73728\n"
    "s_nop 0\n"
    "buffer_load_dwordx4 v24, s[36:39], s65 offen offset:0 nt lds\n"
    "buffer_load_dwordx4 v25, s[36:39], s65 offen offset:1024 nt lds\n"
    "buffer_load_dwordx4 v26, s[36:39], s65 offen offset:2048 nt lds\n"
    "buffer_load_dwordx4 v27, s[36:39], s65 offen offset:3072 nt lds\n"
    "s_mov_b32 m0, 77824\n"
    "s_waitcnt vmcnt(59)\n"
    "buffer_load_dwordx4 v28, s[36:39], s65 offen offset:0 nt lds\n"
    "buffer_load_dwordx4 v29, s[36:39], s65 offen offset:1024 nt lds\n"
    "buffer_load_dwordx4 v30, s[36:39], s65 offen offset:2048 nt lds\n"
    "buffer_load_dwordx4 v31, s[36:39], s65 offen offset:3072 nt lds\n"
    "s_waitcnt vmcnt(48)\n"
    "s_barrier\n"
    "s_mov_b32 s65, 212992\n"
    "s_mov_b32 m0, 81920\n"
    "s_nop 0\n"
    "buffer_load_dwordx4 v16, s[40:43], s65 offen offset:0 nt lds\n"
    "buffer_load_dwordx4 v17, s[40:43], s65 offen offset:1024 nt lds\n"
    "buffer_load_dwordx4 v18, s[40:43], s65 offen offset:2048 nt lds\n"
    "buffer_load_dwordx4 v19, s[40:43], s65 offen offset:3072 nt lds\n"
    "s_mov_b32 m0, 86016\n"
    "s_nop 0\n"
    "buffer_load_dwordx4 v20, s[40:43], s65 offen offset:0 nt lds\n"
    "buffer_load_dwordx4 v21, s[40:43], s65 offen offset:1024 nt lds\n"
    "buffer_load_dwordx4 v22, s[40:43], s65 offen offset:2048 nt lds\n"
    "buffer_load_dwordx4 v23, s[40:43], s65 offen offset:3072 nt lds\n"
    "s_mov_b32 m0, 90112\n"
    "s_nop 0\n"
    "buffer_load_dwordx4 v24, s[40:43], s65 offen offset:0 nt lds\n"
    "buffer_load_dwordx4 v25, s[40:43], s65 offen offset:1024 nt lds\n"
    "buffer_load_dwordx4 v26, s[40:43], s65 offen offset:2048 nt lds\n"
    "buffer_load_dwordx4 v27, s[40:43], s65 offen offset:3072 nt lds\n"
    "s_mov_b32 m0, 94208\n"
    "s_waitcnt vmcnt(59)\n"
    "buffer_load_dwordx4 v28, s[40:43], s65 offen offset:0 nt lds\n"
    "buffer_load_dwordx4 v29, s[40:43], s65 offen offset:1024 nt lds\n"
    "buffer_load_dwordx4 v30, s[40:43], s65 offen offset:2048 nt lds\n"
    "buffer_load_dwordx4 v31, s[40:43], s65 offen offset:3072 nt lds\n"
    "s_waitcnt vmcnt(48)\n"
    "PC39_%=:\n"
    "s_memrealtime s[66:67]\n"
    "s_waitcnt lgkmcnt(0)\n"
    "s_sub_u32 s64, s66, s68\n"
    "s_cmp_lt_i32 s64, 0\n"
    "s_cbranch_scc1 PC39_%=\n"
    "s_min_u32 s64, s64, 8\n"
    "s_sub_u32 s66, s66, s64\n"
    "s_add_u32 s68, s66, s69\n"
    "s_barrier\n"
    "s_mov_b32 s65, 245760\n"
    "s_mov_b32 m0, 98304\n"
    "s_nop 0\n"
    "buffer_load_dwordx4 v16, s[36:39], s65 offen offset:0 nt lds\n"
    "buffer_load_dwordx4 v17, s[36:39], s65 offen offset:1024 nt lds\n"
    "buffer_load_dwordx4 v18, s[36:39], s65 offen offset:2048 nt lds\n"
    "buffer_load_dwordx4 v19, s[36:39], s65 offen offset:3072 nt lds\n"
    "s_mov_b32 m0, 102400\n"
    "s_nop 0\n"
    "buffer_load_dwordx4 v20, s[36:39], s65 offen offset:0 nt lds\n"
    "buffer_load_dwordx4 v21, s[36:39], s65 offen offset:1024 nt lds\n"
    "buffer_load_dwordx4 v22, s[36:39], s65 offen offset:2048 nt lds\n"
    "buffer_load_dwordx4 v23, s[36:39], s65 offen offset:3072 nt lds\n"
    "s_mov_b32 m0, 106496\n"
    "s_nop 0\n"
    "buffer_load_dwordx4 v24, s[36:39], s65 offen offset:0 nt lds\n"
    "buffer_load_dwordx4 v25, s[36:39], s65 offen offset:1024 nt lds\n"
    "buffer_load_dwordx4 v26, s[36:39], s65 offen offset:2048 nt lds\n"
    "buffer_load_dwordx4 v27, s[36:39], s65 offen offset:3072 nt lds\n"
    "s_mov_b32 m0, 110592\n"
    "s_waitcnt vmcnt(59)\n"
    "buffer_load_dwordx4 v28, s[36:39], s65 offen offset:0 nt lds\n"
    "buffer_load_dwordx4 v29, s[36:39], s65 offen offset:1024 nt lds\n"
    "buffer_load_dwordx4 v30, s[36:39], s65 offen offset:2048 nt lds\n"
    "buffer_load_dwordx4 v31, s[36:39], s65 offen offset:3072 nt lds\n"
    "s_mov_b64 s[28:29], %[fp]\n"
    "s_and_b32 s29, s29, 0xffff\n"
    "s_mov_b32 s30, 8320\n"
    "s_mov_b32 s31, 0x20000\n"
    "s_waitcnt vmcnt(62)\n"
    "v_mov_b32 v2, 8192\n"
    "buffer_load_dwordx2 v[64:65], v2, s[28:31], 0 offen sc1\n"
    "s_mov_b64 s[52:53], %[gp]\n"
    "s_and_b32 s53, s53, 0xffff\n"
    "s_mov_b32 s54, 2064384\n"
    "s_mov_b32 s55, 0x20000\n"
    "s_mov_b64 s[96:97], %[op]\n"
    "s_and_b32 s97, s97, 0xffff\n"
    "s_mov_b32 s98, 257152\n"
    "s_mov_b32 s99, 0x20000\n"
    "v_and_b32 v5, 15, v1\n"
    "v_lshrrev_b32 v6, 4, v1\n"
    "v_lshlrev_b32 v68, 3, v1\n"
    "v_cmp_gt_u32 vcc, 48, v1\n"
    "v_mov_b32 v2, 0x100000\n"
    "s_nop 1\n"
    "v_cndmask_b32 v68, v2, v68, vcc\n"
    "s_lshl_b32 s64, %[head], 9\n"
    "v_add_u32 v68, 0, v68\n"
    "v_lshlrev_b32 v72, 4, v1\n"
    "v_mul_u32_u24 v2, 1792, v5\n"
    "v_lshl_add_u32 v73, v6, 4, v2\n"
    "v_lshrrev_b32 v2, 3, v5\n"
    "v_and_b32 v3, 3, v5\n"
    "v_lshl_add_u32 v2, v2, 2, v3\n"
    "v_and_b32 v3, 1, v2\n"
    "v_lshlrev_b32 v3, 4, v3\n"
    "v_mov_b32 v4, 0x3f80\n"
    "v_lshlrev_b32 v3, v3, v4\n"
    "v_lshrrev_b32 v2, 1, v2\n"
    "v_bfe_u32 v4, v5, 2, 1\n"
    "v_lshlrev_b32 v4, 1, v4\n"
    "v_sub_u32 v4, v6, v4\n"
    "v_lshl_add_u32 v2, v4, 2, v2\n"
    "v_cmp_eq_u32 vcc, 0, v2\n"
    "s_nop 1\n"
    "v_cndmask_b32 v76, 0, v3, vcc\n"
    "v_cmp_eq_u32 vcc, 1, v2\n"
    "s_nop 1\n"
    "v_cndmask_b32 v77, 0, v3, vcc\n"
    "v_cmp_eq_u32 vcc, 2, v2\n"
    "s_nop 1\n"
    "v_cndmask_b32 v78, 0, v3, vcc\n"
    "v_cmp_eq_u32 vcc, 3, v2\n"
    "s_nop 1\n"
    "v_cndmask_b32 v79, 0, v3, vcc\n"
    "v_cmp_eq_u32 vcc, 4, v2\n"
    "s_nop 1\n"
    "v_cndmask_b32 v80, 0, v3, vcc\n"
    "v_cmp_eq_u32 vcc, 5, v2\n"
    "s_nop 1\n"
    "v_cndmask_b32 v81, 0, v3, vcc\n"
    "v_cmp_eq_u32 vcc, 6, v2\n"
    "s_nop 1\n"
    "v_cndmask_b32 v82, 0, v3, vcc\n"
    "v_cmp_eq_u32 vcc, 7, v2\n"
    "s_nop 1\n"
    "v_cndmask_b32 v83, 0, v3, vcc\n"
    "s_waitcnt vmcnt(49)\n"
    "s_barrier\n"
    "s_mov_b32 s65, 229376\n"
    "s_mov_b32 m0, 114688\n"
    "s_nop 0\n"
    "buffer_load_dwordx4 v16, s[40:43], s65 offen offset:0 nt lds\n"
    "buffer_load_dwordx4 v17, s[40:43], s65 offen offset:1024 nt lds\n"
    "buffer_load_dwordx4 v18, s[40:43], s65 offen offset:2048 nt lds\n"
    "buffer_load_dwordx4 v19, s[40:43], s65 offen offset:3072 nt lds\n"
    "s_mov_b32 m0, 118784\n"
    "s_nop 0\n"
    "buffer_load_dwordx4 v20, s[40:43], s65 offen offset:0 nt lds\n"
    "buffer_load_dwordx4 v21, s[40:43], s65 offen offset:1024 nt lds\n"
    "buffer_load_dwordx4 v22, s[40:43], s65 offen offset:2048 nt lds\n"
    "buffer_load_dwordx4 v23, s[40:43], s65 offen offset:3072 nt lds\n"
    "s_mov_b32 m0, 122880\n"
    "s_nop 0\n"
    "buffer_load_dwordx4 v24, s[40:43], s65 offen offset:0 nt lds\n"
    "buffer_load_dwordx4 v25, s[40:43], s65 offen offset:1024 nt lds\n"
    "buffer_load_dwordx4 v26, s[40:43], s65 offen offset:2048 nt lds\n"
    "buffer_load_dwordx4 v27, s[40:43], s65 offen offset:3072 nt lds\n"
    "s_mov_b32 m0, 126976\n"
    "s_waitcnt vmcnt(59)\n"
    "buffer_load_dwordx4 v28, s[40:43], s65 offen offset:0 nt lds\n"
    "buffer_load_dwordx4 v29, s[40:43], s65 offen offset:1024 nt lds\n"
    "buffer_load_dwordx4 v30, s[40:43], s65 offen offset:2048 nt lds\n"
    "buffer_load_dwordx4 v31, s[40:43], s65 offen offset:3072 nt lds\n"
    "s_waitcnt vmcnt(49)\n"
    "PC41_%=:\n"
    "s_memrealtime s[66:67]\n"
    "s_waitcnt lgkmcnt(0)\n"
    "s_sub_u32 s64, s66, s68\n"
    "s_cmp_lt_i32 s64, 0\n"
    "s_cbranch_scc1 PC41_%=\n"
    "s_min_u32 s64, s64, 8\n"
    "s_sub_u32 s66, s66, s64\n"
    "s_add_u32 s68, s66, s69\n"
    "s_barrier\n"
    "s_mov_b32 s65, 262144\n"
    "s_mov_b32 m0, 131072\n"
    "s_nop 0\n"
    "buffer_load_dwordx4 v16, s[36:39], s65 offen offset:0 nt lds\n"
    "buffer_load_dwordx4 v17, s[36:39], s65 offen offset:1024 nt lds\n"
    "buffer_load_dwordx4 v18, s[36:39], s65 offen offset:2048 nt lds\n"
    "buffer_load_dwordx4 v19, s[36:39], s65 offen offset:3072 nt lds\n"
    "s_mov_b32 m0, 135168\n"
    "s_nop 0\n"
    "buffer_load_dwordx4 v20, s[36:39], s65 offen offset:0 nt lds\n"
    "buffer_load_dwordx4 v21, s[36:39], s65 offen offset:1024 nt lds\n"
    "buffer_load_dwordx4 v22, s[36:39], s65 offen offset:2048 nt lds\n"
    "buffer_load_dwordx4 v23, s[36:39], s65 offen offset:3072 nt lds\n"
    "s_mov_b32 m0, 139264\n"
    "s_nop 0\n"
    "buffer_load_dwordx4 v24, s[36:39], s65 offen offset:0 nt lds\n"
    "buffer_load_dwordx4 v25, s[36:39], s65 offen offset:1024 nt lds\n"
    "buffer_load_dwordx4 v26, s[36:39], s65 offen offset:2048 nt lds\n"
    "buffer_load_dwordx4 v27, s[36:39], s65 offen offset:3072 nt lds\n"
    "s_mov_b32 m0, 143360\n"
    "s_waitcnt vmcnt(59)\n"
    "buffer_load_dwordx4 v28, s[36:39], s65 offen offset:0 nt lds\n"
    "buffer_load_dwordx4 v29, s[36:39], s65 offen offset:1024 nt lds\n"
    "buffer_load_dwordx4 v30, s[36:39], s65 offen offset:2048 nt lds\n"
    "buffer_load_dwordx4 v31, s[36:39], s65 offen offset:3072 nt lds\n"
    "s_waitcnt vmcnt(49)\n"
    "s_barrier\n"
    "s_mov_b32 s65, 245760\n"
    "s_mov_b32 m0, 147456\n"
    "s_nop 0\n"
    "buffer_load_dwordx4 v16, s[40:43], s65 offen offset:0 nt lds\n"
    "buffer_load_dwordx4 v17, s[40:43], s65 offen offset:1024 nt lds\n"
    "buffer_load_dwordx4 v18, s[40:43], s65 offen offset:2048 nt lds\n"
    "buffer_load_dwordx4 v19, s[40:43], s65 offen offset:3072 nt lds\n"
    "s_mov_b32 m0, 151552\n"
    "s_nop 0\n"
    "buffer_load_dwordx4 v20, s[40:43], s65 offen offset:0 nt lds\n"
    "buffer_load_dwordx4 v21, s[40:43], s65 offen offset:1024 nt lds\n"
    "buffer_load_dwordx4 v22, s[40:43], s65 offen offset:2048 nt lds\n"
    "buffer_load_dwordx4 v23, s[40:43], s65 offen offset:3072 nt lds\n"
    "s_mov_b32 m0, 155648\n"
    "s_nop 0\n"
    "buffer_load_dwordx4 v24, s[40:43], s65 offen offset:0 nt lds\n"
    "buffer_load_dwordx4 v25, s[40:43], s65 offen offset:1024 nt lds\n"
    "buffer_load_dwordx4 v26, s[40:43], s65 offen offset:2048 nt lds\n"
    "buffer_load_dwordx4 v27, s[40:43], s65 offen offset:3072 nt lds\n"
    "s_mov_b32 m0, 159744\n"
    "s_waitcnt vmcnt(59)\n"
    "buffer_load_dwordx4 v28, s[40:43], s65 offen offset:0 nt lds\n"
    "buffer_load_dwordx4 v29, s[40:43], s65 offen offset:1024 nt lds\n"
    "buffer_load_dwordx4 v30, s[40:43], s65 offen offset:2048 nt lds\n"
    "buffer_load_dwordx4 v31, s[40:43], s65 offen offset:3072 nt lds\n"
    "s_waitcnt vmcnt(49)\n"
    "PC43_%=:\n"
    "s_memrealtime s[66:67]\n"
    "s_waitcnt lgkmcnt(0)\n"
    "s_sub_u32 s64, s66, s68\n"
    "s_cmp_lt_i32 s64, 0\n"
    "s_cbranch_scc1 PC43_%=\n"
    "s_min_u32 s64, s64, 8\n"
    "s_sub_u32 s66, s66, s64\n"
    "s_add_u32 s68, s66, s69\n"
    "s_barrier\n"
    "s_mov_b32 s65, 278528\n"
    "s_mov_b32 m0, 0\n"
    "s_nop 0\n"
    "buffer_load_dwordx4 v16, s[36:39], s65 offen offset:0 nt lds\n"
    "buffer_load_dwordx4 v17, s[36:39], s65 offen offset:1024 nt lds\n"
    "buffer_load_dwordx4 v18, s[36:39], s65 offen offset:2048 nt lds\n"
    "buffer_load_dwordx4 v19, s[36:39], s65 offen offset:3072 nt lds\n"
    "s_mov_b32 m0, 4096\n"
    "s_nop 0\n"
    "buffer_load_dwordx4 v20, s[36:39], s65 offen offset:0 nt lds\n"
    "buffer_load_dwordx4 v21, s[36:39], s65 offen offset:1024 nt lds\n"
    "buffer_load_dwordx4 v22, s[36:39], s65 offen offset:2048 nt lds\n"
    "buffer_load_dwordx4 v23, s[36:39], s65 offen offset:3072 nt lds\n"
    "s_mov_b32 m0, 8192\n"
    "s_nop 0\n"
    "buffer_load_dwordx4 v24, s[36:39], s65 offen offset:0 nt lds\n"
    "buffer_load_dwordx4 v25, s[36:39], s65 offen offset:1024 nt lds\n"
    "buffer_load_dwordx4 v26, s[36:39], s65 offen offset:2048 nt lds\n"
    "buffer_load_dwordx4 v27, s[36:39], s65 offen offset:3072 nt lds\n"
    "s_mov_b32 m0, 12288\n"
    "s_waitcnt vmcnt(59)\n"
    "buffer_load_dwordx4 v28, s[36:39], s65 offen offset:0 nt lds\n"
    "buffer_load_dwordx4 v29, s[36:39], s65 offen offset:1024 nt lds\n"
    "buffer_load_dwordx4 v30, s[36:39], s65 offen offset:2048 nt lds\n"
    "buffer_load_dwordx4 v31, s[36:39], s65 offen offset:3072 nt lds\n"
    "s_waitcnt vmcnt(48)\n"
    "s_barrier\n"
    "s_mov_b32 s65, 262144\n"
    "s_mov_b32 m0, 16384\n"
    "s_nop 0\n"
    "buffer_load_dwordx4 v16, s[40:43], s65 offen offset:0 nt lds\n"
    "buffer_load_dwordx4 v17, s[40:43], s65 offen offset:1024 nt lds\n"
    "buffer_load_dwordx4 v18, s[40:43], s65 offen offset:2048 nt lds\n"
    "buffer_load_dwordx4 v19, s[40:43], s65 offen offset:3072 nt lds\n"
    "s_mov_b32 m0, 20480\n"
    "s_nop 0\n"
    "buffer_load_dwordx4 v20, s[40:43], s65 offen offset:0 nt lds\n"
    "buffer_load_dwordx4 v21, s[40:43], s65 offen offset:1024 nt lds\n"
    "buffer_load_dwordx4 v22, s[40:43], s65 offen offset:2048 nt lds\n"
    "buffer_load_dwordx4 v23, s[40:43], s65 offen offset:3072 nt lds\n"
    "s_mov_b32 m0, 24576\n"
    "s_nop 0\n"
    "buffer_load_dwordx4 v24, s[40:43], s65 offen offset:0 nt lds\n"
    "buffer_load_dwordx4 v25, s[40:43], s65 offen offset:1024 nt lds\n"
    "buffer_load_dwordx4 v26, s[40:43], s65 offen offset:2048 nt lds\n"
    "buffer_load_dwordx4 v27, s[40:43], s65 offen offset:3072 nt lds\n"
    "s_mov_b32 m0, 28672\n"
    "s_waitcnt vmcnt(59)\n"
    "buffer_load_dwordx4 v28, s[40:43], s65 offen offset:0 nt lds\n"
    "buffer_load_dwordx4 v29, s[40:43], s65 offen offset:1024 nt lds\n"
    "buffer_load_dwordx4 v30, s[40:43], s65 offen offset:2048 nt lds\n"
    "buffer_load_dwordx4 v31, s[40:43], s65 offen offset:3072 nt lds\n"
    "s_waitcnt vmcnt(48)\n"
    "PC45_%=:\n"
    "s_memrealtime s[66:67]\n"
    "s_waitcnt lgkmcnt(0)\n"
    "s_sub_u32 s64, s66, s68\n"
    "s_cmp_lt_i32 s64, 0\n"
    "s_cbranch_scc1 PC45_%=\n"
    "s_min_u32 s64, s64, 8\n"
    "s_sub_u32 s66, s66, s64\n"
    "s_add_u32 s68, s66, s69\n"
    "s_barrier\n"
    "s_mov_b32 s65, 294912\n"
    "s_mov_b32 m0, 32768\n"
    "s_nop 0\n"
    "buffer_load_dwordx4 v16, s[36:39], s65 offen offset:0 nt lds\n"
    "buffer_load_dwordx4 v17, s[36:39], s65 offen offset:1024 nt lds\n"
    "buffer_load_dwordx4 v18, s[36:39], s65 offen offset:2048 nt lds\n"
    "buffer_load_dwordx4 v19, s[36:39], s65 offen offset:3072 nt lds\n"
    "s_mov_b32 m0, 36864\n"
    "s_nop 0\n"
    "buffer_load_dwordx4 v20, s[36:39], s65 offen offset:0 nt lds\n"
    "buffer_load_dwordx4 v21, s[36:39], s65 offen offset:1024 nt lds\n"
    "buffer_load_dwordx4 v22, s[36:39], s65 offen offset:2048 nt lds\n"
    "buffer_load_dwordx4 v23, s[36:39], s65 offen offset:3072 nt lds\n"
    "s_mov_b32 m0, 40960\n"
    "s_nop 0\n"
    "buffer_load_dwordx4 v24, s[36:39], s65 offen offset:0 nt lds\n"
    "buffer_load_dwordx4 v25, s[36:39], s65 offen offset:1024 nt lds\n"
    "buffer_load_dwordx4 v26, s[36:39], s65 offen offset:2048 nt lds\n"
    "buffer_load_dwordx4 v27, s[36:39], s65 offen offset:3072 nt lds\n"
    "s_mov_b32 m0, 45056\n"
    "s_waitcnt vmcnt(59)\n"
    "buffer_load_dwordx4 v28, s[36:39], s65 offen offset:0 nt lds\n"
    "buffer_load_dwordx4 v29, s[36:39], s65 offen offset:1024 nt lds\n"
    "buffer_load_dwordx4 v30, s[36:39], s65 offen offset:2048 nt lds\n"
    "buffer_load_dwordx4 v31, s[36:39], s65 offen offset:3072 nt lds\n"
    "s_waitcnt vmcnt(48)\n"
    "s_barrier\n"
    "s_mov_b32 s65, 278528\n"
    "s_mov_b32 m0, 49152\n"
    "s_nop 0\n"
    "buffer_load_dwordx4 v16, s[40:43], s65 offen offset:0 nt lds\n"
    "buffer_load_dwordx4 v17, s[40:43], s65 offen offset:1024 nt lds\n"
    "buffer_load_dwordx4 v18, s[40:43], s65 offen offset:2048 nt lds\n"
    "buffer_load_dwordx4 v19, s[40:43], s65 offen offset:3072 nt lds\n"
    "s_mov_b32 m0, 53248\n"
    "s_nop 0\n"
    "buffer_load_dwordx4 v20, s[40:43], s65 offen offset:0 nt lds\n"
    "buffer_load_dwordx4 v21, s[40:43], s65 offen offset:1024 nt lds\n"
    "buffer_load_dwordx4 v22, s[40:43], s65 offen offset:2048 nt lds\n"
    "buffer_load_dwordx4 v23, s[40:43], s65 offen offset:3072 nt lds\n"
    "s_mov_b32 m0, 57344\n"
    "s_nop 0\n"
    "buffer_load_dwordx4 v24, s[40:43], s65 offen offset:0 nt lds\n"
    "buffer_load_dwordx4 v25, s[40:43], s65 offen offset:1024 nt lds\n"
    "buffer_load_dwordx4 v26, s[40:43], s65 offen offset:2048 nt lds\n"
    "buffer_load_dwordx4 v27, s[40:43], s65 offen offset:3072 nt lds\n"
    "s_mov_b32 m0, 61440\n"
    "s_waitcnt vmcnt(59)\n"
    "buffer_load_dwordx4 v28, s[40:43], s65 offen offset:0 nt lds\n"
    "buffer_load_dwordx4 v29, s[40:43], s65 offen offset:1024 nt lds\n"
    "buffer_load_dwordx4 v30, s[40:43], s65 offen offset:2048 nt lds\n"
    "buffer_load_dwordx4 v31, s[40:43], s65 offen offset:3072 nt lds\n"
    "s_mov_b32 s65, 512\n"
    "s_waitcnt vmcnt(59)\n"
    "buffer_load_dwordx4 a[0:3], v32, s[48:51], s65 offen offset:0 nt\n"
    "buffer_load_dwordx4 a[4:7], v33, s[48:51], s65 offen offset:1024 nt\n"
    "buffer_load_dwordx4 a[8:11], v34, s[48:51], s65 offen offset:2048 nt\n"
    "buffer_load_dwordx4 a[12:15], v35, s[48:51], s65 offen offset:3072 nt\n"
    "s_waitcnt vmcnt(52)\n"
    "PC47_%=:\n"
    "s_memrealtime s[66:67]\n"
    "s_waitcnt lgkmcnt(0)\n"
    "s_sub_u32 s64, s66, s68\n"
    "s_cmp_lt_i32 s64, 0\n"
    "s_cbranch_scc1 PC47_%=\n"
    "s_min_u32 s64, s64, 8\n"
    "s_sub_u32 s66, s66, s64\n"
    "s_add_u32 s68, s66, s69\n"
    "s_barrier\n"
    "s_mov_b32 s65, 311296\n"
    "s_mov_b32 m0, 65536\n"
    "s_nop 0\n"
    "buffer_load_dwordx4 v16, s[36:39], s65 offen offset:0 nt lds\n"
    "buffer_load_dwordx4 v17, s[36:39], s65 offen offset:1024 nt lds\n"
    "buffer_load_dwordx4 v18, s[36:39], s65 offen offset:2048 nt lds\n"
    "buffer_load_dwordx4 v19, s[36:39], s65 offen offset:3072 nt lds\n"
    "s_mov_b32 m0, 69632\n"
    "s_nop 0\n"
    "buffer_load_dwordx4 v20, s[36:39], s65 offen offset:0 nt lds\n"
    "buffer_load_dwordx4 v21, s[36:39], s65 offen offset:1024 nt lds\n"
    "buffer_load_dwordx4 v22, s[36:39], s65 offen offset:2048 nt lds\n"
    "buffer_load_dwordx4 v23, s[36:39], s65 offen offset:3072 nt lds\n"
    "s_mov_b32 m0, 73728\n"
    "s_waitcnt vmcnt(59)\n"
    "buffer_load_dwordx4 v24, s[36:39], s65 offen offset:0 nt lds\n"
    "buffer_load_dwordx4 v25, s[36:39], s65 offen offset:1024 nt lds\n"
    "buffer_load_dwordx4 v26, s[36:39], s65 offen offset:2048 nt lds\n"
    "buffer_load_dwordx4 v27, s[36:39], s65 offen offset:3072 nt lds\n"
    "s_mov_b32 m0, 77824\n"
    "s_waitcnt vmcnt(59)\n"
    "buffer_load_dwordx4 v28, s[36:39], s65 offen offset:0 nt lds\n"
    "buffer_load_dwordx4 v29, s[36:39], s65 offen offset:1024 nt lds\n"
    "buffer_load_dwordx4 v30, s[36:39], s65 offen offset:2048 nt lds\n"
    "buffer_load_dwordx4 v31, s[36:39], s65 offen offset:3072 nt lds\n"
    "s_mov_b32 s65, 512\n"
    "s_waitcnt vmcnt(59)\n"
    "buffer_load_dwordx4 a[16:19], v36, s[48:51], s65 offen offset:0 nt\n"
    "buffer_load_dwordx4 a[20:23], v37, s[48:51], s65 offen offset:1024 nt\n"
    "buffer_load_dwordx4 a[24:27], v38, s[48:51], s65 offen offset:2048 nt\n"
    "buffer_load_dwordx4 a[28:31], v39, s[48:51], s65 offen offset:3072 nt\n"
    "s_waitcnt vmcnt(56)\n"
    "s_barrier\n"
    "s_mov_b32 s65, 294912\n"
    "s_mov_b32 m0, 81920\n"
    "s_nop 0\n"
    "buffer_load_dwordx4 v16, s[40:43], s65 offen offset:0 nt lds\n"
    "buffer_load_dwordx4 v17, s[40:43], s65 offen offset:1024 nt lds\n"
    "buffer_load_dwordx4 v18, s[40:43], s65 offen offset:2048 nt lds\n"
    "buffer_load_dwordx4 v19, s[40:43], s65 offen offset:3072 nt lds\n"
    "s_mov_b32 m0, 86016\n"
    "s_waitcnt vmcnt(59)\n"
    "buffer_load_dwordx4 v20, s[40:43], s65 offen offset:0 nt lds\n"
    "buffer_load_dwordx4 v21, s[40:43], s65 offen offset:1024 nt lds\n"
    "buffer_load_dwordx4 v22, s[40:43], s65 offen offset:2048 nt lds\n"
    "buffer_load_dwordx4 v23, s[40:43], s65 offen offset:3072 nt lds\n"
    "s_mov_b32 m0, 90112\n"
    "s_waitcnt vmcnt(59)\n"
    "buffer_load_dwordx4 v24, s[40:43], s65 offen offset:0 nt lds\n"
    "buffer_load_dwordx4 v25, s[40:43], s65 offen offset:1024 nt lds\n"
    "buffer_load_dwordx4 v26, s[40:43], s65 offen offset:2048 nt lds\n"
    "buffer_load_dwordx4 v27, s[40:43], s65 offen offset:3072 nt lds\n"
    "s_mov_b32 m0, 94208\n"
    "s_waitcnt vmcnt(59)\n"
    "buffer_load_dwordx4 v28, s[40:43], s65 offen offset:0 nt lds\n"
    "buffer_load_dwordx4 v29, s[40:43], s65 offen offset:1024 nt lds\n"
    "buffer_load_dwordx4 v30, s[40:43], s65 offen offset:2048 nt lds\n"
    "buffer_load_dwordx4 v31, s[40:43], s65 offen offset:3072 nt lds\n"
    "s_mov_b32 s65, 512\n"
    "s_waitcnt vmcnt(59)\n"
    "buffer_load_dwordx4 a[32:35], v40, s[48:51], s65 offen offset:0 nt\n"
    "buffer_load_dwordx4 a[36:39], v41, s[48:51], s65 offen offset:1024 nt\n"
    "buffer_load_dwordx4 a[40:43], v42, s[48:51], s65 offen offset:2048 nt\n"
    "buffer_load_dwordx4 a[44:47], v43, s[48:51], s65 offen offset:3072 nt\n"
    "s_waitcnt vmcnt(60)\n"
    "PC49_%=:\n"
    "s_memrealtime s[66:67]\n"
    "s_waitcnt lgkmcnt(0)\n"
    "s_sub_u32 s64, s66, s68\n"
    "s_cmp_lt_i32 s64, 0\n"
    "s_cbranch_scc1 PC49_%=\n"
    "s_min_u32 s64, s64, 8\n"
    "s_sub_u32 s66, s66, s64\n"
    "s_add_u32 s68, s66, s69\n"
    "s_barrier\n"
    "s_mov_b32 s65, 327680\n"
    "s_mov_b32 m0, 98304\n"
    "s_waitcnt vmcnt(59)\n"
    "buffer_load_dwordx4 v16, s[36:39], s65 offen offset:0 nt lds\n"
    "buffer_load_dwordx4 v17, s[36:39], s65 offen offset:1024 nt lds\n"
    "buffer_load_dwordx4 v18, s[36:39], s65 offen offset:2048 nt lds\n"
    "buffer_load_dwordx4 v19, s[36:39], s65 offen offset:3072 nt lds\n"
    "s_mov_b32 m0, 102400\n"
    "s_waitcnt vmcnt(59)\n"
    "buffer_load_dwordx4 v20, s[36:39], s65 offen offset:0 nt lds\n"
    "buffer_load_dwordx4 v21, s[36:39], s65 offen offset:1024 nt lds\n"
    "buffer_load_dwordx4 v22, s[36:39], s65 offen offset:2048 nt lds\n"
    "buffer_load_dwordx4 v23, s[36:39], s65 offen offset:3072 nt lds\n"
    "s_mov_b32 m0, 106496\n"
    "s_waitcnt vmcnt(59)\n"
    "buffer_load_dwordx4 v24, s[36:39], s65 offen offset:0 nt lds\n"
    "buffer_load_dwordx4 v25, s[36:39], s65 offen offset:1024 nt lds\n"
    "buffer_load_dwordx4 v26, s[36:39], s65 offen offset:2048 nt lds\n"
    "buffer_load_dwordx4 v27, s[36:39], s65 offen offset:3072 nt lds\n"
    "s_mov_b32 m0, 110592\n"
    "s_waitcnt vmcnt(59)\n"
    "buffer_load_dwordx4 v28, s[36:39], s65 offen offset:0 nt lds\n"
    "buffer_load_dwordx4 v29, s[36:39], s65 offen offset:1024 nt lds\n"
    "buffer_load_dwordx4 v30, s[36:39], s65 offen offset:2048 nt lds\n"
    "buffer_load_dwordx4 v31, s[36:39], s65 offen offset:3072 nt lds\n"
    "s_mov_b32 s65, 512\n"
    "s_waitcnt vmcnt(59)\n"
    "buffer_load_dwordx4 a[48:51], v44, s[48:51], s65 offen offset:0 nt\n"
    "buffer_load_dwordx4 a[52:55], v45, s[48:51], s65 offen offset:1024 nt\n"
    "buffer_load_dwordx4 a[56:59], v46, s[48:51], s65 offen offset:2048 nt\n"
    "buffer_load_dwordx4 a[60:63], v47, s[48:51], s65 offen offset:3072 nt\n"
    "s_barrier\n"
    "s_mov_b32 s65, 311296\n"
    "s_mov_b32 m0, 114688\n"
    "s_waitcnt vmcnt(59)\n"
    "buffer_load_dwordx4 v16, s[40:43], s65 offen offset:0 nt lds\n"
    "buffer_load_dwordx4 v17, s[40:43], s65 offen offset:1024 nt lds\n"
    "buffer_load_dwordx4 v18, s[40:43], s65 offen offset:2048 nt lds\n"
    "buffer_load_dwordx4 v19, s[40:43], s65 offen offset:3072 nt lds\n"
    "s_mov_b32 m0, 118784\n"
    "s_waitcnt vmcnt(59)\n"
    "buffer_load_dwordx4 v20, s[40:43], s65 offen offset:0 nt lds\n"
    "buffer_load_dwordx4 v21, s[40:43], s65 offen offset:1024 nt lds\n"
    "buffer_load_dwordx4 v22, s[40:43], s65 offen offset:2048 nt lds\n"
    "buffer_load_dwordx4 v23, s[40:43], s65 offen offset:3072 nt lds\n"
    "s_mov_b32 m0, 122880\n"
    "s_waitcnt vmcnt(59)\n"
    "buffer_load_dwordx4 v24, s[40:43], s65 offen offset:0 nt lds\n"
    "buffer_load_dwordx4 v25, s[40:43], s65 offen offset:1024 nt lds\n"
    "buffer_load_dwordx4 v26, s[40:43], s65 offen offset:2048 nt lds\n"
    "buffer_load_dwordx4 v27, s[40:43], s65 offen offset:3072 nt lds\n"
    "s_mov_b32 m0, 126976\n"
    "s_waitcnt vmcnt(59)\n"
    "buffer_load_dwordx4 v28, s[40:43], s65 offen offset:0 nt lds\n"
    "buffer_load_dwordx4 v29, s[40:43], s65 offen offset:1024 nt lds\n"
    "buffer_load_dwordx4 v30, s[40:43], s65 offen offset:2048 nt lds\n"
    "buffer_load_dwordx4 v31, s[40:43], s65 offen offset:3072 nt lds\n"
    "s_mov_b32 s65, 115200\n"
    "s_waitcnt vmcnt(59)\n"
    "buffer_load_dwordx4 a[64:67], v32, s[48:51], s65 offen offset:0 nt\n"
    "buffer_load_dwordx4 a[68:71], v33, s[48:51], s65 offen offset:1024 nt\n"
    "buffer_load_dwordx4 a[72:75], v34, s[48:51], s65 offen offset:2048 nt\n"
    "buffer_load_dwordx4 a[76:79], v35, s[48:51], s65 offen offset:3072 nt\n"
    "PC51_%=:\n"
    "s_memrealtime s[66:67]\n"
    "s_waitcnt lgkmcnt(0)\n"
    "s_sub_u32 s64, s66, s68\n"
    "s_cmp_lt_i32 s64, 0\n"
    "s_cbranch_scc1 PC51_%=\n"
    "s_min_u32 s64, s64, 8\n"
    "s_sub_u32 s66, s66, s64\n"
    "s_add_u32 s68, s66, s69\n"
    "s_barrier\n"
    "s_mov_b32 s65, 344064\n"
    "s_mov_b32 m0, 131072\n"
    "s_waitcnt vmcnt(59)\n"
    "buffer_load_dwordx4 v16, s[36:39], s65 offen offset:0 nt lds\n"
    "buffer_load_dwordx4 v17, s[36:39], s65 offen offset:1024 nt lds\n"
    "buffer_load_dwordx4 v18, s[36:39], s65 offen offset:2048 nt lds\n"
    "buffer_load_dwordx4 v19, s[36:39], s65 offen offset:3072 nt lds\n"
    "s_mov_b32 m0, 135168\n"
    "s_waitcnt vmcnt(59)\n"
    "buffer_load_dwordx4 v20, s[36:39], s65 offen offset:0 nt lds\n"
    "buffer_load_dwordx4 v21, s[36:39], s65 offen offset:1024 nt lds\n"
    "buffer_load_dwordx4 v22, s[36:39], s65 offen offset:2048 nt lds\n"
    "buffer_load_dwordx4 v23, s[36:39], s65 offen offset:3072 nt lds\n"
    "s_mov_b32 m0, 139264\n"
    "s_waitcnt vmcnt(59)\n"
    "buffer_load_dwordx4 v24, s[36:39], s65 offen offset:0 nt lds\n"
    "buffer_load_dwordx4 v25, s[36:39], s65 offen offset:1024 nt lds\n"
    "buffer_load_dwordx4 v26, s[36:39], s65 offen offset:2048 nt lds\n"
    "buffer_load_dwordx4 v27, s[36:39], s65 offen offset:3072 nt lds\n"
    "s_mov_b32 m0, 143360\n"
    "s_waitcnt vmcnt(59)\n"
    "buffer_load_dwordx4 v28, s[36:39], s65 offen offset:0 nt lds\n"
    "buffer_load_dwordx4 v29, s[36:39], s65 offen offset:1024 nt lds\n"
    "buffer_load_dwordx4 v30, s[36:39], s65 offen offset:2048 nt lds\n"
    "buffer_load_dwordx4 v31, s[36:39], s65 offen offset:3072 nt lds\n"
    "s_mov_b32 s65, 115200\n"
    "s_waitcnt vmcnt(59)\n"
    "buffer_load_dwordx4 a[80:83], v36, s[48:51], s65 offen offset:0 nt\n"
    "buffer_load_dwordx4 a[84:87], v37, s[48:51], s65 offen offset:1024 nt\n"
    "buffer_load_dwordx4 a[88:91], v38, s[48:51], s65 offen offset:2048 nt\n"
    "buffer_load_dwordx4 a[92:95], v39, s[48:51], s65 offen offset:3072 nt\n"
    "s_barrier\n"
    "s_mov_b32 s65, 327680\n"
    "s_mov_b32 m0, 147456\n"
    "s_waitcnt vmcnt(59)\n"
    "buffer_load_dwordx4 v16, s[40:43], s65 offen offset:0 nt lds\n"
    "buffer_load_dwordx4 v17, s[40:43], s65 offen offset:1024 nt lds\n"
    "buffer_load_dwordx4 v18, s[40:43], s65 offen offset:2048 nt lds\n"
    "buffer_load_dwordx4 v19, s[40:43], s65 offen offset:3072 nt lds\n"
    "s_mov_b32 m0, 151552\n"
    "s_waitcnt vmcnt(59)\n"
    "buffer_load_dwordx4 v20, s[40:43], s65 offen offset:0 nt lds\n"
    "buffer_load_dwordx4 v21, s[40:43], s65 offen offset:1024 nt lds\n"
    "buffer_load_dwordx4 v22, s[40:43], s65 offen offset:2048 nt lds\n"
    "buffer_load_dwordx4 v23, s[40:43], s65 offen offset:3072 nt lds\n"
    "s_mov_b32 m0, 155648\n"
    "s_waitcnt vmcnt(59)\n"
    "buffer_load_dwordx4 v24, s[40:43], s65 offen offset:0 nt lds\n"
    "buffer_load_dwordx4 v25, s[40:43], s65 offen offset:1024 nt lds\n"
    "buffer_load_dwordx4 v26, s[40:43], s65 offen offset:2048 nt lds\n"
    "buffer_load_dwordx4 v27, s[40:43], s65 offen offset:3072 nt lds\n"
    "s_mov_b32 m0, 159744\n"
    "s_waitcnt vmcnt(59)\n"
    "buffer_load_dwordx4 v28, s[40:43], s65 offen offset:0 nt lds\n"
    "buffer_load_dwordx4 v29, s[40:43], s65 offen offset:1024 nt lds\n"
    "buffer_load_dwordx4 v30, s[40:43], s65 offen offset:2048 nt lds\n"
    "buffer_load_dwordx4 v31, s[40:43], s65 offen offset:3072 nt lds\n"
    "s_mov_b32 s65, 115200\n"
    "s_waitcnt vmcnt(59)\n"
    "buffer_load_dwordx4 a[96:99], v40, s[48:51], s65 offen offset:0 nt\n"
    "buffer_load_dwordx4 a[100:103], v41, s[48:51], s65 offen offset:1024 nt\n"
    "buffer_load_dwordx4 a[104:107], v42, s[48:51], s65 offen offset:2048 nt\n"
    "buffer_load_dwordx4 a[108:111], v43, s[48:51], s65 offen offset:3072 nt\n"
    "PC53_%=:\n"
    "s_memrealtime s[66:67]\n"
    "s_waitcnt lgkmcnt(0)\n"
    "s_sub_u32 s64, s66, s68\n"
    "s_cmp_lt_i32 s64, 0\n"
    "s_cbranch_scc1 PC53_%=\n"
    "s_min_u32 s64, s64, 8\n"
    "s_sub_u32 s66, s66, s64\n"
    "s_add_u32 s68, s66, s69\n"
    "s_barrier\n"
    "s_mov_b32 s65, 360448\n"
    "s_mov_b32 m0, 0\n"
    "s_waitcnt vmcnt(59)\n"
    "buffer_load_dwordx4 v16, s[36:39], s65 offen offset:0 nt lds\n"
    "buffer_load_dwordx4 v17, s[36:39], s65 offen offset:1024 nt lds\n"
    "buffer_load_dwordx4 v18, s[36:39], s65 offen offset:2048 nt lds\n"
    "buffer_load_dwordx4 v19, s[36:39], s65 offen offset:3072 nt lds\n"
    "s_mov_b32 m0, 4096\n"
    "s_waitcnt vmcnt(59)\n"
    "buffer_load_dwordx4 v20, s[36:39], s65 offen offset:0 nt lds\n"
    "buffer_load_dwordx4 v21, s[36:39], s65 offen offset:1024 nt lds\n"
    "buffer_load_dwordx4 v22, s[36:39], s65 offen offset:2048 nt lds\n"
    "buffer_load_dwordx4 v23, s[36:39], s65 offen offset:3072 nt lds\n"
    "s_mov_b32 m0, 8192\n"
    "s_waitcnt vmcnt(59)\n"
    "buffer_load_dwordx4 v24, s[36:39], s65 offen offset:0 nt lds\n"
    "buffer_load_dwordx4 v25, s[36:39], s65 offen offset:1024 nt lds\n"
    "buffer_load_dwordx4 v26, s[36:39], s65 offen offset:2048 nt lds\n"
    "buffer_load_dwordx4 v27, s[36:39], s65 offen offset:3072 nt lds\n"
    "s_mov_b32 m0, 12288\n"
    "s_waitcnt vmcnt(59)\n"
    "buffer_load_dwordx4 v28, s[36:39], s65 offen offset:0 nt lds\n"
    "buffer_load_dwordx4 v29, s[36:39], s65 offen offset:1024 nt lds\n"
    "buffer_load_dwordx4 v30, s[36:39], s65 offen offset:2048 nt lds\n"
    "buffer_load_dwordx4 v31, s[36:39], s65 offen offset:3072 nt lds\n"
    "s_mov_b32 s65, 115200\n"
    "s_waitcnt vmcnt(59)\n"
    "buffer_load_dwordx4 a[112:115], v44, s[48:51], s65 offen offset:0 nt\n"
    "buffer_load_dwordx4 a[116:119], v45, s[48:51], s65 offen offset:1024 nt\n"
    "buffer_load_dwordx4 a[120:123], v46, s[48:51], s65 offen offset:2048 nt\n"
    "buffer_load_dwordx4 a[124:127], v47, s[48:51], s65 offen offset:3072 nt\n"
    "s_barrier\n"
    "s_mov_b32 s65, 344064\n"
    "s_mov_b32 m0, 16384\n"
    "s_waitcnt vmcnt(59)\n"
    "buffer_load_dwordx4 v16, s[40:43], s65 offen offset:0 nt lds\n"
    "buffer_load_dwordx4 v17, s[40:43], s65 offen offset:1024 nt lds\n"
    "buffer_load_dwordx4 v18, s[40:43], s65 offen offset:2048 nt lds\n"
    "buffer_load_dwordx4 v19, s[40:43], s65 offen offset:3072 nt lds\n"
    "s_mov_b32 m0, 20480\n"
    "s_waitcnt vmcnt(59)\n"
    "buffer_load_dwordx4 v20, s[40:43], s65 offen offset:0 nt lds\n"
    "buffer_load_dwordx4 v21, s[40:43], s65 offen offset:1024 nt lds\n"
    "buffer_load_dwordx4 v22, s[40:43], s65 offen offset:2048 nt lds\n"
    "buffer_load_dwordx4 v23, s[40:43], s65 offen offset:3072 nt lds\n"
    "s_mov_b32 m0, 24576\n"
    "s_waitcnt vmcnt(59)\n"
    "buffer_load_dwordx4 v24, s[40:43], s65 offen offset:0 nt lds\n"
    "buffer_load_dwordx4 v25, s[40:43], s65 offen offset:1024 nt lds\n"
    "buffer_load_dwordx4 v26, s[40:43], s65 offen offset:2048 nt lds\n"
    "buffer_load_dwordx4 v27, s[40:43], s65 offen offset:3072 nt lds\n"
    "s_mov_b32 m0, 28672\n"
    "s_waitcnt vmcnt(59)\n"
    "buffer_load_dwordx4 v28, s[40:43], s65 offen offset:0 nt lds\n"
    "buffer_load_dwordx4 v29, s[40:43], s65 offen offset:1024 nt lds\n"
    "buffer_load_dwordx4 v30, s[40:43], s65 offen offset:2048 nt lds\n"
    "buffer_load_dwordx4 v31, s[40:43], s65 offen offset:3072 nt lds\n"
    "s_mov_b32 s65, 640\n"
    "s_waitcnt vmcnt(59)\n"
    "buffer_load_dwordx4 a[128:131], v32, s[48:51], s65 offen offset:0 nt\n"
    "buffer_load_dwordx4 a[132:135], v33, s[48:51], s65 offen offset:1024 nt\n"
    "buffer_load_dwordx4 a[136:139], v34, s[48:51], s65 offen offset:2048 nt\n"
    "buffer_load_dwordx4 a[140:143], v35, s[48:51], s65 offen offset:3072 nt\n"
    "PC55_%=:\n"
    "s_memrealtime s[66:67]\n"
    "s_waitcnt lgkmcnt(0)\n"
    "s_sub_u32 s64, s66, s68\n"
    "s_cmp_lt_i32 s64, 0\n"
    "s_cbranch_scc1 PC55_%=\n"
    "s_min_u32 s64, s64, 8\n"
    "s_sub_u32 s66, s66, s64\n"
    "s_add_u32 s68, s66, s69\n"
    "s_barrier\n"
    "s_mov_b32 s65, 376832\n"
    "s_mov_b32 m0, 32768\n"
    "s_waitcnt vmcnt(59)\n"
    "buffer_load_dwordx4 v16, s[36:39], s65 offen offset:0 nt lds\n"
    "buffer_load_dwordx4 v17, s[36:39], s65 offen offset:1024 nt lds\n"
    "buffer_load_dwordx4 v18, s[36:39], s65 offen offset:2048 nt lds\n"
    "buffer_load_dwordx4 v19, s[36:39], s65 offen offset:3072 nt lds\n"
    "s_mov_b32 m0, 36864\n"
    "s_waitcnt vmcnt(59)\n"
    "buffer_load_dwordx4 v20, s[36:39], s65 offen offset:0 nt lds\n"
    "buffer_load_dwordx4 v21, s[36:39], s65 offen offset:1024 nt lds\n"
    "buffer_load_dwordx4 v22, s[36:39], s65 offen offset:2048 nt lds\n"
    "buffer_load_dwordx4 v23, s[36:39], s65 offen offset:3072 nt lds\n"
    "s_mov_b32 m0, 40960\n"
    "s_waitcnt vmcnt(59)\n"
    "buffer_load_dwordx4 v24, s[36:39], s65 offen offset:0 nt lds\n"
    "buffer_load_dwordx4 v25, s[36:39], s65 offen offset:1024 nt lds\n"
    "buffer_load_dwordx4 v26, s[36:39], s65 offen offset:2048 nt lds\n"
    "buffer_load_dwordx4 v27, s[36:39], s65 offen offset:3072 nt lds\n"
    "s_mov_b32 m0, 45056\n"
    "s_waitcnt vmcnt(59)\n"
    "buffer_load_dwordx4 v28, s[36:39], s65 offen offset:0 nt lds\n"
    "buffer_load_dwordx4 v29, s[36:39], s65 offen offset:1024 nt lds\n"
    "buffer_load_dwordx4 v30, s[36:39], s65 offen offset:2048 nt lds\n"
    "buffer_load_dwordx4 v31, s[36:39], s65 offen offset:3072 nt lds\n"
    "s_mov_b32 s65, 640\n"
    "s_waitcnt vmcnt(59)\n"
    "buffer_load_dwordx4 a[144:147], v36, s[48:51], s65 offen offset:0 nt\n"
    "buffer_load_dwordx4 a[148:151], v37, s[48:51], s65 offen offset:1024 nt\n"
    "buffer_load_dwordx4 a[152:155], v38, s[48:51], s65 offen offset:2048 nt\n"
    "buffer_load_dwordx4 a[156:159], v39, s[48:51], s65 offen offset:3072 nt\n"
    "s_barrier\n"
    "s_mov_b32 s65, 360448\n"
    "s_mov_b32 m0, 49152\n"
    "s_waitcnt vmcnt(59)\n"
    "buffer_load_dwordx4 v16, s[40:43], s65 offen offset:0 nt lds\n"
    "buffer_load_dwordx4 v17, s[40:43], s65 offen offset:1024 nt lds\n"
    "buffer_load_dwordx4 v18, s[40:43], s65 offen offset:2048 nt lds\n"
    "buffer_load_dwordx4 v19, s[40:43], s65 offen offset:3072 nt lds\n"
    "s_mov_b32 m0, 53248\n"
    "s_waitcnt vmcnt(59)\n"
    "buffer_load_dwordx4 v20, s[40:43], s65 offen offset:0 nt lds\n"
    "buffer_load_dwordx4 v21, s[40:43], s65 offen offset:1024 nt lds\n"
    "buffer_load_dwordx4 v22, s[40:43], s65 offen offset:2048 nt lds\n"
    "buffer_load_dwordx4 v23, s[40:43], s65 offen offset:3072 nt lds\n"
    "s_mov_b32 m0, 57344\n"
    "s_waitcnt vmcnt(59)\n"
    "buffer_load_dwordx4 v24, s[40:43], s65 offen offset:0 nt lds\n"
    "buffer_load_dwordx4 v25, s[40:43], s65 offen offset:1024 nt lds\n"
    "buffer_load_dwordx4 v26, s[40:43], s65 offen offset:2048 nt lds\n"
    "buffer_load_dwordx4 v27, s[40:43], s65 offen offset:3072 nt lds\n"
    "s_mov_b32 m0, 61440\n"
    "s_waitcnt vmcnt(59)\n"
    "buffer_load_dwordx4 v28, s[40:43], s65 offen offset:0 nt lds\n"
    "buffer_load_dwordx4 v29, s[40:43], s65 offen offset:1024 nt lds\n"
    "buffer_load_dwordx4 v30, s[40:43], s65 offen offset:2048 nt lds\n"
    "buffer_load_dwordx4 v31, s[40:43], s65 offen offset:3072 nt lds\n"
    "s_mov_b32 s65, 640\n"
    "s_waitcnt vmcnt(59)\n"
    "buffer_load_dwordx4 a[160:163], v40, s[48:51], s65 offen offset:0 nt\n"
    "buffer_load_dwordx4 a[164:167], v41, s[48:51], s65 offen offset:1024 nt\n"
    "buffer_load_dwordx4 a[168:171], v42, s[48:51], s65 offen offset:2048 nt\n"
    "buffer_load_dwordx4 a[172:175], v43, s[48:51], s65 offen offset:3072 nt\n"
    "PC57_%=:\n"
    "s_memrealtime s[66:67]\n"
    "s_waitcnt lgkmcnt(0)\n"
    "s_sub_u32 s64, s66, s68\n"
    "s_cmp_lt_i32 s64, 0\n"
    "s_cbranch_scc1 PC57_%=\n"
    "s_min_u32 s64, s64, 8\n"
    "s_sub_u32 s66, s66, s64\n"
    "s_add_u32 s68, s66, s69\n"
    "s_barrier\n"
    "s_mov_b32 s65, 393216\n"
    "s_mov_b32 m0, 65536\n"
    "s_waitcnt vmcnt(59)\n"
    "buffer_load_dwordx4 v16, s[36:39], s65 offen offset:0 nt lds\n"
    "buffer_load_dwordx4 v17, s[36:39], s65 offen offset:1024 nt lds\n"
    "buffer_load_dwordx4 v18, s[36:39], s65 offen offset:2048 nt lds\n"
    "buffer_load_dwordx4 v19, s[36:39], s65 offen offset:3072 nt lds\n"
    "s_mov_b32 m0, 69632\n"
    "s_waitcnt vmcnt(59)\n"
    "buffer_load_dwordx4 v20, s[36:39], s65 offen offset:0 nt lds\n"
    "buffer_load_dwordx4 v21, s[36:39], s65 offen offset:1024 nt lds\n"
    "buffer_load_dwordx4 v22, s[36:39], s65 offen offset:2048 nt lds\n"
    "buffer_load_dwordx4 v23, s[36:39], s65 offen offset:3072 nt lds\n"
    "s_mov_b32 m0, 73728\n"
    "s_waitcnt vmcnt(59)\n"
    "buffer_load_dwordx4 v24, s[36:39], s65 offen offset:0 nt lds\n"
    "buffer_load_dwordx4 v25, s[36:39], s65 offen offset:1024 nt lds\n"
    "buffer_load_dwordx4 v26, s[36:39], s65 offen offset:2048 nt lds\n"
    "buffer_load_dwordx4 v27, s[36:39], s65 offen offset:3072 nt lds\n"
    "s_mov_b32 m0, 77824\n"
    "s_waitcnt vmcnt(59)\n"
    "buffer_load_dwordx4 v28, s[36:39], s65 offen offset:0 nt lds\n"
    "buffer_load_dwordx4 v29, s[36:39], s65 offen offset:1024 nt lds\n"
    "buffer_load_dwordx4 v30, s[36:39], s65 offen offset:2048 nt lds\n"
    "buffer_load_dwordx4 v31, s[36:39], s65 offen offset:3072 nt lds\n"
    "s_mov_b32 s65, 640\n"
    "s_waitcnt vmcnt(59)\n"
    "buffer_load_dwordx4 a[176:179], v44, s[48:51], s65 offen offset:0 nt\n"
    "buffer_load_dwordx4 a[180:183], v45, s[48:51], s65 offen offset:1024 nt\n"
    "buffer_load_dwordx4 a[184:187], v46, s[48:51], s65 offen offset:2048 nt\n"
    "buffer_load_dwordx4 a[188:191], v47, s[48:51], s65 offen offset:3072 nt\n"
    "s_barrier\n"
    "s_mov_b32 s65, 376832\n"
    "s_mov_b32 m0, 81920\n"
    "s_waitcnt vmcnt(59)\n"
    "buffer_load_dwordx4 v16, s[40:43], s65 offen offset:0 nt lds\n"
    "buffer_load_dwordx4 v17, s[40:43], s65 offen offset:1024 nt lds\n"
    "buffer_load_dwordx4 v18, s[40:43], s65 offen offset:2048 nt lds\n"
    "buffer_load_dwordx4 v19, s[40:43], s65 offen offset:3072 nt lds\n"
    "s_mov_b32 m0, 86016\n"
    "s_waitcnt vmcnt(59)\n"
    "buffer_load_dwordx4 v20, s[40:43], s65 offen offset:0 nt lds\n"
    "buffer_load_dwordx4 v21, s[40:43], s65 offen offset:1024 nt lds\n"
    "buffer_load_dwordx4 v22, s[40:43], s65 offen offset:2048 nt lds\n"
    "buffer_load_dwordx4 v23, s[40:43], s65 offen offset:3072 nt lds\n"
    "s_mov_b32 m0, 90112\n"
    "s_waitcnt vmcnt(59)\n"
    "buffer_load_dwordx4 v24, s[40:43], s65 offen offset:0 nt lds\n"
    "buffer_load_dwordx4 v25, s[40:43], s65 offen offset:1024 nt lds\n"
    "buffer_load_dwordx4 v26, s[40:43], s65 offen offset:2048 nt lds\n"
    "buffer_load_dwordx4 v27, s[40:43], s65 offen offset:3072 nt lds\n"
    "s_mov_b32 m0, 94208\n"
    "s_waitcnt vmcnt(59)\n"
    "buffer_load_dwordx4 v28, s[40:43], s65 offen offset:0 nt lds\n"
    "buffer_load_dwordx4 v29, s[40:43], s65 offen offset:1024 nt lds\n"
    "buffer_load_dwordx4 v30, s[40:43], s65 offen offset:2048 nt lds\n"
    "buffer_load_dwordx4 v31, s[40:43], s65 offen offset:3072 nt lds\n"
    "s_mov_b32 s65, 115328\n"
    "s_waitcnt vmcnt(59)\n"
    "buffer_load_dwordx4 a[192:195], v32, s[48:51], s65 offen offset:0 nt\n"
    "buffer_load_dwordx4 a[196:199], v33, s[48:51], s65 offen offset:1024 nt\n"
    "buffer_load_dwordx4 a[200:203], v34, s[48:51], s65 offen offset:2048 nt\n"
    "buffer_load_dwordx4 a[204:207], v35, s[48:51], s65 offen offset:3072 nt\n"
    "PC59_%=:\n"
    "s_memrealtime s[66:67]\n"
    "s_waitcnt lgkmcnt(0)\n"
    "s_sub_u32 s64, s66, s68\n"
    "s_cmp_lt_i32 s64, 0\n"
    "s_cbranch_scc1 PC59_%=\n"
    "s_min_u32 s64, s64, 8\n"
    "s_sub_u32 s66, s66, s64\n"
    "s_add_u32 s68, s66, s69\n"
    "s_barrier\n"
    "s_mov_b32 s65, 409600\n"
    "s_mov_b32 m0, 98304\n"
    "s_waitcnt vmcnt(59)\n"
    "buffer_load_dwordx4 v16, s[36:39], s65 offen offset:0 nt lds\n"
    "buffer_load_dwordx4 v17, s[36:39], s65 offen offset:1024 nt lds\n"
    "buffer_load_dwordx4 v18, s[36:39], s65 offen offset:2048 nt lds\n"
    "buffer_load_dwordx4 v19, s[36:39], s65 offen offset:3072 nt lds\n"
    "s_mov_b32 m0, 102400\n"
    "s_waitcnt vmcnt(59)\n"
    "buffer_load_dwordx4 v20, s[36:39], s65 offen offset:0 nt lds\n"
    "buffer_load_dwordx4 v21, s[36:39], s65 offen offset:1024 nt lds\n"
    "buffer_load_dwordx4 v22, s[36:39], s65 offen offset:2048 nt lds\n"
    "buffer_load_dwordx4 v23, s[36:39], s65 offen offset:3072 nt lds\n"
    "s_mov_b32 m0, 106496\n"
    "s_waitcnt vmcnt(59)\n"
    "buffer_load_dwordx4 v24, s[36:39], s65 offen offset:0 nt lds\n"
    "buffer_load_dwordx4 v25, s[36:39], s65 offen offset:1024 nt lds\n"
    "buffer_load_dwordx4 v26, s[36:39], s65 offen offset:2048 nt lds\n"
    "buffer_load_dwordx4 v27, s[36:39], s65 offen offset:3072 nt lds\n"
    "s_mov_b32 m0, 110592\n"
    "s_waitcnt vmcnt(59)\n"
    "buffer_load_dwordx4 v28, s[36:39], s65 offen offset:0 nt lds\n"
    "buffer_load_dwordx4 v29, s[36:39], s65 offen offset:1024 nt lds\n"
    "buffer_load_dwordx4 v30, s[36:39], s65 offen offset:2048 nt lds\n"
    "buffer_load_dwordx4 v31, s[36:39], s65 offen offset:3072 nt lds\n"
    "s_mov_b32 s65, 115328\n"
    "s_waitcnt vmcnt(59)\n"
    "buffer_load_dwordx4 a[208:211], v36, s[48:51], s65 offen offset:0 nt\n"
    "buffer_load_dwordx4 a[212:215], v37, s[48:51], s65 offen offset:1024 nt\n"
    "buffer_load_dwordx4 a[216:219], v38, s[48:51], s65 offen offset:2048 nt\n"
    "buffer_load_dwordx4 a[220:223], v39, s[48:51], s65 offen offset:3072 nt\n"
    "s_barrier\n"
    "s_mov_b32 s65, 393216\n"
    "s_mov_b32 m0, 114688\n"
    "s_waitcnt vmcnt(59)\n"
    "buffer_load_dwordx4 v16, s[40:43], s65 offen offset:0 nt lds\n"
    "buffer_load_dwordx4 v17, s[40:43], s65 offen offset:1024 nt lds\n"
    "buffer_load_dwordx4 v18, s[40:43], s65 offen offset:2048 nt lds\n"
    "buffer_load_dwordx4 v19, s[40:43], s65 offen offset:3072 nt lds\n"
    "s_mov_b32 m0, 118784\n"
    "s_waitcnt vmcnt(59)\n"
    "buffer_load_dwordx4 v20, s[40:43], s65 offen offset:0 nt lds\n"
    "buffer_load_dwordx4 v21, s[40:43], s65 offen offset:1024 nt lds\n"
    "buffer_load_dwordx4 v22, s[40:43], s65 offen offset:2048 nt lds\n"
    "buffer_load_dwordx4 v23, s[40:43], s65 offen offset:3072 nt lds\n"
    "s_mov_b32 m0, 122880\n"
    "s_waitcnt vmcnt(59)\n"
    "buffer_load_dwordx4 v24, s[40:43], s65 offen offset:0 nt lds\n"
    "buffer_load_dwordx4 v25, s[40:43], s65 offen offset:1024 nt lds\n"
    "buffer_load_dwordx4 v26, s[40:43], s65 offen offset:2048 nt lds\n"
    "buffer_load_dwordx4 v27, s[40:43], s65 offen offset:3072 nt lds\n"
    "s_mov_b32 m0, 126976\n"
    "s_waitcnt vmcnt(59)\n"
    "buffer_load_dwordx4 v28, s[40:43], s65 offen offset:0 nt lds\n"
    "buffer_load_dwordx4 v29, s[40:43], s65 offen offset:1024 nt lds\n"
    "buffer_load_dwordx4 v30, s[40:43], s65 offen offset:2048 nt lds\n"
    "buffer_load_dwordx4 v31, s[40:43], s65 offen offset:3072 nt lds\n"
    "s_mov_b32 s65, 115328\n"
    "s_waitcnt vmcnt(59)\n"
    "buffer_load_dwordx4 a[224:227], v40, s[48:51], s65 offen offset:0 nt\n"
    "buffer_load_dwordx4 a[228:231], v41, s[48:51], s65 offen offset:1024 nt\n"
    "buffer_load_dwordx4 a[232:235], v42, s[48:51], s65 offen offset:2048 nt\n"
    "buffer_load_dwordx4 a[236:239], v43, s[48:51], s65 offen offset:3072 nt\n"
    "PC61_%=:\n"
    "s_memrealtime s[66:67]\n"
    "s_waitcnt lgkmcnt(0)\n"
    "s_sub_u32 s64, s66, s68\n"
    "s_cmp_lt_i32 s64, 0\n"
    "s_cbranch_scc1 PC61_%=\n"
    "s_min_u32 s64, s64, 8\n"
    "s_sub_u32 s66, s66, s64\n"
    "s_add_u32 s68, s66, s69\n"
    "s_barrier\n"
    "s_mov_b32 s65, 425984\n"
    "s_mov_b32 m0, 131072\n"
    "s_waitcnt vmcnt(59)\n"
    "buffer_load_dwordx4 v16, s[36:39], s65 offen offset:0 nt lds\n"
    "buffer_load_dwordx4 v17, s[36:39], s65 offen offset:1024 nt lds\n"
    "buffer_load_dwordx4 v18, s[36:39], s65 offen offset:2048 nt lds\n"
    "buffer_load_dwordx4 v19, s[36:39], s65 offen offset:3072 nt lds\n"
    "s_mov_b32 m0, 135168\n"
    "s_waitcnt vmcnt(59)\n"
    "buffer_load_dwordx4 v20, s[36:39], s65 offen offset:0 nt lds\n"
    "buffer_load_dwordx4 v21, s[36:39], s65 offen offset:1024 nt lds\n"
    "buffer_load_dwordx4 v22, s[36:39], s65 offen offset:2048 nt lds\n"
    "buffer_load_dwordx4 v23, s[36:39], s65 offen offset:3072 nt lds\n"
    "s_mov_b32 m0, 139264\n"
    "s_waitcnt vmcnt(59)\n"
    "buffer_load_dwordx4 v24, s[36:39], s65 offen offset:0 nt lds\n"
    "buffer_load_dwordx4 v25, s[36:39], s65 offen offset:1024 nt lds\n"
    "buffer_load_dwordx4 v26, s[36:39], s65 offen offset:2048 nt lds\n"
    "buffer_load_dwordx4 v27, s[36:39], s65 offen offset:3072 nt lds\n"
    "s_mov_b32 m0, 143360\n"
    "s_waitcnt vmcnt(59)\n"
    "buffer_load_dwordx4 v28, s[36:39], s65 offen offset:0 nt lds\n"
    "buffer_load_dwordx4 v29, s[36:39], s65 offen offset:1024 nt lds\n"
    "buffer_load_dwordx4 v30, s[36:39], s65 offen offset:2048 nt lds\n"
    "buffer_load_dwordx4 v31, s[36:39], s65 offen offset:3072 nt lds\n"
    "s_mov_b32 s65, 115328\n"
    "s_waitcnt vmcnt(59)\n"
    "buffer_load_dwordx4 a[240:243], v44, s[48:51], s65 offen offset:0 nt\n"
    "buffer_load_dwordx4 a[244:247], v45, s[48:51], s65 offen offset:1024 nt\n"
    "buffer_load_dwordx4 a[248:251], v46, s[48:51], s65 offen offset:2048 nt\n"
    "buffer_load_dwordx4 a[252:255], v47, s[48:51], s65 offen offset:3072 nt\n"
    "s_barrier\n"
    "s_mov_b32 s65, 409600\n"
    "s_mov_b32 m0, 147456\n"
    "s_waitcnt vmcnt(59)\n"
    "buffer_load_dwordx4 v16, s[40:43], s65 offen offset:0 nt lds\n"
    "buffer_load_dwordx4 v17, s[40:43], s65 offen offset:1024 nt lds\n"
    "buffer_load_dwordx4 v18, s[40:43], s65 offen offset:2048 nt lds\n"
    "buffer_load_dwordx4 v19, s[40:43], s65 offen offset:3072 nt lds\n"
    "s_mov_b32 m0, 151552\n"
    "s_waitcnt vmcnt(59)\n"
    "buffer_load_dwordx4 v20, s[40:43], s65 offen offset:0 nt lds\n"
    "buffer_load_dwordx4 v21, s[40:43], s65 offen offset:1024 nt lds\n"
    "buffer_load_dwordx4 v22, s[40:43], s65 offen offset:2048 nt lds\n"
    "buffer_load_dwordx4 v23, s[40:43], s65 offen offset:3072 nt lds\n"
    "s_mov_b32 m0, 155648\n"
    "s_waitcnt vmcnt(59)\n"
    "buffer_load_dwordx4 v24, s[40:43], s65 offen offset:0 nt lds\n"
    "buffer_load_dwordx4 v25, s[40:43], s65 offen offset:1024 nt lds\n"
    "buffer_load_dwordx4 v26, s[40:43], s65 offen offset:2048 nt lds\n"
    "buffer_load_dwordx4 v27, s[40:43], s65 offen offset:3072 nt lds\n"
    "s_mov_b32 m0, 159744\n"
    "s_waitcnt vmcnt(59)\n"
    "buffer_load_dwordx4 v28, s[40:43], s65 offen offset:0 nt lds\n"
    "buffer_load_dwordx4 v29, s[40:43], s65 offen offset:1024 nt lds\n"
    "buffer_load_dwordx4 v30, s[40:43], s65 offen offset:2048 nt lds\n"
    "buffer_load_dwordx4 v31, s[40:43], s65 offen offset:3072 nt lds\n"
    "s_mov_b32 s65, 768\n"
    "s_waitcnt vmcnt(59)\n"
    "buffer_load_dwordx4 v[128:131], v32, s[48:51], s65 offen offset:0 nt\n"
    "buffer_load_dwordx4 v[132:135], v33, s[48:51], s65 offen offset:1024 nt\n"
    "buffer_load_dwordx4 v[136:139], v34, s[48:51], s65 offen offset:2048 nt\n"
    "buffer_load_dwordx4 v[140:143], v35, s[48:51], s65 offen offset:3072 nt\n"
    "PC63_%=:\n"
    "s_memrealtime s[66:67]\n"
    "s_waitcnt lgkmcnt(0)\n"
    "s_sub_u32 s64, s66, s68\n"
    "s_cmp_lt_i32 s64, 0\n"
    "s_cbranch_scc1 PC63_%=\n"
    "s_min_u32 s64, s64, 8\n"
    "s_sub_u32 s66, s66, s64\n"
    "s_add_u32 s68, s66, s69\n"
    "s_barrier\n"
    "s_mov_b32 s65, 442368\n"
    "s_mov_b32 m0, 0\n"
    "s_waitcnt vmcnt(59)\n"
    "buffer_load_dwordx4 v16, s[36:39], s65 offen offset:0 nt lds\n"
    "buffer_load_dwordx4 v17, s[36:39], s65 offen offset:1024 nt lds\n"
    "buffer_load_dwordx4 v18, s[36:39], s65 offen offset:2048 nt lds\n"
    "buffer_load_dwordx4 v19, s[36:39], s65 offen offset:3072 nt lds\n"
    "s_mov_b32 m0, 4096\n"
    "s_waitcnt vmcnt(59)\n"
    "buffer_load_dwordx4 v20, s[36:39], s65 offen offset:0 nt lds\n"
    "buffer_load_dwordx4 v21, s[36:39], s65 offen offset:1024 nt lds\n"
    "buffer_load_dwordx4 v22, s[36:39], s65 offen offset:2048 nt lds\n"
    "buffer_load_dwordx4 v23, s[36:39], s65 offen offset:3072 nt lds\n"
    "s_mov_b32 m0, 8192\n"
    "s_waitcnt vmcnt(59)\n"
    "buffer_load_dwordx4 v24, s[36:39], s65 offen offset:0 nt lds\n"
    "buffer_load_dwordx4 v25, s[36:39], s65 offen offset:1024 nt lds\n"
    "buffer_load_dwordx4 v26, s[36:39], s65 offen offset:2048 nt lds\n"
    "buffer_load_dwordx4 v27, s[36:39], s65 offen offset:3072 nt lds\n"
    "s_mov_b32 m0, 12288\n"
    "s_waitcnt vmcnt(59)\n"
    "buffer_load_dwordx4 v28, s[36:39], s65 offen offset:0 nt lds\n"
    "buffer_load_dwordx4 v29, s[36:39], s65 offen offset:1024 nt lds\n"
    "buffer_load_dwordx4 v30, s[36:39], s65 offen offset:2048 nt lds\n"
    "buffer_load_dwordx4 v31, s[36:39], s65 offen offset:3072 nt lds\n"
    "s_mov_b32 s65, 768\n"
    "s_waitcnt vmcnt(59)\n"
    "buffer_load_dwordx4 v[144:147], v36, s[48:51], s65 offen offset:0 nt\n"
    "buffer_load_dwordx4 v[148:151], v37, s[48:51], s65 offen offset:1024 nt\n"
    "buffer_load_dwordx4 v[152:155], v38, s[48:51], s65 offen offset:2048 nt\n"
    "buffer_load_dwordx4 v[156:159], v39, s[48:51], s65 offen offset:3072 nt\n"
    "s_barrier\n"
    "s_mov_b32 s65, 425984\n"
    "s_mov_b32 m0, 16384\n"
    "s_waitcnt vmcnt(59)\n"
    "buffer_load_dwordx4 v16, s[40:43], s65 offen offset:0 nt lds\n"
    "buffer_load_dwordx4 v17, s[40:43], s65 offen offset:1024 nt lds\n"
    "buffer_load_dwordx4 v18, s[40:43], s65 offen offset:2048 nt lds\n"
    "buffer_load_dwordx4 v19, s[40:43], s65 offen offset:3072 nt lds\n"
    "s_mov_b32 m0, 20480\n"
    "s_waitcnt vmcnt(59)\n"
    "buffer_load_dwordx4 v20, s[40:43], s65 offen offset:0 nt lds\n"
    "buffer_load_dwordx4 v21, s[40:43], s65 offen offset:1024 nt lds\n"
    "buffer_load_dwordx4 v22, s[40:43], s65 offen offset:2048 nt lds\n"
    "buffer_load_dwordx4 v23, s[40:43], s65 offen offset:3072 nt lds\n"
    "s_mov_b32 m0, 24576\n"
    "s_waitcnt vmcnt(59)\n"
    "buffer_load_dwordx4 v24, s[40:43], s65 offen offset:0 nt lds\n"
    "buffer_load_dwordx4 v25, s[40:43], s65 offen offset:1024 nt lds\n"
    "buffer_load_dwordx4 v26, s[40:43], s65 offen offset:2048 nt lds\n"
    "buffer_load_dwordx4 v27, s[40:43], s65 offen offset:3072 nt lds\n"
    "s_mov_b32 m0, 28672\n"
    "s_waitcnt vmcnt(59)\n"
    "buffer_load_dwordx4 v28, s[40:43], s65 offen offset:0 nt lds\n"
    "buffer_load_dwordx4 v29, s[40:43], s65 offen offset:1024 nt lds\n"
    "buffer_load_dwordx4 v30, s[40:43], s65 offen offset:2048 nt lds\n"
    "buffer_load_dwordx4 v31, s[40:43], s65 offen offset:3072 nt lds\n"
    "s_mov_b32 s65, 768\n"
    "s_waitcnt vmcnt(59)\n"
    "buffer_load_dwordx4 v[160:163], v40, s[48:51], s65 offen offset:0 nt\n"
    "buffer_load_dwordx4 v[164:167], v41, s[48:51], s65 offen offset:1024 nt\n"
    "buffer_load_dwordx4 v[168:171], v42, s[48:51], s65 offen offset:2048 nt\n"
    "buffer_load_dwordx4 v[172:175], v43, s[48:51], s65 offen offset:3072 nt\n"
    "PC65_%=:\n"
    "s_memrealtime s[66:67]\n"
    "s_waitcnt lgkmcnt(0)\n"
    "s_sub_u32 s64, s66, s68\n"
    "s_cmp_lt_i32 s64, 0\n"
    "s_cbranch_scc1 PC65_%=\n"
    "s_min_u32 s64, s64, 8\n"
    "s_sub_u32 s66, s66, s64\n"
    "s_add_u32 s68, s66, s69\n"
    "s_barrier\n"
    "s_mov_b32 s65, 458752\n"
    "s_mov_b32 m0, 32768\n"
    "s_waitcnt vmcnt(59)\n"
    "buffer_load_dwordx4 v16, s[36:39], s65 offen offset:0 nt lds\n"
    "buffer_load_dwordx4 v17, s[36:39], s65 offen offset:1024 nt lds\n"
    "buffer_load_dwordx4 v18, s[36:39], s65 offen offset:2048 nt lds\n"
    "buffer_load_dwordx4 v19, s[36:39], s65 offen offset:3072 nt lds\n"
    "s_mov_b32 m0, 36864\n"
    "s_waitcnt vmcnt(59)\n"
    "buffer_load_dwordx4 v20, s[36:39], s65 offen offset:0 nt lds\n"
    "buffer_load_dwordx4 v21, s[36:39], s65 offen offset:1024 nt lds\n"
    "buffer_load_dwordx4 v22, s[36:39], s65 offen offset:2048 nt lds\n"
    "buffer_load_dwordx4 v23, s[36:39], s65 offen offset:3072 nt lds\n"
    "s_mov_b32 m0, 40960\n"
    "s_waitcnt vmcnt(59)\n"
    "buffer_load_dwordx4 v24, s[36:39], s65 offen offset:0 nt lds\n"
    "buffer_load_dwordx4 v25, s[36:39], s65 offen offset:1024 nt lds\n"
    "buffer_load_dwordx4 v26, s[36:39], s65 offen offset:2048 nt lds\n"
    "buffer_load_dwordx4 v27, s[36:39], s65 offen offset:3072 nt lds\n"
    "s_mov_b32 m0, 45056\n"
    "s_waitcnt vmcnt(59)\n"
    "buffer_load_dwordx4 v28, s[36:39], s65 offen offset:0 nt lds\n"
    "buffer_load_dwordx4 v29, s[36:39], s65 offen offset:1024 nt lds\n"
    "buffer_load_dwordx4 v30, s[36:39], s65 offen offset:2048 nt lds\n"
    "buffer_load_dwordx4 v31, s[36:39], s65 offen offset:3072 nt lds\n"
    "s_mov_b32 s65, 768\n"
    "s_waitcnt vmcnt(59)\n"
    "buffer_load_dwordx4 v[176:179], v44, s[48:51], s65 offen offset:0 nt\n"
    "buffer_load_dwordx4 v[180:183], v45, s[48:51], s65 offen offset:1024 nt\n"
    "buffer_load_dwordx4 v[184:187], v46, s[48:51], s65 offen offset:2048 nt\n"
    "buffer_load_dwordx4 v[188:191], v47, s[48:51], s65 offen offset:3072 nt\n"
    "s_barrier\n"
    "s_mov_b32 s65, 442368\n"
    "s_mov_b32 m0, 49152\n"
    "s_waitcnt vmcnt(59)\n"
    "buffer_load_dwordx4 v16, s[40:43], s65 offen offset:0 nt lds\n"
    "buffer_load_dwordx4 v17, s[40:43], s65 offen offset:1024 nt lds\n"
    "buffer_load_dwordx4 v18, s[40:43], s65 offen offset:2048 nt lds\n"
    "buffer_load_dwordx4 v19, s[40:43], s65 offen offset:3072 nt lds\n"
    "s_mov_b32 m0, 53248\n"
    "s_waitcnt vmcnt(59)\n"
    "buffer_load_dwordx4 v20, s[40:43], s65 offen offset:0 nt lds\n"
    "buffer_load_dwordx4 v21, s[40:43], s65 offen offset:1024 nt lds\n"
    "buffer_load_dwordx4 v22, s[40:43], s65 offen offset:2048 nt lds\n"
    "buffer_load_dwordx4 v23, s[40:43], s65 offen offset:3072 nt lds\n"
    "s_mov_b32 m0, 57344\n"
    "s_waitcnt vmcnt(59)\n"
    "buffer_load_dwordx4 v24, s[40:43], s65 offen offset:0 nt lds\n"
    "buffer_load_dwordx4 v25, s[40:43], s65 offen offset:1024 nt lds\n"
    "buffer_load_dwordx4 v26, s[40:43], s65 offen offset:2048 nt lds\n"
    "buffer_load_dwordx4 v27, s[40:43], s65 offen offset:3072 nt lds\n"
    "s_mov_b32 m0, 61440\n"
    "s_waitcnt vmcnt(59)\n"
    "buffer_load_dwordx4 v28, s[40:43], s65 offen offset:0 nt lds\n"
    "buffer_load_dwordx4 v29, s[40:43], s65 offen offset:1024 nt lds\n"
    "buffer_load_dwordx4 v30, s[40:43], s65 offen offset:2048 nt lds\n"
    "buffer_load_dwordx4 v31, s[40:43], s65 offen offset:3072 nt lds\n"
    "s_mov_b32 s65, 115456\n"
    "s_waitcnt vmcnt(59)\n"
    "buffer_load_dwordx4 v[192:195], v32, s[48:51], s65 offen offset:0 nt\n"
    "buffer_load_dwordx4 v[196:199], v33, s[48:51], s65 offen offset:1024 nt\n"
    "buffer_load_dwordx4 v[200:203], v34, s[48:51], s65 offen offset:2048 nt\n"
    "buffer_load_dwordx4 v[204:207], v35, s[48:51], s65 offen offset:3072 nt\n"
    "PC67_%=:\n"
    "s_memrealtime s[66:67]\n"
    "s_waitcnt lgkmcnt(0)\n"
    "s_sub_u32 s64, s66, s68\n"
    "s_cmp_lt_i32 s64, 0\n"
    "s_cbranch_scc1 PC67_%=\n"
    "s_min_u32 s64, s64, 8\n"
    "s_sub_u32 s66, s66, s64\n"
    "s_add_u32 s68, s66, s69\n"
    "s_barrier\n"
    "s_mov_b32 s65, 475136\n"
    "s_mov_b32 m0, 65536\n"
    "s_waitcnt vmcnt(59)\n"
    "buffer_load_dwordx4 v16, s[36:39], s65 offen offset:0 nt lds\n"
    "buffer_load_dwordx4 v17, s[36:39], s65 offen offset:1024 nt lds\n"
    "buffer_load_dwordx4 v18, s[36:39], s65 offen offset:2048 nt lds\n"
    "buffer_load_dwordx4 v19, s[36:39], s65 offen offset:3072 nt lds\n"
    "s_mov_b32 m0, 69632\n"
    "s_waitcnt vmcnt(59)\n"
    "buffer_load_dwordx4 v20, s[36:39], s65 offen offset:0 nt lds\n"
    "buffer_load_dwordx4 v21, s[36:39], s65 offen offset:1024 nt lds\n"
    "buffer_load_dwordx4 v22, s[36:39], s65 offen offset:2048 nt lds\n"
    "buffer_load_dwordx4 v23, s[36:39], s65 offen offset:3072 nt lds\n"
    "s_mov_b32 m0, 73728\n"
    "s_waitcnt vmcnt(59)\n"
    "buffer_load_dwordx4 v24, s[36:39], s65 offen offset:0 nt lds\n"
    "buffer_load_dwordx4 v25, s[36:39], s65 offen offset:1024 nt lds\n"
    "buffer_load_dwordx4 v26, s[36:39], s65 offen offset:2048 nt lds\n"
    "buffer_load_dwordx4 v27, s[36:39], s65 offen offset:3072 nt lds\n"
    "s_mov_b32 m0, 77824\n"
    "s_waitcnt vmcnt(59)\n"
    "buffer_load_dwordx4 v28, s[36:39], s65 offen offset:0 nt lds\n"
    "buffer_load_dwordx4 v29, s[36:39], s65 offen offset:1024 nt lds\n"
    "buffer_load_dwordx4 v30, s[36:39], s65 offen offset:2048 nt lds\n"
    "buffer_load_dwordx4 v31, s[36:39], s65 offen offset:3072 nt lds\n"
    "s_mov_b32 s65, 115456\n"
    "s_waitcnt vmcnt(59)\n"
    "buffer_load_dwordx4 v[208:211], v36, s[48:51], s65 offen offset:0 nt\n"
    "buffer_load_dwordx4 v[212:215], v37, s[48:51], s65 offen offset:1024 nt\n"
    "buffer_load_dwordx4 v[216:219], v38, s[48:51], s65 offen offset:2048 nt\n"
    "buffer_load_dwordx4 v[220:223], v39, s[48:51], s65 offen offset:3072 nt\n"
    "s_barrier\n"
    "s_mov_b32 s65, 458752\n"
    "s_mov_b32 m0, 81920\n"
    "s_waitcnt vmcnt(59)\n"
    "buffer_load_dwordx4 v16, s[40:43], s65 offen offset:0 nt lds\n"
    "buffer_load_dwordx4 v17, s[40:43], s65 offen offset:1024 nt lds\n"
    "buffer_load_dwordx4 v18, s[40:43], s65 offen offset:2048 nt lds\n"
    "buffer_load_dwordx4 v19, s[40:43], s65 offen offset:3072 nt lds\n"
    "s_mov_b32 m0, 86016\n"
    "s_waitcnt vmcnt(59)\n"
    "buffer_load_dwordx4 v20, s[40:43], s65 offen offset:0 nt lds\n"
    "buffer_load_dwordx4 v21, s[40:43], s65 offen offset:1024 nt lds\n"
    "buffer_load_dwordx4 v22, s[40:43], s65 offen offset:2048 nt lds\n"
    "buffer_load_dwordx4 v23, s[40:43], s65 offen offset:3072 nt lds\n"
    "s_mov_b32 m0, 90112\n"
    "s_waitcnt vmcnt(59)\n"
    "buffer_load_dwordx4 v24, s[40:43], s65 offen offset:0 nt lds\n"
    "buffer_load_dwordx4 v25, s[40:43], s65 offen offset:1024 nt lds\n"
    "buffer_load_dwordx4 v26, s[40:43], s65 offen offset:2048 nt lds\n"
    "buffer_load_dwordx4 v27, s[40:43], s65 offen offset:3072 nt lds\n"
    "s_mov_b32 m0, 94208\n"
    "s_waitcnt vmcnt(59)\n"
    "buffer_load_dwordx4 v28, s[40:43], s65 offen offset:0 nt lds\n"
    "buffer_load_dwordx4 v29, s[40:43], s65 offen offset:1024 nt lds\n"
    "buffer_load_dwordx4 v30, s[40:43], s65 offen offset:2048 nt lds\n"
    "buffer_load_dwordx4 v31, s[40:43], s65 offen offset:3072 nt lds\n"
    "s_mov_b32 s65, 115456\n"
    "s_waitcnt vmcnt(59)\n"
    "buffer_load_dwordx4 v[224:227], v40, s[48:51], s65 offen offset:0 nt\n"
    "buffer_load_dwordx4 v[228:231], v41, s[48:51], s65 offen offset:1024 nt\n"
    "buffer_load_dwordx4 v[232:235], v42, s[48:51], s65 offen offset:2048 nt\n"
    "buffer_load_dwordx4 v[236:239], v43, s[48:51], s65 offen offset:3072 nt\n"
    "PC69_%=:\n"
    "s_memrealtime s[66:67]\n"
    "s_waitcnt lgkmcnt(0)\n"
    "s_sub_u32 s64, s66, s68\n"
    "s_cmp_lt_i32 s64, 0\n"
    "s_cbranch_scc1 PC69_%=\n"
    "s_min_u32 s64, s64, 8\n"
    "s_sub_u32 s66, s66, s64\n"
    "s_add_u32 s68, s66, s69\n"
    "s_barrier\n"
    "s_mov_b32 s65, 491520\n"
    "s_mov_b32 m0, 98304\n"
    "s_waitcnt vmcnt(59)\n"
    "buffer_load_dwordx4 v16, s[36:39], s65 offen offset:0 nt lds\n"
    "buffer_load_dwordx4 v17, s[36:39], s65 offen offset:1024 nt lds\n"
    "buffer_load_dwordx4 v18, s[36:39], s65 offen offset:2048 nt lds\n"
    "buffer_load_dwordx4 v19, s[36:39], s65 offen offset:3072 nt lds\n"
    "s_mov_b32 m0, 102400\n"
    "s_waitcnt vmcnt(59)\n"
    "buffer_load_dwordx4 v20, s[36:39], s65 offen offset:0 nt lds\n"
    "buffer_load_dwordx4 v21, s[36:39], s65 offen offset:1024 nt lds\n"
    "buffer_load_dwordx4 v22, s[36:39], s65 offen offset:2048 nt lds\n"
    "buffer_load_dwordx4 v23, s[36:39], s65 offen offset:3072 nt lds\n"
    "s_mov_b32 m0, 106496\n"
    "s_waitcnt vmcnt(59)\n"
    "buffer_load_dwordx4 v24, s[36:39], s65 offen offset:0 nt lds\n"
    "buffer_load_dwordx4 v25, s[36:39], s65 offen offset:1024 nt lds\n"
    "buffer_load_dwordx4 v26, s[36:39], s65 offen offset:2048 nt lds\n"
    "buffer_load_dwordx4 v27, s[36:39], s65 offen offset:3072 nt lds\n"
    "s_mov_b32 m0, 110592\n"
    "s_waitcnt vmcnt(59)\n"
    "buffer_load_dwordx4 v28, s[36:39], s65 offen offset:0 nt lds\n"
    "buffer_load_dwordx4 v29, s[36:39], s65 offen offset:1024 nt lds\n"
    "buffer_load_dwordx4 v30, s[36:39], s65 offen offset:2048 nt lds\n"
    "buffer_load_dwordx4 v31, s[36:39], s65 offen offset:3072 nt lds\n"
    "s_mov_b32 s65, 115456\n"
    "s_waitcnt vmcnt(59)\n"
    "buffer_load_dwordx4 v[240:243], v44, s[48:51], s65 offen offset:0 nt\n"
    "buffer_load_dwordx4 v[244:247], v45, s[48:51], s65 offen offset:1024 nt\n"
    "buffer_load_dwordx4 v[248:251], v46, s[48:51], s65 offen offset:2048 nt\n"
    "buffer_load_dwordx4 v[252:255], v47, s[48:51], s65 offen offset:3072 nt\n"
    "s_barrier\n"
    "s_mov_b32 s65, 475136\n"
    "s_mov_b32 m0, 114688\n"
    "s_waitcnt vmcnt(59)\n"
    "buffer_load_dwordx4 v16, s[40:43], s65 offen offset:0 nt lds\n"
    "buffer_load_dwordx4 v17, s[40:43], s65 offen offset:1024 nt lds\n"
    "buffer_load_dwordx4 v18, s[40:43], s65 offen offset:2048 nt lds\n"
    "buffer_load_dwordx4 v19, s[40:43], s65 offen offset:3072 nt lds\n"
    "s_mov_b32 m0, 118784\n"
    "s_waitcnt vmcnt(59)\n"
    "buffer_load_dwordx4 v20, s[40:43], s65 offen offset:0 nt lds\n"
    "buffer_load_dwordx4 v21, s[40:43], s65 offen offset:1024 nt lds\n"
    "buffer_load_dwordx4 v22, s[40:43], s65 offen offset:2048 nt lds\n"
    "buffer_load_dwordx4 v23, s[40:43], s65 offen offset:3072 nt lds\n"
    "s_mov_b32 m0, 122880\n"
    "s_waitcnt vmcnt(59)\n"
    "buffer_load_dwordx4 v24, s[40:43], s65 offen offset:0 nt lds\n"
    "buffer_load_dwordx4 v25, s[40:43], s65 offen offset:1024 nt lds\n"
    "buffer_load_dwordx4 v26, s[40:43], s65 offen offset:2048 nt lds\n"
    "buffer_load_dwordx4 v27, s[40:43], s65 offen offset:3072 nt lds\n"
    "s_mov_b32 m0, 126976\n"
    "s_waitcnt vmcnt(59)\n"
    "buffer_load_dwordx4 v28, s[40:43], s65 offen offset:0 nt lds\n"
    "buffer_load_dwordx4 v29, s[40:43], s65 offen offset:1024 nt lds\n"
    "buffer_load_dwordx4 v30, s[40:43], s65 offen offset:2048 nt lds\n"
    "buffer_load_dwordx4 v31, s[40:43], s65 offen offset:3072 nt lds\n"
    "s_waitcnt vmcnt(60)\n"
    "PC71_%=:\n"
    "s_memrealtime s[66:67]\n"
    "s_waitcnt lgkmcnt(0)\n"
    "s_sub_u32 s64, s66, s68\n"
    "s_cmp_lt_i32 s64, 0\n"
    "s_cbranch_scc1 PC71_%=\n"
    "s_min_u32 s64, s64, 8\n"
    "s_sub_u32 s66, s66, s64\n"
    "s_add_u32 s68, s66, s69\n"
    "s_barrier\n"
    "s_mov_b32 s65, 507904\n"
    "s_mov_b32 m0, 131072\n"
    "s_waitcnt vmcnt(59)\n"
    "buffer_load_dwordx4 v16, s[36:39], s65 offen offset:0 nt lds\n"
    "buffer_load_dwordx4 v17, s[36:39], s65 offen offset:1024 nt lds\n"
    "buffer_load_dwordx4 v18, s[36:39], s65 offen offset:2048 nt lds\n"
    "buffer_load_dwordx4 v19, s[36:39], s65 offen offset:3072 nt lds\n"
    "s_mov_b32 m0, 135168\n"
    "s_waitcnt vmcnt(59)\n"
    "buffer_load_dwordx4 v20, s[36:39], s65 offen offset:0 nt lds\n"
    "buffer_load_dwordx4 v21, s[36:39], s65 offen offset:1024 nt lds\n"
    "buffer_load_dwordx4 v22, s[36:39], s65 offen offset:2048 nt lds\n"
    "buffer_load_dwordx4 v23, s[36:39], s65 offen offset:3072 nt lds\n"
    "s_mov_b32 m0, 139264\n"
    "s_waitcnt vmcnt(59)\n"
    "buffer_load_dwordx4 v24, s[36:39], s65 offen offset:0 nt lds\n"
    "buffer_load_dwordx4 v25, s[36:39], s65 offen offset:1024 nt lds\n"
    "buffer_load_dwordx4 v26, s[36:39], s65 offen offset:2048 nt lds\n"
    "buffer_load_dwordx4 v27, s[36:39], s65 offen offset:3072 nt lds\n"
    "s_mov_b32 m0, 143360\n"
    "s_waitcnt vmcnt(59)\n"
    "buffer_load_dwordx4 v28, s[36:39], s65 offen offset:0 nt lds\n"
    "buffer_load_dwordx4 v29, s[36:39], s65 offen offset:1024 nt lds\n"
    "buffer_load_dwordx4 v30, s[36:39], s65 offen offset:2048 nt lds\n"
    "buffer_load_dwordx4 v31, s[36:39], s65 offen offset:3072 nt lds\n"
    "s_waitcnt vmcnt(56)\n"
    "s_barrier\n"
    "s_mov_b32 s65, 491520\n"
    "s_mov_b32 m0, 147456\n"
    "s_nop 0\n"
    "buffer_load_dwordx4 v16, s[40:43], s65 offen offset:0 nt lds\n"
    "buffer_load_dwordx4 v17, s[40:43], s65 offen offset:1024 nt lds\n"
    "buffer_load_dwordx4 v18, s[40:43], s65 offen offset:2048 nt lds\n"
    "buffer_load_dwordx4 v19, s[40:43], s65 offen offset:3072 nt lds\n"
    "s_mov_b32 m0, 151552\n"
    "s_waitcnt vmcnt(59)\n"
    "buffer_load_dwordx4 v20, s[40:43], s65 offen offset:0 nt lds\n"
    "buffer_load_dwordx4 v21, s[40:43], s65 offen offset:1024 nt lds\n"
    "buffer_load_dwordx4 v22, s[40:43], s65 offen offset:2048 nt lds\n"
    "buffer_load_dwordx4 v23, s[40:43], s65 offen offset:3072 nt lds\n"
    "s_mov_b32 m0, 155648\n"
    "s_waitcnt vmcnt(59)\n"
    "buffer_load_dwordx4 v24, s[40:43], s65 offen offset:0 nt lds\n"
    "buffer_load_dwordx4 v25, s[40:43], s65 offen offset:1024 nt lds\n"
    "buffer_load_dwordx4 v26, s[40:43], s65 offen offset:2048 nt lds\n"
    "buffer_load_dwordx4 v27, s[40:43], s65 offen offset:3072 nt lds\n"
    "s_mov_b32 m0, 159744\n"
    "s_waitcnt vmcnt(59)\n"
    "buffer_load_dwordx4 v28, s[40:43], s65 offen offset:0 nt lds\n"
    "buffer_load_dwordx4 v29, s[40:43], s65 offen offset:1024 nt lds\n"
    "buffer_load_dwordx4 v30, s[40:43], s65 offen offset:2048 nt lds\n"
    "buffer_load_dwordx4 v31, s[40:43], s65 offen offset:3072 nt lds\n"
    "s_waitcnt vmcnt(52)\n"
    "PC73_%=:\n"
    "s_memrealtime s[66:67]\n"
    "s_waitcnt lgkmcnt(0)\n"
    "s_sub_u32 s64, s66, s68\n"
    "s_cmp_lt_i32 s64, 0\n"
    "s_cbranch_scc1 PC73_%=\n"
    "s_min_u32 s64, s64, 8\n"
    "s_sub_u32 s66, s66, s64\n"
    "s_add_u32 s68, s66, s69\n"
    "s_barrier\n"
    "s_mov_b32 s65, 507904\n"
    "s_mov_b32 m0, 0\n"
    "s_nop 0\n"
    "buffer_load_dwordx4 v16, s[40:43], s65 offen offset:0 nt lds\n"
    "buffer_load_dwordx4 v17, s[40:43], s65 offen offset:1024 nt lds\n"
    "buffer_load_dwordx4 v18, s[40:43], s65 offen offset:2048 nt lds\n"
    "buffer_load_dwordx4 v19, s[40:43], s65 offen offset:3072 nt lds\n"
    "s_mov_b32 m0, 4096\n"
    "s_nop 0\n"
    "buffer_load_dwordx4 v20, s[40:43], s65 offen offset:0 nt lds\n"
    "buffer_load_dwordx4 v21, s[40:43], s65 offen offset:1024 nt lds\n"
    "buffer_load_dwordx4 v22, s[40:43], s65 offen offset:2048 nt lds\n"
    "buffer_load_dwordx4 v23, s[40:43], s65 offen offset:3072 nt lds\n"
    "s_mov_b32 m0, 8192\n"
    "s_waitcnt vmcnt(59)\n"
    "buffer_load_dwordx4 v24, s[40:43], s65 offen offset:0 nt lds\n"
    "buffer_load_dwordx4 v25, s[40:43], s65 offen offset:1024 nt lds\n"
    "buffer_load_dwordx4 v26, s[40:43], s65 offen offset:2048 nt lds\n"
    "buffer_load_dwordx4 v27, s[40:43], s65 offen offset:3072 nt lds\n"
    "s_mov_b32 m0, 12288\n"
    "s_waitcnt vmcnt(59)\n"
    "buffer_load_dwordx4 v28, s[40:43], s65 offen offset:0 nt lds\n"
    "buffer_load_dwordx4 v29, s[40:43], s65 offen offset:1024 nt lds\n"
    "buffer_load_dwordx4 v30, s[40:43], s65 offen offset:2048 nt lds\n"
    "buffer_load_dwordx4 v31, s[40:43], s65 offen offset:3072 nt lds\n"
    "s_waitcnt vmcnt(48)\n"
    "s_barrier\n"
    "s_mov_b32 s65, 0\n"
    "s_mov_b32 m0, 16384\n"
    "s_nop 0\n"
    "buffer_load_dwordx4 v32, s[48:51], s65 offen offset:0 nt lds\n"
    "buffer_load_dwordx4 v33, s[48:51], s65 offen offset:1024 nt lds\n"
    "buffer_load_dwordx4 v34, s[48:51], s65 offen offset:2048 nt lds\n"
    "buffer_load_dwordx4 v35, s[48:51], s65 offen offset:3072 nt lds\n"
    "s_mov_b32 m0, 20480\n"
    "s_nop 0\n"
    "buffer_load_dwordx4 v36, s[48:51], s65 offen offset:0 nt lds\n"
    "buffer_load_dwordx4 v37, s[48:51], s65 offen offset:1024 nt lds\n"
    "buffer_load_dwordx4 v38, s[48:51], s65 offen offset:2048 nt lds\n"
    "buffer_load_dwordx4 v39, s[48:51], s65 offen offset:3072 nt lds\n"
    "s_mov_b32 m0, 24576\n"
    "s_nop 0\n"
    "buffer_load_dwordx4 v40, s[48:51], s65 offen offset:0 nt lds\n"
    "buffer_load_dwordx4 v41, s[48:51], s65 offen offset:1024 nt lds\n"
    "buffer_load_dwordx4 v42, s[48:51], s65 offen offset:2048 nt lds\n"
    "buffer_load_dwordx4 v43, s[48:51], s65 offen offset:3072 nt lds\n"
    "s_mov_b32 m0, 28672\n"
    "s_waitcnt vmcnt(59)\n"
    "buffer_load_dwordx4 v44, s[48:51], s65 offen offset:0 nt lds\n"
    "buffer_load_dwordx4 v45, s[48:51], s65 offen offset:1024 nt lds\n"
    "buffer_load_dwordx4 v46, s[48:51], s65 offen offset:2048 nt lds\n"
    "buffer_load_dwordx4 v47, s[48:51], s65 offen offset:3072 nt lds\n"
    "s_waitcnt vmcnt(48)\n"
    "PC75_%=:\n"
    "s_memrealtime s[66:67]\n"
    "s_waitcnt lgkmcnt(0)\n"
    "s_sub_u32 s64, s66, s68\n"
    "s_cmp_lt_i32 s64, 0\n"
    "s_cbranch_scc1 PC75_%=\n"
    "s_min_u32 s64, s64, 8\n"
    "s_sub_u32 s66, s66, s64\n"
    "s_add_u32 s68, s66, s69\n"
    "s_barrier\n"
    "s_mov_b32 s65, 114688\n"
    "s_mov_b32 m0, 32768\n"
    "s_nop 0\n"
    "buffer_load_dwordx4 v32, s[48:51], s65 offen offset:0 nt lds\n"
    "buffer_load_dwordx4 v33, s[48:51], s65 offen offset:1024 nt lds\n"
    "buffer_load_dwordx4 v34, s[48:51], s65 offen offset:2048 nt lds\n"
    "buffer_load_dwordx4 v35, s[48:51], s65 offen offset:3072 nt lds\n"
    "s_mov_b32 m0, 36864\n"
    "s_nop 0\n"
    "buffer_load_dwordx4 v36, s[48:51], s65 offen offset:0 nt lds\n"
    "buffer_load_dwordx4 v37, s[48:51], s65 offen offset:1024 nt lds\n"
    "buffer_load_dwordx4 v38, s[48:51], s65 offen offset:2048 nt lds\n"
    "buffer_load_dwordx4 v39, s[48:51], s65 offen offset:3072 nt lds\n"
    "s_mov_b32 m0, 40960\n"
    "s_nop 0\n"
    "buffer_load_dwordx4 v40, s[48:51], s65 offen offset:0 nt lds\n"
    "buffer_load_dwordx4 v41, s[48:51], s65 offen offset:1024 nt lds\n"
    "buffer_load_dwordx4 v42, s[48:51], s65 offen offset:2048 nt lds\n"
    "buffer_load_dwordx4 v43, s[48:51], s65 offen offset:3072 nt lds\n"
    "s_mov_b32 m0, 45056\n"
    "s_waitcnt vmcnt(59)\n"
    "buffer_load_dwordx4 v44, s[48:51], s65 offen offset:0 nt lds\n"
    "buffer_load_dwordx4 v45, s[48:51], s65 offen offset:1024 nt lds\n"
    "buffer_load_dwordx4 v46, s[48:51], s65 offen offset:2048 nt lds\n"
    "buffer_load_dwordx4 v47, s[48:51], s65 offen offset:3072 nt lds\n"
    "s_mov_b32 s65, 128\n"
    "s_mov_b32 m0, 49152\n"
    "s_waitcnt vmcnt(59)\n"
    "buffer_load_dwordx4 v32, s[48:51], s65 offen offset:0 nt lds\n"
    "buffer_load_dwordx4 v33, s[48:51], s65 offen offset:1024 nt lds\n"
    "buffer_load_dwordx4 v34, s[48:51], s65 offen offset:2048 nt lds\n"
    "buffer_load_dwordx4 v35, s[48:51], s65 offen offset:3072 nt lds\n"
    "s_mov_b32 m0, 53248\n"
    "s_waitcnt vmcnt(59)\n"
    "buffer_load_dwordx4 v36, s[48:51], s65 offen offset:0 nt lds\n"
    "buffer_load_dwordx4 v37, s[48:51], s65 offen offset:1024 nt lds\n"
    "buffer_load_dwordx4 v38, s[48:51], s65 offen offset:2048 nt lds\n"
    "buffer_load_dwordx4 v39, s[48:51], s65 offen offset:3072 nt lds\n"
    "s_mov_b32 m0, 57344\n"
    "s_waitcnt vmcnt(59)\n"
    "buffer_load_dwordx4 v40, s[48:51], s65 offen offset:0 nt lds\n"
    "buffer_load_dwordx4 v41, s[48:51], s65 offen offset:1024 nt lds\n"
    "buffer_load_dwordx4 v42, s[48:51], s65 offen offset:2048 nt lds\n"
    "buffer_load_dwordx4 v43, s[48:51], s65 offen offset:3072 nt lds\n"
    "s_mov_b32 m0, 61440\n"
    "s_waitcnt vmcnt(59)\n"
    "buffer_load_dwordx4 v44, s[48:51], s65 offen offset:0 nt lds\n"
    "buffer_load_dwordx4 v45, s[48:51], s65 offen offset:1024 nt lds\n"
    "buffer_load_dwordx4 v46, s[48:51], s65 offen offset:2048 nt lds\n"
    "buffer_load_dwordx4 v47, s[48:51], s65 offen offset:3072 nt lds\n"
    "s_barrier\n"
    "s_mov_b32 s65, 114816\n"
    "s_mov_b32 m0, 65536\n"
    "s_waitcnt vmcnt(59)\n"
    "buffer_load_dwordx4 v32, s[48:51], s65 offen offset:0 nt lds\n"
    "buffer_load_dwordx4 v33, s[48:51], s65 offen offset:1024 nt lds\n"
    "buffer_load_dwordx4 v34, s[48:51], s65 offen offset:2048 nt lds\n"
    "buffer_load_dwordx4 v35, s[48:51], s65 offen offset:3072 nt lds\n"
    "s_mov_b32 m0, 69632\n"
    "s_waitcnt vmcnt(59)\n"
    "buffer_load_dwordx4 v36, s[48:51], s65 offen offset:0 nt lds\n"
    "buffer_load_dwordx4 v37, s[48:51], s65 offen offset:1024 nt lds\n"
    "buffer_load_dwordx4 v38, s[48:51], s65 offen offset:2048 nt lds\n"
    "buffer_load_dwordx4 v39, s[48:51], s65 offen offset:3072 nt lds\n"
    "s_mov_b32 m0, 73728\n"
    "s_waitcnt vmcnt(59)\n"
    "buffer_load_dwordx4 v40, s[48:51], s65 offen offset:0 nt lds\n"
    "buffer_load_dwordx4 v41, s[48:51], s65 offen offset:1024 nt lds\n"
    "buffer_load_dwordx4 v42, s[48:51], s65 offen offset:2048 nt lds\n"
    "buffer_load_dwordx4 v43, s[48:51], s65 offen offset:3072 nt lds\n"
    "s_mov_b32 m0, 77824\n"
    "s_waitcnt vmcnt(59)\n"
    "buffer_load_dwordx4 v44, s[48:51], s65 offen offset:0 nt lds\n"
    "buffer_load_dwordx4 v45, s[48:51], s65 offen offset:1024 nt lds\n"
    "buffer_load_dwordx4 v46, s[48:51], s65 offen offset:2048 nt lds\n"
    "buffer_load_dwordx4 v47, s[48:51], s65 offen offset:3072 nt lds\n"
    "s_mov_b32 s65, 256\n"
    "s_mov_b32 m0, 81920\n"
    "s_waitcnt vmcnt(59)\n"
    "buffer_load_dwordx4 v32, s[48:51], s65 offen offset:0 nt lds\n"
    "buffer_load_dwordx4 v33, s[48:51], s65 offen offset:1024 nt lds\n"
    "buffer_load_dwordx4 v34, s[48:51], s65 offen offset:2048 nt lds\n"
    "buffer_load_dwordx4 v35, s[48:51], s65 offen offset:3072 nt lds\n"
    "s_mov_b32 m0, 86016\n"
    "s_waitcnt vmcnt(59)\n"
    "buffer_load_dwordx4 v36, s[48:51], s65 offen offset:0 nt lds\n"
    "buffer_load_dwordx4 v37, s[48:51], s65 offen offset:1024 nt lds\n"
    "buffer_load_dwordx4 v38, s[48:51], s65 offen offset:2048 nt lds\n"
    "buffer_load_dwordx4 v39, s[48:51], s65 offen offset:3072 nt lds\n"
    "s_mov_b32 m0, 90112\n"
    "s_waitcnt vmcnt(59)\n"
    "buffer_load_dwordx4 v40, s[48:51], s65 offen offset:0 nt lds\n"
    "buffer_load_dwordx4 v41, s[48:51], s65 offen offset:1024 nt lds\n"
    "buffer_load_dwordx4 v42, s[48:51], s65 offen offset:2048 nt lds\n"
    "buffer_load_dwordx4 v43, s[48:51], s65 offen offset:3072 nt lds\n"
    "s_mov_b32 m0, 94208\n"
    "s_waitcnt vmcnt(59)\n"
    "buffer_load_dwordx4 v44, s[48:51], s65 offen offset:0 nt lds\n"
    "buffer_load_dwordx4 v45, s[48:51], s65 offen offset:1024 nt lds\n"
    "buffer_load_dwordx4 v46, s[48:51], s65 offen offset:2048 nt lds\n"
    "buffer_load_dwordx4 v47, s[48:51], s65 offen offset:3072 nt lds\n"
    "PC77_%=:\n"
    "s_memrealtime s[66:67]\n"
    "s_waitcnt lgkmcnt(0)\n"
    "s_sub_u32 s64, s66, s68\n"
    "s_cmp_lt_i32 s64, 0\n"
    "s_cbranch_scc1 PC77_%=\n"
    "s_min_u32 s64, s64, 8\n"
    "s_sub_u32 s66, s66, s64\n"
    "s_add_u32 s68, s66, s69\n"
    "s_barrier\n"
    "s_mov_b32 s65, 114944\n"
    "s_mov_b32 m0, 98304\n"
    "s_waitcnt vmcnt(59)\n"
    "buffer_load_dwordx4 v32, s[48:51], s65 offen offset:0 nt lds\n"
    "buffer_load_dwordx4 v33, s[48:51], s65 offen offset:1024 nt lds\n"
    "buffer_load_dwordx4 v34, s[48:51], s65 offen offset:2048 nt lds\n"
    "buffer_load_dwordx4 v35, s[48:51], s65 offen offset:3072 nt lds\n"
    "s_mov_b32 m0, 102400\n"
    "s_waitcnt vmcnt(59)\n"
    "buffer_load_dwordx4 v36, s[48:51], s65 offen offset:0 nt lds\n"
    "buffer_load_dwordx4 v37, s[48:51], s65 offen offset:1024 nt lds\n"
    "buffer_load_dwordx4 v38, s[48:51], s65 offen offset:2048 nt lds\n"
    "buffer_load_dwordx4 v39, s[48:51], s65 offen offset:3072 nt lds\n"
    "s_mov_b32 m0, 106496\n"
    "s_waitcnt vmcnt(59)\n"
    "buffer_load_dwordx4 v40, s[48:51], s65 offen offset:0 nt lds\n"
    "buffer_load_dwordx4 v41, s[48:51], s65 offen offset:1024 nt lds\n"
    "buffer_load_dwordx4 v42, s[48:51], s65 offen offset:2048 nt lds\n"
    "buffer_load_dwordx4 v43, s[48:51], s65 offen offset:3072 nt lds\n"
    "s_mov_b32 m0, 110592\n"
    "s_waitcnt vmcnt(59)\n"
    "buffer_load_dwordx4 v44, s[48:51], s65 offen offset:0 nt lds\n"
    "buffer_load_dwordx4 v45, s[48:51], s65 offen offset:1024 nt lds\n"
    "buffer_load_dwordx4 v46, s[48:51], s65 offen offset:2048 nt lds\n"
    "buffer_load_dwordx4 v47, s[48:51], s65 offen offset:3072 nt lds\n"
    "s_barrier\n"
    "s_mov_b32 s65, 384\n"
    "s_mov_b32 m0, 114688\n"
    "s_waitcnt vmcnt(59)\n"
    "buffer_load_dwordx4 v32, s[48:51], s65 offen offset:0 nt lds\n"
    "buffer_load_dwordx4 v33, s[48:51], s65 offen offset:1024 nt lds\n"
    "buffer_load_dwordx4 v34, s[48:51], s65 offen offset:2048 nt lds\n"
    "buffer_load_dwordx4 v35, s[48:51], s65 offen offset:3072 nt lds\n"
    "s_mov_b32 m0, 118784\n"
    "s_waitcnt vmcnt(59)\n"
    "buffer_load_dwordx4 v36, s[48:51], s65 offen offset:0 nt lds\n"
    "buffer_load_dwordx4 v37, s[48:51], s65 offen offset:1024 nt lds\n"
    "buffer_load_dwordx4 v38, s[48:51], s65 offen offset:2048 nt lds\n"
    "buffer_load_dwordx4 v39, s[48:51], s65 offen offset:3072 nt lds\n"
    "s_mov_b32 m0, 122880\n"
    "s_waitcnt vmcnt(59)\n"
    "buffer_load_dwordx4 v40, s[48:51], s65 offen offset:0 nt lds\n"
    "buffer_load_dwordx4 v41, s[48:51], s65 offen offset:1024 nt lds\n"
    "buffer_load_dwordx4 v42, s[48:51], s65 offen offset:2048 nt lds\n"
    "buffer_load_dwordx4 v43, s[48:51], s65 offen offset:3072 nt lds\n"
    "s_mov_b32 m0, 126976\n"
    "s_waitcnt vmcnt(59)\n"
    "buffer_load_dwordx4 v44, s[48:51], s65 offen offset:0 nt lds\n"
    "buffer_load_dwordx4 v45, s[48:51], s65 offen offset:1024 nt lds\n"
    "buffer_load_dwordx4 v46, s[48:51], s65 offen offset:2048 nt lds\n"
    "buffer_load_dwordx4 v47, s[48:51], s65 offen offset:3072 nt lds\n"
    "v_add_u32 v7, 147456, v9\n"
    "ds_write_b128 v7, a[0:3] offset:0\n"
    "ds_write_b128 v7, a[4:7] offset:1024\n"
    "ds_write_b128 v7, a[8:11] offset:2048\n"
    "ds_write_b128 v7, a[12:15] offset:3072\n"
    "ds_write_b128 v7, a[16:19] offset:4096\n"
    "ds_write_b128 v7, a[20:23] offset:5120\n"
    "ds_write_b128 v7, a[24:27] offset:6144\n"
    "ds_write_b128 v7, a[28:31] offset:7168\n"
    "ds_write_b128 v7, a[32:35] offset:8192\n"
    "ds_write_b128 v7, a[36:39] offset:9216\n"
    "ds_write_b128 v7, a[40:43] offset:10240\n"
    "ds_write_b128 v7, a[44:47] offset:11264\n"
    "ds_write_b128 v7, a[48:51] offset:12288\n"
    "ds_write_b128 v7, a[52:55] offset:13312\n"
    "s_waitcnt lgkmcnt(0)\n"
    "s_barrier\n"
    "s_mov_b32 s65, 115072\n"
    "s_mov_b32 m0, 131072\n"
    "s_waitcnt vmcnt(59)\n"
    "buffer_load_dwordx4 v32, s[48:51], s65 offen offset:0 nt lds\n"
    "buffer_load_dwordx4 v33, s[48:51], s65 offen offset:1024 nt lds\n"
    "buffer_load_dwordx4 v34, s[48:51], s65 offen offset:2048 nt lds\n"
    "buffer_load_dwordx4 v35, s[48:51], s65 offen offset:3072 nt lds\n"
    "s_mov_b32 m0, 135168\n"
    "s_waitcnt vmcnt(59)\n"
    "buffer_load_dwordx4 v36, s[48:51], s65 offen offset:0 nt lds\n"
    "buffer_load_dwordx4 v37, s[48:51], s65 offen offset:1024 nt lds\n"
    "buffer_load_dwordx4 v38, s[48:51], s65 offen offset:2048 nt lds\n"
    "buffer_load_dwordx4 v39, s[48:51], s65 offen offset:3072 nt lds\n"
    "s_mov_b32 m0, 139264\n"
    "s_waitcnt vmcnt(59)\n"
    "buffer_load_dwordx4 v40, s[48:51], s65 offen offset:0 nt lds\n"
    "buffer_load_dwordx4 v41, s[48:51], s65 offen offset:1024 nt lds\n"
    "buffer_load_dwordx4 v42, s[48:51], s65 offen offset:2048 nt lds\n"
    "buffer_load_dwordx4 v43, s[48:51], s65 offen offset:3072 nt lds\n"
    "s_mov_b32 m0, 143360\n"
    "s_waitcnt vmcnt(59)\n"
    "buffer_load_dwordx4 v44, s[48:51], s65 offen offset:0 nt lds\n"
    "buffer_load_dwordx4 v45, s[48:51], s65 offen offset:1024 nt lds\n"
    "buffer_load_dwordx4 v46, s[48:51], s65 offen offset:2048 nt lds\n"
    "buffer_load_dwordx4 v47, s[48:51], s65 offen offset:3072 nt lds\n"
    "ds_write_b128 v7, a[56:59] offset:14336\n"
    "ds_write_b128 v7, a[60:63] offset:15360\n"
    "v_add_u32 v7, 0, v9\n"
    "ds_write_b128 v7, a[64:67] offset:0\n"
    "ds_write_b128 v7, a[68:71] offset:1024\n"
    "ds_write_b128 v7, a[72:75] offset:2048\n"
    "ds_write_b128 v7, a[76:79] offset:3072\n"
    "ds_write_b128 v7, a[80:83] offset:4096\n"
    "ds_write_b128 v7, a[84:87] offset:5120\n"
    "ds_write_b128 v7, a[88:91] offset:6144\n"
    "ds_write_b128 v7, a[92:95] offset:7168\n"
    "ds_write_b128 v7, a[96:99] offset:8192\n"
    "ds_write_b128 v7, a[100:103] offset:9216\n"
    "ds_write_b128 v7, a[104:107] offset:10240\n"
    "ds_write_b128 v7, a[108:111] offset:11264\n"
    "s_waitcnt lgkmcnt(0)\n"
    "s_barrier\n"
    "ds_write_b128 v7, a[112:115] offset:12288\n"
    "ds_write_b128 v7, a[116:119] offset:13312\n"
    "ds_write_b128 v7, a[120:123] offset:14336\n"
    "ds_write_b128 v7, a[124:127] offset:15360\n"
    "v_add_u32 v7, 16384, v9\n"
    "ds_write_b128 v7, a[128:131] offset:0\n"
    "ds_write_b128 v7, a[132:135] offset:1024\n"
    "ds_write_b128 v7, a[136:139] offset:2048\n"
    "ds_write_b128 v7, a[140:143] offset:3072\n"
    "ds_write_b128 v7, a[144:147] offset:4096\n"
    "ds_write_b128 v7, a[148:151] offset:5120\n"
    "ds_write_b128 v7, a[152:155] offset:6144\n"
    "ds_write_b128 v7, a[156:159] offset:7168\n"
    "ds_write_b128 v7, a[160:163] offset:8192\n"
    "ds_write_b128 v7, a[164:167] offset:9216\n"
    "s_waitcnt lgkmcnt(0)\n"
    "s_barrier\n"
    "ds_write_b128 v7, a[168:171] offset:10240\n"
    "ds_write_b128 v7, a[172:175] offset:11264\n"
    "ds_write_b128 v7, a[176:179] offset:12288\n"
    "ds_write_b128 v7, a[180:183] offset:13312\n"
    "ds_write_b128 v7, a[184:187] offset:14336\n"
    "ds_write_b128 v7, a[188:191] offset:15360\n"
    "v_add_u32 v7, 32768, v9\n"
    "ds_write_b128 v7, a[192:195] offset:0\n"
    "ds_write_b128 v7, a[196:199] offset:1024\n"
    "ds_write_b128 v7, a[200:203] offset:2048\n"
    "ds_write_b128 v7, a[204:207] offset:3072\n"
    "ds_write_b128 v7, a[208:211] offset:4096\n"
    "ds_write_b128 v7, a[212:215] offset:5120\n"
    "ds_write_b128 v7, a[216:219] offset:6144\n"
    "ds_write_b128 v7, a[220:223] offset:7168\n"
    "s_waitcnt lgkmcnt(0)\n"
    "s_waitcnt vmcnt(48)\n"
    "s_barrier\n"
    "ds_write_b128 v7, a[224:227] offset:8192\n"
    "ds_write_b128 v7, a[228:231] offset:9216\n"
    "ds_write_b128 v7, a[232:235] offset:10240\n"
    "ds_write_b128 v7, a[236:239] offset:11264\n"
    "ds_write_b128 v7, a[240:243] offset:12288\n"
    "ds_write_b128 v7, a[244:247] offset:13312\n"
    "ds_write_b128 v7, a[248:251] offset:14336\n"
    "ds_write_b128 v7, a[252:255] offset:15360\n"
    "v_add_u32 v7, 49152, v9\n"
    "ds_write_b128 v7, v[128:131] offset:0\n"
    "ds_write_b128 v7, v[132:135] offset:1024\n"
    "ds_write_b128 v7, v[136:139] offset:2048\n"
    "ds_write_b128 v7, v[140:143] offset:3072\n"
    "ds_write_b128 v7, v[144:147] offset:4096\n"
    "ds_write_b128 v7, v[148:151] offset:5120\n"
    "s_waitcnt lgkmcnt(0)\n"
    "s_waitcnt vmcnt(32)\n"
    "s_barrier\n"
    "ds_write_b128 v7, v[152:155] offset:6144\n"
    "ds_write_b128 v7, v[156:159] offset:7168\n"
    "ds_write_b128 v7, v[160:163] offset:8192\n"
    "ds_write_b128 v7, v[164:167] offset:9216\n"
    "ds_write_b128 v7, v[168:171] offset:10240\n"
    "ds_write_b128 v7, v[172:175] offset:11264\n"
    "ds_write_b128 v7, v[176:179] offset:12288\n"
    "ds_write_b128 v7, v[180:183] offset:13312\n"
    "ds_write_b128 v7, v[184:187] offset:14336\n"
    "ds_write_b128 v7, v[188:191] offset:15360\n"
    "v_add_u32 v7, 65536, v9\n"
    "ds_write_b128 v7, v[192:195] offset:0\n"
    "ds_write_b128 v7, v[196:199] offset:1024\n"
    "ds_write_b128 v7, v[200:203] offset:2048\n"
    "ds_write_b128 v7, v[204:207] offset:3072\n"
    "s_waitcnt lgkmcnt(0)\n"
    "s_waitcnt vmcnt(16)\n"
    "s_barrier\n"
    "ds_write_b128 v7, v[208:211] offset:4096\n"
    "ds_write_b128 v7, v[212:215] offset:5120\n"
    "ds_write_b128 v7, v[216:219] offset:6144\n"
    "ds_write_b128 v7, v[220:223] offset:7168\n"
    "ds_write_b128 v7, v[224:227] offset:8192\n"
    "ds_write_b128 v7, v[228:231] offset:9216\n"
    "ds_write_b128 v7, v[232:235] offset:10240\n"
    "ds_write_b128 v7, v[236:239] offset:11264\n"
    "ds_write_b128 v7, v[240:243] offset:12288\n"
    "ds_write_b128 v7, v[244:247] offset:13312\n"
    "ds_write_b128 v7, v[248:251] offset:14336\n"
    "ds_write_b128 v7, v[252:255] offset:15360\n"
    "s_waitcnt lgkmcnt(0)\n"
    "s_waitcnt vmcnt(0)\n"
    "s_barrier\n"
    "REDUCE_%=:\n"
    "v_add_u32 v64, 1, v64\n"
    "v_lshlrev_b32 v66, 4, v64\n"
    "v_xor_b32 v67, 0x5a5a5a5a, v65\n"
    "s_mov_b32 s57, %[head]\n"
    "s_add_u32 s58, s57, 128\n"
    "s_min_u32 s58, s58, 126\n"
    "s_mov_b32 s67, 0\n"
    "s_mov_b32 s63, %[head]\n"
    "RD_POLL_%=:\n"
    "buffer_load_dwordx2 v[70:71], v68, s[28:31], 0 offen sc1\n"
    "s_mov_b32 s71, 0\n"
    "s_waitcnt vmcnt(0)\n"
    "v_sub_u32 v70, v70, v66\n"
    "v_cmp_eq_u32 vcc, v71, v67\n"
    "s_nop 1\n"
    "v_cndmask_b32 v70, 0, v70, vcc\n"
    "v_cmp_gt_u32 vcc, 8, v70\n"
    "s_nop 1\n"
    "v_cndmask_b32 v70, 0, v70, vcc\n"
    "s_cmp_ge_u32 s57, s58\n"
    "s_cbranch_scc1 RD_G0_%=\n"
    "s_mul_i32 s59, s57, 3641\n"
    "s_lshr_b32 s59, s59, 16\n"
    "s_mul_i32 s64, s59, 18\n"
    "s_sub_u32 s60, s57, s64\n"
    "s_mul_i32 s68, s60, 43\n"
    "s_lshr_b32 s68, s68, 8\n"
    "s_lshl_b32 s68, s68, 4\n"
    "s_cmp_ge_u32 s59, 7\n"
    "s_cselect_b32 s64, 6, s59\n"
    "v_cmp_lt_u32 vcc, s64, v70\n"
    "s_nop 3\n"
    "s_lshr_b64 s[64:65], vcc, s68\n"
    "s_and_b32 s64, s64, 0xffff\n"
    "s_cmp_eq_u32 s64, 0xffff\n"
    "s_cbranch_scc0 RD_G0_%=\n"
    "s_lshr_b32 s64, s60, 1\n"
    "s_mul_i32 s64, s64, 28672\n"
    "s_and_b32 s65, s60, 1\n"
    "s_lshl_b32 s65, s65, 6\n"
    "s_add_u32 s64, s64, s65\n"
    "s_lshl_b32 s65, s59, 7\n"
    "s_add_u32 s36, s64, s65\n"
    "s_lshl_b32 s66, s57, 10\n"
    "buffer_load_dwordx4 a[0:3], v72, s[52:55], s66 offen sc1 nt\n"
    "s_add_u32 s66, s66, 129024\n"
    "buffer_load_dwordx4 a[4:7], v72, s[52:55], s66 offen sc1 nt\n"
    "s_add_u32 s66, s66, 129024\n"
    "buffer_load_dwordx4 a[8:11], v72, s[52:55], s66 offen sc1 nt\n"
    "s_add_u32 s66, s66, 129024\n"
    "buffer_load_dwordx4 a[12:15], v72, s[52:55], s66 offen sc1 nt\n"
    "s_add_u32 s66, s66, 129024\n"
    "buffer_load_dwordx4 a[16:19], v72, s[52:55], s66 offen sc1 nt\n"
    "s_add_u32 s66, s66, 129024\n"
    "buffer_load_dwordx4 a[20:23], v72, s[52:55], s66 offen sc1 nt\n"
    "s_add_u32 s66, s66, 129024\n"
    "buffer_load_dwordx4 a[24:27], v72, s[52:55], s66 offen sc1 nt\n"
    "s_add_u32 s66, s66, 129024\n"
    "buffer_load_dwordx4 a[28:31], v72, s[52:55], s66 offen sc1 nt\n"
    "s_add_u32 s66, s66, 129024\n"
    "buffer_load_dwordx4 a[32:35], v72, s[52:55], s66 offen sc1 nt\n"
    "s_add_u32 s66, s66, 129024\n"
    "buffer_load_dwordx4 a[36:39], v72, s[52:55], s66 offen sc1 nt\n"
    "s_add_u32 s66, s66, 129024\n"
    "buffer_load_dwordx4 a[40:43], v72, s[52:55], s66 offen sc1 nt\n"
    "s_add_u32 s66, s66, 129024\n"
    "buffer_load_dwordx4 a[44:47], v72, s[52:55], s66 offen sc1 nt\n"
    "s_add_u32 s66, s66, 129024\n"
    "buffer_load_dwordx4 a[48:51], v72, s[52:55], s66 offen sc1 nt\n"
    "s_add_u32 s66, s66, 129024\n"
    "buffer_load_dwordx4 a[52:55], v72, s[52:55], s66 offen sc1 nt\n"
    "s_add_u32 s66, s66, 129024\n"
    "buffer_load_dwordx4 a[56:59], v72, s[52:55], s66 offen sc1 nt\n"
    "s_add_u32 s66, s66, 129024\n"
    "buffer_load_dwordx4 a[60:63], v72, s[52:55], s66 offen sc1 nt\n"
    "s_add_u32 s57, s57, 16\n"
    "s_mov_b32 s70, 1\n"
    "s_cmp_ge_u32 s57, s58\n"
    "s_cbranch_scc1 RD_G1_%=\n"
    "s_mul_i32 s59, s57, 3641\n"
    "s_lshr_b32 s59, s59, 16\n"
    "s_mul_i32 s64, s59, 18\n"
    "s_sub_u32 s60, s57, s64\n"
    "s_mul_i32 s68, s60, 43\n"
    "s_lshr_b32 s68, s68, 8\n"
    "s_lshl_b32 s68, s68, 4\n"
    "s_cmp_ge_u32 s59, 7\n"
    "s_cselect_b32 s64, 6, s59\n"
    "v_cmp_lt_u32 vcc, s64, v70\n"
    "s_nop 3\n"
    "s_lshr_b64 s[64:65], vcc, s68\n"
    "s_and_b32 s64, s64, 0xffff\n"
    "s_cmp_eq_u32 s64, 0xffff\n"
    "s_cbranch_scc0 RD_G1_%=\n"
    "s_lshr_b32 s64, s60, 1\n"
    "s_mul_i32 s64, s64, 28672\n"
    "s_and_b32 s65, s60, 1\n"
    "s_lshl_b32 s65, s65, 6\n"
    "s_add_u32 s64, s64, s65\n"
    "s_lshl_b32 s65, s59, 7\n"
    "s_add_u32 s37, s64, s65\n"
    "s_lshl_b32 s66, s57, 10\n"
    "buffer_load_dwordx4 a[64:67], v72, s[52:55], s66 offen sc1 nt\n"
    "s_add_u32 s66, s66, 129024\n"
    "buffer_load_dwordx4 a[68:71], v72, s[52:55], s66 offen sc1 nt\n"
    "s_add_u32 s66, s66, 129024\n"
    "buffer_load_dwordx4 a[72:75], v72, s[52:55], s66 offen sc1 nt\n"
    "s_add_u32 s66, s66, 129024\n"
    "buffer_load_dwordx4 a[76:79], v72, s[52:55], s66 offen sc1 nt\n"
    "s_add_u32 s66, s66, 129024\n"
    "buffer_load_dwordx4 a[80:83], v72, s[52:55], s66 offen sc1 nt\n"
    "s_add_u32 s66, s66, 129024\n"
    "buffer_load_dwordx4 a[84:87], v72, s[52:55], s66 offen sc1 nt\n"
    "s_add_u32 s66, s66, 129024\n"
    "buffer_load_dwordx4 a[88:91], v72, s[52:55], s66 offen sc1 nt\n"
    "s_add_u32 s66, s66, 129024\n"
    "buffer_load_dwordx4 a[92:95], v72, s[52:55], s66 offen sc1 nt\n"
    "s_add_u32 s66, s66, 129024\n"
    "buffer_load_dwordx4 a[96:99], v72, s[52:55], s66 offen sc1 nt\n"
    "s_add_u32 s66, s66, 129024\n"
    "buffer_load_dwordx4 a[100:103], v72, s[52:55], s66 offen sc1 nt\n"
    "s_add_u32 s66, s66, 129024\n"
    "buffer_load_dwordx4 a[104:107], v72, s[52:55], s66 offen sc1 nt\n"
    "s_add_u32 s66, s66, 129024\n"
    "buffer_load_dwordx4 a[108:111], v72, s[52:55], s66 offen sc1 nt\n"
    "s_add_u32 s66, s66, 129024\n"
    "buffer_load_dwordx4 a[112:115], v72, s[52:55], s66 offen sc1 nt\n"
    "s_add_u32 s66, s66, 129024\n"
    "buffer_load_dwordx4 a[116:119], v72, s[52:55], s66 offen sc1 nt\n"
    "s_add_u32 s66, s66, 129024\n"
    "buffer_load_dwordx4 a[120:123], v72, s[52:55], s66 offen sc1 nt\n"
    "s_add_u32 s66, s66, 129024\n"
    "buffer_load_dwordx4 a[124:127], v72, s[52:55], s66 offen sc1 nt\n"
    "s_add_u32 s57, s57, 16\n"
    "s_mov_b32 s70, 2\n"
    "s_waitcnt vmcnt(28)\n"
    "v_mfma_f32_16x16x32_bf16 v[84:87], v[76:79], a[0:3], 0\n"
    "v_mfma_f32_16x16x32_bf16 v[88:91], v[80:83], a[0:3], 0\n"
    "v_mfma_f32_16x16x32_bf16 v[84:87], v[76:79], a[4:7], v[84:87]\n"
    "v_mfma_f32_16x16x32_bf16 v[88:91], v[80:83], a[4:7], v[88:91]\n"
    "v_mfma_f32_16x16x32_bf16 v[84:87], v[76:79], a[8:11], v[84:87]\n"
    "v_mfma_f32_16x16x32_bf16 v[88:91], v[80:83], a[8:11], v[88:91]\n"
    "v_mfma_f32_16x16x32_bf16 v[84:87], v[76:79], a[12:15], v[84:87]\n"
    "v_mfma_f32_16x16x32_bf16 v[88:91], v[80:83], a[12:15], v[88:91]\n"
    "s_waitcnt vmcnt(24)\n"
    "v_mfma_f32_16x16x32_bf16 v[84:87], v[76:79], a[16:19], v[84:87]\n"
    "v_mfma_f32_16x16x32_bf16 v[88:91], v[80:83], a[16:19], v[88:91]\n"
    "v_mfma_f32_16x16x32_bf16 v[84:87], v[76:79], a[20:23], v[84:87]\n"
    "v_mfma_f32_16x16x32_bf16 v[88:91], v[80:83], a[20:23], v[88:91]\n"
    "v_mfma_f32_16x16x32_bf16 v[84:87], v[76:79], a[24:27], v[84:87]\n"
    "v_mfma_f32_16x16x32_bf16 v[88:91], v[80:83], a[24:27], v[88:91]\n"
    "v_mfma_f32_16x16x32_bf16 v[84:87], v[76:79], a[28:31], v[84:87]\n"
    "v_mfma_f32_16x16x32_bf16 v[88:91], v[80:83], a[28:31], v[88:91]\n"
    "s_waitcnt vmcnt(20)\n"
    "v_mfma_f32_16x16x32_bf16 v[84:87], v[76:79], a[32:35], v[84:87]\n"
    "v_mfma_f32_16x16x32_bf16 v[88:91], v[80:83], a[32:35], v[88:91]\n"
    "v_mfma_f32_16x16x32_bf16 v[84:87], v[76:79], a[36:39], v[84:87]\n"
    "v_mfma_f32_16x16x32_bf16 v[88:91], v[80:83], a[36:39], v[88:91]\n"
    "v_mfma_f32_16x16x32_bf16 v[84:87], v[76:79], a[40:43], v[84:87]\n"
    "v_mfma_f32_16x16x32_bf16 v[88:91], v[80:83], a[40:43], v[88:91]\n"
    "v_mfma_f32_16x16x32_bf16 v[84:87], v[76:79], a[44:47], v[84:87]\n"
    "v_mfma_f32_16x16x32_bf16 v[88:91], v[80:83], a[44:47], v[88:91]\n"
    "s_waitcnt vmcnt(16)\n"
    "v_mfma_f32_16x16x32_bf16 v[84:87], v[76:79], a[48:51], v[84:87]\n"
    "v_mfma_f32_16x16x32_bf16 v[88:91], v[80:83], a[48:51], v[88:91]\n"
    "v_mfma_f32_16x16x32_bf16 v[84:87], v[76:79], a[52:55], v[84:87]\n"
    "v_mfma_f32_16x16x32_bf16 v[88:91], v[80:83], a[52:55], v[88:91]\n"
    "v_mfma_f32_16x16x32_bf16 v[84:87], v[76:79], a[56:59], v[84:87]\n"
    "v_mfma_f32_16x16x32_bf16 v[88:91], v[80:83], a[56:59], v[88:91]\n"
    "v_mfma_f32_16x16x32_bf16 v[84:87], v[76:79], a[60:63], v[84:87]\n"
    "v_mfma_f32_16x16x32_bf16 v[88:91], v[80:83], a[60:63], v[88:91]\n"
    "s_nop 7\n"
    "v_cvt_pk_bf16_f32 v92, v84, v85\n"
    "v_cvt_pk_bf16_f32 v93, v86, v87\n"
    "v_cvt_pk_bf16_f32 v94, v88, v89\n"
    "v_cvt_pk_bf16_f32 v95, v90, v91\n"
    "buffer_store_dwordx4 v[92:95], v73, s[96:99], s36 offen nt\n"
    "s_waitcnt vmcnt(13)\n"
    "v_mfma_f32_16x16x32_bf16 v[84:87], v[76:79], a[64:67], 0\n"
    "v_mfma_f32_16x16x32_bf16 v[88:91], v[80:83], a[64:67], 0\n"
    "v_mfma_f32_16x16x32_bf16 v[84:87], v[76:79], a[68:71], v[84:87]\n"
    "v_mfma_f32_16x16x32_bf16 v[88:91], v[80:83], a[68:71], v[88:91]\n"
    "v_mfma_f32_16x16x32_bf16 v[84:87], v[76:79], a[72:75], v[84:87]\n"
    "v_mfma_f32_16x16x32_bf16 v[88:91], v[80:83], a[72:75], v[88:91]\n"
    "v_mfma_f32_16x16x32_bf16 v[84:87], v[76:79], a[76:79], v[84:87]\n"
    "v_mfma_f32_16x16x32_bf16 v[88:91], v[80:83], a[76:79], v[88:91]\n"
    "s_waitcnt vmcnt(9)\n"
    "v_mfma_f32_16x16x32_bf16 v[84:87], v[76:79], a[80:83], v[84:87]\n"
    "v_mfma_f32_16x16x32_bf16 v[88:91], v[80:83], a[80:83], v[88:91]\n"
    "v_mfma_f32_16x16x32_bf16 v[84:87], v[76:79], a[84:87], v[84:87]\n"
    "v_mfma_f32_16x16x32_bf16 v[88:91], v[80:83], a[84:87], v[88:91]\n"
    "v_mfma_f32_16x16x32_bf16 v[84:87], v[76:79], a[88:91], v[84:87]\n"
    "v_mfma_f32_16x16x32_bf16 v[88:91], v[80:83], a[88:91], v[88:91]\n"
    "v_mfma_f32_16x16x32_bf16 v[84:87], v[76:79], a[92:95], v[84:87]\n"
    "v_mfma_f32_16x16x32_bf16 v[88:91], v[80:83], a[92:95], v[88:91]\n"
    "s_waitcnt vmcnt(5)\n"
    "v_mfma_f32_16x16x32_bf16 v[84:87], v[76:79], a[96:99], v[84:87]\n"
    "v_mfma_f32_16x16x32_bf16 v[88:91], v[80:83], a[96:99], v[88:91]\n"
    "v_mfma_f32_16x16x32_bf16 v[84:87], v[76:79], a[100:103], v[84:87]\n"
    "v_mfma_f32_16x16x32_bf16 v[88:91], v[80:83], a[100:103], v[88:91]\n"
    "v_mfma_f32_16x16x32_bf16 v[84:87], v[76:79], a[104:107], v[84:87]\n"
    "v_mfma_f32_16x16x32_bf16 v[88:91], v[80:83], a[104:107], v[88:91]\n"
    "v_mfma_f32_16x16x32_bf16 v[84:87], v[76:79], a[108:111], v[84:87]\n"
    "v_mfma_f32_16x16x32_bf16 v[88:91], v[80:83], a[108:111], v[88:91]\n"
    "s_waitcnt vmcnt(1)\n"
    "v_mfma_f32_16x16x32_bf16 v[84:87], v[76:79], a[112:115], v[84:87]\n"
    "v_mfma_f32_16x16x32_bf16 v[88:91], v[80:83], a[112:115], v[88:91]\n"
    "v_mfma_f32_16x16x32_bf16 v[84:87], v[76:79], a[116:119], v[84:87]\n"
    "v_mfma_f32_16x16x32_bf16 v[88:91], v[80:83], a[116:119], v[88:91]\n"
    "v_mfma_f32_16x16x32_bf16 v[84:87], v[76:79], a[120:123], v[84:87]\n"
    "v_mfma_f32_16x16x32_bf16 v[88:91], v[80:83], a[120:123], v[88:91]\n"
    "v_mfma_f32_16x16x32_bf16 v[84:87], v[76:79], a[124:127], v[84:87]\n"
    "v_mfma_f32_16x16x32_bf16 v[88:91], v[80:83], a[124:127], v[88:91]\n"
    "s_nop 7\n"
    "v_cvt_pk_bf16_f32 v92, v84, v85\n"
    "v_cvt_pk_bf16_f32 v93, v86, v87\n"
    "v_cvt_pk_bf16_f32 v94, v88, v89\n"
    "v_cvt_pk_bf16_f32 v95, v90, v91\n"
    "buffer_store_dwordx4 v[92:95], v73, s[96:99], s37 offen nt\n"
    "s_branch RD_NEXT_%=\n"
    "RD_G1_%=:\n"
    "s_waitcnt vmcnt(12)\n"
    "v_mfma_f32_16x16x32_bf16 v[84:87], v[76:79], a[0:3], 0\n"
    "v_mfma_f32_16x16x32_bf16 v[88:91], v[80:83], a[0:3], 0\n"
    "v_mfma_f32_16x16x32_bf16 v[84:87], v[76:79], a[4:7], v[84:87]\n"
    "v_mfma_f32_16x16x32_bf16 v[88:91], v[80:83], a[4:7], v[88:91]\n"
    "v_mfma_f32_16x16x32_bf16 v[84:87], v[76:79], a[8:11], v[84:87]\n"
    "v_mfma_f32_16x16x32_bf16 v[88:91], v[80:83], a[8:11], v[88:91]\n"
    "v_mfma_f32_16x16x32_bf16 v[84:87], v[76:79], a[12:15], v[84:87]\n"
    "v_mfma_f32_16x16x32_bf16 v[88:91], v[80:83], a[12:15], v[88:91]\n"
    "s_waitcnt vmcnt(8)\n"
    "v_mfma_f32_16x16x32_bf16 v[84:87], v[76:79], a[16:19], v[84:87]\n"
    "v_mfma_f32_16x16x32_bf16 v[88:91], v[80:83], a[16:19], v[88:91]\n"
    "v_mfma_f32_16x16x32_bf16 v[84:87], v[76:79], a[20:23], v[84:87]\n"
    "v_mfma_f32_16x16x32_bf16 v[88:91], v[80:83], a[20:23], v[88:91]\n"
    "v_mfma_f32_16x16x32_bf16 v[84:87], v[76:79], a[24:27], v[84:87]\n"
    "v_mfma_f32_16x16x32_bf16 v[88:91], v[80:83], a[24:27], v[88:91]\n"
    "v_mfma_f32_16x16x32_bf16 v[84:87], v[76:79], a[28:31], v[84:87]\n"
    "v_mfma_f32_16x16x32_bf16 v[88:91], v[80:83], a[28:31], v[88:91]\n"
    "s_waitcnt vmcnt(4)\n"
    "v_mfma_f32_16x16x32_bf16 v[84:87], v[76:79], a[32:35], v[84:87]\n"
    "v_mfma_f32_16x16x32_bf16 v[88:91], v[80:83], a[32:35], v[88:91]\n"
    "v_mfma_f32_16x16x32_bf16 v[84:87], v[76:79], a[36:39], v[84:87]\n"
    "v_mfma_f32_16x16x32_bf16 v[88:91], v[80:83], a[36:39], v[88:91]\n"
    "v_mfma_f32_16x16x32_bf16 v[84:87], v[76:79], a[40:43], v[84:87]\n"
    "v_mfma_f32_16x16x32_bf16 v[88:91], v[80:83], a[40:43], v[88:91]\n"
    "v_mfma_f32_16x16x32_bf16 v[84:87], v[76:79], a[44:47], v[84:87]\n"
    "v_mfma_f32_16x16x32_bf16 v[88:91], v[80:83], a[44:47], v[88:91]\n"
    "s_waitcnt vmcnt(0)\n"
    "v_mfma_f32_16x16x32_bf16 v[84:87], v[76:79], a[48:51], v[84:87]\n"
    "v_mfma_f32_16x16x32_bf16 v[88:91], v[80:83], a[48:51], v[88:91]\n"
    "v_mfma_f32_16x16x32_bf16 v[84:87], v[76:79], a[52:55], v[84:87]\n"
    "v_mfma_f32_16x16x32_bf16 v[88:91], v[80:83], a[52:55], v[88:91]\n"
    "v_mfma_f32_16x16x32_bf16 v[84:87], v[76:79], a[56:59], v[84:87]\n"
    "v_mfma_f32_16x16x32_bf16 v[88:91], v[80:83], a[56:59], v[88:91]\n"
    "v_mfma_f32_16x16x32_bf16 v[84:87], v[76:79], a[60:63], v[84:87]\n"
    "v_mfma_f32_16x16x32_bf16 v[88:91], v[80:83], a[60:63], v[88:91]\n"
    "s_nop 7\n"
    "v_cvt_pk_bf16_f32 v92, v84, v85\n"
    "v_cvt_pk_bf16_f32 v93, v86, v87\n"
    "v_cvt_pk_bf16_f32 v94, v88, v89\n"
    "v_cvt_pk_bf16_f32 v95, v90, v91\n"
    "buffer_store_dwordx4 v[92:95], v73, s[96:99], s36 offen nt\n"
    "RD_NEXT_%=:\n"
    "s_cmp_eq_u32 s63, 0\n"
    "s_cbranch_scc0 RD_NOE_%=\n"
    "s_mov_b32 s63, 1\n"
    "v_lshlrev_b32 v2, 20, v1\n"
    "v_add_u32 v2, 8192, v2\n"
    "buffer_store_dwordx2 v[64:65], v2, s[28:31], 0 offen sc1\n"
    "RD_NOE_%=:\n"
    "s_cmp_lt_u32 s57, s58\n"
    "s_cbranch_scc1 RD_POLL_%=\n"
    "s_branch END_%=\n"
    "RD_G0_%=:\n"
    "s_sleep 3\n"
    "s_add_u32 s67, s67, 1\n"
    "s_cmp_lt_u32 s67, 0x40000\n"
    "s_cbranch_scc1 RD_POLL_%=\n"
    "s_branch END_%=\n"
    "END_%=:\n"
    :
    : [tid] "v"(threadIdx.x), [xp] "s"(xp), [kp] "s"(kp), [vp] "s"(vp), [wq] "s"(wq), [wo] "s"(wo), [pp] "s"(pp), [gp] "s"(gp), [fp] "s"(fp), [op] "s"(op), [slot] "s"(slot), [head] "s"(h), [rot] "s"(rot), [lds] "s"((unsigned)(size_t)lds)
    :
      "memory", "vcc", "scc", "s28", "s29", "s30", "s31", "s36", "s37", "s38", "s39", "s40", "s41", "s42", "s43", "s44", "s45", "s46", "s47", "s48", "s49", "s50", "s51", "s52",
      "s53", "s54", "s55", "s56", "s57", "s58", "s59", "s60", "s61", "s62", "s63", "s64", "s65", "s66", "s67", "s68", "s69", "s70", "s71", "s72", "s73", "s74", "s75", "s76",
      "s77", "s78", "s79", "s80", "s81", "s82", "s83", "s84", "s85", "s86", "s87", "s88", "s89", "s90", "s91", "s92", "s93", "s94", "s95", "s96", "s97", "s98", "s99", "v1",
      "v2", "v3", "v4", "v5", "v6", "v7", "v8", "v9", "v10", "v11", "v12", "v13", "v14", "v15", "v16", "v17", "v18", "v19", "v20", "v21", "v22", "v23", "v24", "v25",
      "v26", "v27", "v28", "v29", "v30", "v31", "v32", "v33", "v34", "v35", "v36", "v37", "v38", "v39", "v40", "v41", "v42", "v43", "v44", "v45", "v46", "v47", "v48", "v49",
      "v50", "v51", "v52", "v53", "v54", "v55", "v56", "v57", "v58", "v59", "v60", "v61", "v62", "v63", "v64", "v65", "v66", "v67", "v68", "v69", "v70", "v71", "v72", "v73",
      "v74", "v75", "v76", "v77", "v78", "v79", "v80", "v81", "v82", "v83", "v84", "v85", "v86", "v87", "v88", "v89", "v90", "v91", "v92", "v93", "v94", "v95", "v96", "v97",
      "v98", "v99", "v100", "v101", "v102", "v103", "v104", "v105", "v106", "v107", "v108", "v109", "v110", "v111", "v112", "v113", "v114", "v115", "v116", "v117", "v118", "v119", "v120", "v121",
      "v122", "v123", "v124", "v125", "v126", "v127", "v128", "v129", "v130", "v131", "v132", "v133", "v134", "v135", "v136", "v137", "v138", "v139", "v140", "v141", "v142", "v143", "v144", "v145",
      "v146", "v147", "v148", "v149", "v150", "v151", "v152", "v153", "v154", "v155", "v156", "v157", "v158", "v159", "v160", "v161", "v162", "v163", "v164", "v165", "v166", "v167", "v168", "v169",
      "v170", "v171", "v172", "v173", "v174", "v175", "v176", "v177", "v178", "v179", "v180", "v181", "v182", "v183", "v184", "v185", "v186", "v187", "v188", "v189", "v190", "v191", "v192", "v193",
      "v194", "v195", "v196", "v197", "v198", "v199", "v200", "v201", "v202", "v203", "v204", "v205", "v206", "v207", "v208", "v209", "v210", "v211", "v212", "v213", "v214", "v215", "v216", "v217",
      "v218", "v219", "v220", "v221", "v222", "v223", "v224", "v225", "v226", "v227", "v228", "v229", "v230", "v231", "v232", "v233", "v234", "v235", "v236", "v237", "v238", "v239", "v240", "v241",
      "v242", "v243", "v244", "v245", "v246", "v247", "v248", "v249", "v250", "v251", "v252", "v253", "v254", "v255", "a0", "a1", "a2", "a3", "a4", "a5", "a6", "a7", "a8", "a9",
      "a10", "a11", "a12", "a13", "a14", "a15", "a16", "a17", "a18", "a19", "a20", "a21", "a22", "a23", "a24", "a25", "a26", "a27", "a28", "a29", "a30", "a31", "a32", "a33",
      "a34", "a35", "a36", "a37", "a38", "a39", "a40", "a41", "a42", "a43", "a44", "a45", "a46", "a47", "a48", "a49", "a50", "a51", "a52", "a53", "a54", "a55", "a56", "a57",
      "a58", "a59", "a60", "a61", "a62", "a63", "a64", "a65", "a66", "a67", "a68", "a69", "a70", "a71", "a72", "a73", "a74", "a75", "a76", "a77", "a78", "a79", "a80", "a81",
      "a82", "a83", "a84", "a85", "a86", "a87", "a88", "a89", "a90", "a91", "a92", "a93", "a94", "a95", "a96", "a97", "a98", "a99", "a100", "a101", "a102", "a103", "a104", "a105",
      "a106", "a107", "a108", "a109", "a110", "a111", "a112", "a113", "a114", "a115", "a116", "a117", "a118", "a119", "a120", "a121", "a122", "a123", "a124", "a125", "a126", "a127", "a128", "a129",
      "a130", "a131", "a132", "a133", "a134", "a135", "a136", "a137", "a138", "a139", "a140", "a141", "a142", "a143", "a144", "a145", "a146", "a147", "a148", "a149", "a150", "a151", "a152", "a153",
      "a154", "a155", "a156", "a157", "a158", "a159", "a160", "a161", "a162", "a163", "a164", "a165", "a166", "a167", "a168", "a169", "a170", "a171", "a172", "a173", "a174", "a175", "a176", "a177",
      "a178", "a179", "a180", "a181", "a182", "a183", "a184", "a185", "a186", "a187", "a188", "a189", "a190", "a191", "a192", "a193", "a194", "a195", "a196", "a197", "a198", "a199", "a200", "a201",
      "a202", "a203", "a204", "a205", "a206", "a207", "a208", "a209", "a210", "a211", "a212", "a213", "a214", "a215", "a216", "a217", "a218", "a219", "a220", "a221", "a222", "a223", "a224", "a225",
      "a226", "a227", "a228", "a229", "a230", "a231", "a232", "a233", "a234", "a235", "a236", "a237", "a238", "a239", "a240", "a241", "a242", "a243", "a244", "a245", "a246", "a247", "a248", "a249",
      "a250", "a251", "a252", "a253", "a254", "a255"
  );
}

extern "C" void kernel_launch(void* const* d_in, const int* in_sizes, int n_in, void* d_out, int out_size, void* d_ws, size_t ws_size, hipStream_t stream) {
  const char* const* in = (const char* const*)d_in;
  char* part = (char*)d_ws;

  fused_attn_kernel<<<256, 256, 0, stream>>>(in[0], in[1], in[2], in[3], in[4], in[5], in[6], in[7], in[8], in[9], part, (char*)d_out);

}
```
